```python
import torch

def _softmax(x, axis=-1):
    return torch.softmax(x, dim=axis)

def _gelu(x):
    return torch.nn.functional.gelu(x, approximate='tanh')

def _silu(x):
    return torch.nn.functional.silu(x)

def _sigmoid(x):
    return torch.sigmoid(x)

def _layernorm(x, axis=-1, eps=1e-05):
    m = x.mean(dim=axis, keepdim=True)
    v = x.var(dim=axis, keepdim=True, unbiased=False)
    return (x - m) / torch.sqrt(v + eps)

def _scatter_add(tgt, idx, src, axis=0):
    return tgt.clone().index_add_(axis, idx, src)

def _segment_sum(src, seg, n):
    out = torch.zeros((n,) + src.shape[1:], dtype=src.dtype)
    return out.index_add_(0, seg, src)

def _pad(x, pw):
    flat = tuple((v for pair in reversed(pw) for v in pair))
    return torch.nn.functional.pad(x, flat)
kernel_name = 'adaLN_cond__sliding_block_cached__dense_activation_squared_relu__prompt'

def setup_inputs():

    _w = lambda a: (lambda t: t.to(torch.bfloat16) if t.is_floating_point() else t)(torch.as_tensor(a))

    def _A_setup():
        B, Sq, D = (1, 2048, 640)
        Hq, Hkv, Sk, Dh = (32, 32, 2048, 128)
        assert Hq % Hkv == 0, (Hq, Hkv)
        return {'x': _w((lambda *s: torch.randn(*s, dtype=torch.bfloat16) * 0.05)(B, Sq, D)), 'k_cache': _w((lambda *s: torch.randn(*s, dtype=torch.bfloat16) * 3.2)(B, Hkv, Sk, Dh)), 'v_cache': _w((lambda *s: torch.randn(*s, dtype=torch.bfloat16) * 0.4)(B, Hkv, Sk, Dh)), 'wq': _w((lambda *s: torch.randn(*s, dtype=torch.bfloat16) * 3.2)(D, Hq * Dh) * D ** (-0.5)), 'wo': _w((lambda *s: torch.randn(*s, dtype=torch.bfloat16) * 0.4)(Hq * Dh, D) * (Hq * Dh) ** (-0.5))}
    _A_in = _A_setup()

    def _B_setup():
        B, S, D, F = (1, 2048, 640, 1728)
        return {'x': _w((lambda *s: torch.randn(*s, dtype=torch.bfloat16) * 0.05)(B, S, D)), 'w1': _w((lambda *s: torch.randn(*s, dtype=torch.bfloat16) * 0.05)(D, F)), 'b1': _w((lambda *s: torch.randn(*s, dtype=torch.bfloat16) * 0.8)(F)), 'w2': _w((lambda *s: torch.randn(*s, dtype=torch.bfloat16) * 0.05)(F, D)), 'b2': _w((lambda *s: torch.randn(*s, dtype=torch.bfloat16) * 6.4)(D))}
    _B_in = _B_setup()
    return {'A_x': _A_in['x'], 'A_k_cache': _A_in['k_cache'], 'A_v_cache': _A_in['v_cache'], 'A_wq': _A_in['wq'], 'A_wo': _A_in['wo'], 'B_x': _B_in['x'], 'B_w1': _B_in['w1'], 'B_b1': _B_in['b1'], 'B_w2': _B_in['w2'], 'B_b2': _B_in['b2'], 'c': _w((lambda *s: torch.randn(*s, dtype=torch.bfloat16) * 0.1)(1, 640) * 8.0), 'w_mod': _w((lambda *s: torch.randn(*s, dtype=torch.bfloat16) * 0.1)(640, 6 * 640) * 640 ** (-0.5) * 8.0)}

def reference(A_x, A_k_cache, A_v_cache, A_wq, A_wo, B_x, B_w1, B_b1, B_w2, B_b2, c, w_mod):

    def _A_ref(x, k_cache, v_cache, wq, wo):
        B, Sq, D = x.shape
        Hq, Dh = (32, 128)
        Hkv, Sk = (k_cache.shape[1], k_cache.shape[2])
        W = min(4096, max(256, Sk // 4))
        rep = Hq // Hkv
        q = (lambda x, *d: x.permute(*d))((x @ wq).reshape(B, Sq, Hq, Dh), 0, 2, 1, 3)
        k = (lambda x, n, axis: torch.repeat_interleave(x, n, dim=axis))(k_cache[:, :, Sk - W:, :], rep, axis=1)
        v = (lambda x, n, axis: torch.repeat_interleave(x, n, dim=axis))(v_cache[:, :, Sk - W:, :], rep, axis=1)
        scores = torch.einsum('bhqd,bhkd->bhqk', q, k) * Dh ** (-0.5)
        p = _softmax(scores, axis=-1)
        o = torch.einsum('bhqk,bhkd->bhqd', p, v)
        return (lambda x, *d: x.permute(*d))(o, 0, 2, 1, 3).reshape(B, Sq, Hq * Dh) @ wo

    def _B_ref(x, w1, b1, w2, b2):
        h = torch.einsum('bsd,df->bsf', x, w1) + b1
        zero = torch.zeros_like(h)
        r = torch.where(h > zero, h, zero)
        h = r * r
        out = torch.einsum('bsf,fd->bsd', h, w2) + b2
        return out
    x = A_x
    mod = (_silu(c) @ w_mod).reshape(c.shape[0], 6, 1, -1)
    sa, ca, ga, sb, cb, gb = (mod[:, 0].clone(), mod[:, 1].clone(), mod[:, 2].clone(), mod[:, 3].clone(), mod[:, 4].clone(), mod[:, 5].clone())
    h = _layernorm(x, axis=-1) * (1 + ca) + sa
    x = x + ga * _A_ref(h, A_k_cache, A_v_cache, A_wq, A_wo)
    h = _layernorm(x, axis=-1) * (1 + cb) + sb
    x = x + gb * _B_ref(h, B_w1, B_b1, B_w2, B_b2)
    return x
```

```cpp
#include <hip/hip_runtime.h>
#include <stdint.h>
#include <utility>

typedef unsigned short u16;
typedef short bf16x8 __attribute__((ext_vector_type(8)));
typedef short s16x4 __attribute__((ext_vector_type(4)));
typedef float f32x16 __attribute__((ext_vector_type(16)));
typedef float f32x4 __attribute__((ext_vector_type(4)));
typedef float f32x2 __attribute__((ext_vector_type(2)));
typedef __bf16 bf16x2 __attribute__((ext_vector_type(2)));
typedef unsigned u32x4 __attribute__((ext_vector_type(4)));
typedef unsigned u32x2 __attribute__((ext_vector_type(2)));
#define DI __device__ __forceinline__
#define GAS __attribute__((address_space(1)))
#define LAS __attribute__((address_space(3)))
#define SBAR() __builtin_amdgcn_sched_barrier(0)
#define PIN(x) asm volatile("" : "+v"(x))

constexpr int SQ = 2048, DM = 640, NH = 32, DH = 128, SK = 2048, WIN = 512, FF = 1728;
constexpr size_t WS_MOD = 0;
constexpr size_t WS_CS1 = 8192;
constexpr size_t WS_B1P = 16384;
constexpr size_t WS_STAT = 32768;
constexpr size_t WS_H = WS_STAT + 2048 * 8 * 2 * 4;
constexpr size_t WS_O = WS_H + (size_t)SQ * DM * 2;
constexpr size_t WS_WOT = WS_O + (size_t)SQ * 4096 * 2;
constexpr size_t WS_W1T = WS_WOT + (size_t)4096 * DM * 2;
constexpr size_t WS_W2T = WS_W1T + (size_t)DM * FF * 2;
constexpr size_t WS_X1 = WS_W2T + (size_t)DM * FF * 2 + 65536;
constexpr size_t WS_HID = WS_X1 + (size_t)SQ * DM * 2;
constexpr size_t WS_END = WS_HID + (size_t)SQ * FF * 2 + 65536;

template <int... I, class F> DI void static_for_impl(std::integer_sequence<int, I...>, F&& f) { (f(std::integral_constant<int, I>{}), ...); }
template <int N, class F> DI void static_for(F&& f) { static_for_impl(std::make_integer_sequence<int, N>{}, f); }
#define LAMBDA(ic) [&](auto ic) __attribute__((always_inline))

DI float bf_lo(unsigned w) { return __uint_as_float(w << 16); }
DI float bf_hi(unsigned w) { return __uint_as_float(w & 0xffff0000u); }
DI float bf2f(u16 v) { return __uint_as_float((unsigned)v << 16); }
DI unsigned cvtpk(float lo, float hi) { f32x2 v = {lo, hi}; bf16x2 b = __builtin_convertvector(v, bf16x2); return __builtin_bit_cast(unsigned, b); }
DI u16 f2bf(float x) { return (u16)(cvtpk(x, 0.f) & 0xffffu); }
DI float rbf(float x) { return bf_lo(cvtpk(x, 0.f)); }
DI f32x16 mfma32(bf16x8 a, bf16x8 b, f32x16 c) { return __builtin_amdgcn_mfma_f32_32x32x16_bf16(a, b, c, 0, 0, 0); }
DI f32x4 mfma16(bf16x8 a, bf16x8 b, f32x4 c) { return __builtin_amdgcn_mfma_f32_16x16x32_bf16(a, b, c, 0, 0, 0); }
DI float swap_max(float m) { auto r = __builtin_amdgcn_permlane32_swap(__float_as_uint(m), __float_as_uint(m), false, false); return __builtin_fmaxf(__uint_as_float(r[0]), __uint_as_float(r[1])); }
DI float swap_add(float m) { auto r = __builtin_amdgcn_permlane32_swap(__float_as_uint(m), __float_as_uint(m), false, false); return __uint_as_float(r[0]) + __uint_as_float(r[1]); }

__global__ __launch_bounds__(512) void k_mod(const u16* __restrict__ c, const u16* __restrict__ wmod, u16* __restrict__ mod) {
  __shared__ float red[8][64];
  const int t = threadIdx.x, cc = t & 7, r0 = t >> 3, col0 = blockIdx.x * 64 + cc * 8;
  u32x4 w[10]; float s[10];
#pragma unroll
  for (int i = 0; i < 10; ++i) w[i] = *(const u32x4*)(wmod + (size_t)(r0 + 64 * i) * 3840 + col0);
#pragma unroll
  for (int i = 0; i < 10; ++i) { float x = bf2f(c[r0 + 64 * i]); s[i] = rbf(x / (1.f + __expf(-x))); }
  float a[8] = {};
#pragma unroll
  for (int i = 0; i < 10; ++i)
#pragma unroll
    for (int j = 0; j < 4; ++j) { a[2 * j] += s[i] * bf_lo(w[i][j]); a[2 * j + 1] += s[i] * bf_hi(w[i][j]); }
#pragma unroll
  for (int j = 0; j < 8; ++j) { a[j] += __shfl_xor(a[j], 8); a[j] += __shfl_xor(a[j], 16); a[j] += __shfl_xor(a[j], 32); }
  if ((t & 63) < 8) {
#pragma unroll
    for (int j = 0; j < 8; ++j) red[t >> 6][cc * 8 + j] = a[j];
  }
  __syncthreads();
  if (t < 64) { float v = 0; for (int w8 = 0; w8 < 8; ++w8) v += red[w8][t]; mod[blockIdx.x * 64 + t] = f2bf(v); }
}

__global__ __launch_bounds__(256) void k_ln1(const u16* __restrict__ x, const u16* __restrict__ mod, u16* __restrict__ h) {
  const int t = threadIdx.x, i = t & 15, row = blockIdx.x * 16 + (t >> 4);
  u32x4 v[5], sa[5], ca[5];
#pragma unroll
  for (int j = 0; j < 5; ++j) v[j] = *(const u32x4*)(x + (size_t)row * DM + (i + 16 * j) * 8);
#pragma unroll
  for (int j = 0; j < 5; ++j) { sa[j] = *(const u32x4*)(mod + (i + 16 * j) * 8); ca[j] = *(const u32x4*)(mod + 640 + (i + 16 * j) * 8); }
  float f[40]; float s = 0;
#pragma unroll
  for (int j = 0; j < 5; ++j)
#pragma unroll
    for (int e = 0; e < 4; ++e) { f[j * 8 + 2 * e] = bf_lo(v[j][e]); f[j * 8 + 2 * e + 1] = bf_hi(v[j][e]); s += f[j * 8 + 2 * e] + f[j * 8 + 2 * e + 1]; }
#pragma unroll
  for (int m = 1; m < 16; m <<= 1) s += __shfl_xor(s, m);
  const float mean = s * (1.f / DM); float q = 0;
#pragma unroll
  for (int e = 0; e < 40; ++e) { f[e] -= mean; q += f[e] * f[e]; }
#pragma unroll
  for (int m = 1; m < 16; m <<= 1) q += __shfl_xor(q, m);
  const float rstd = rsqrtf(q * (1.f / DM) + 1e-5f);
  const int qb = row >> 5, r32 = row & 31;
#pragma unroll
  for (int j = 0; j < 5; ++j) {
    u32x4 o;
#pragma unroll
    for (int e = 0; e < 4; ++e) {
      float lo = f[j * 8 + 2 * e] * rstd * rbf(1.f + bf_lo(ca[j][e])) + bf_lo(sa[j][e]);
      float hi = f[j * 8 + 2 * e + 1] * rstd * rbf(1.f + bf_hi(ca[j][e])) + bf_hi(sa[j][e]);
      o[e] = cvtpk(lo, hi);
    }
    *(u32x4*)(h + (((size_t)qb * 80 + (i + 16 * j)) * 32 + r32) * 8) = o;
  }
}

__global__ void k_retile(const u16* __restrict__ W, u16* __restrict__ out, int K, int N, const u16* __restrict__ cscale) {
  const int idx = blockIdx.x * blockDim.x + threadIdx.x;
  const int KC = K / 8; if (idx >= (N / 16) * KC * 16) return;
  const int n16 = idx & 15, c = (idx >> 4) % KC, nt = (idx >> 4) / KC;
  float v[8];
  for (int j = 0; j < 8; ++j) { v[j] = bf2f(W[(size_t)(8 * c + j) * N + nt * 16 + n16]); if (cscale) v[j] *= rbf(1.f + bf2f(cscale[8 * c + j])); }
  u32x4 o = {cvtpk(v[0], v[1]), cvtpk(v[2], v[3]), cvtpk(v[4], v[5]), cvtpk(v[6], v[7])};
  *(u32x4*)(out + (size_t)idx * 8) = o;
}
__global__ void k_w1sums(const u16* __restrict__ w1, const u16* __restrict__ b1, const u16* __restrict__ mod, float* __restrict__ cs1, float* __restrict__ b1p) {
  const int f = blockIdx.x * blockDim.x + threadIdx.x; if (f >= FF) return;
  float cs = 0, bb = 0;
  for (int k = 0; k < DM; ++k) { const float w = bf2f(w1[(size_t)k * FF + f]); cs += rbf(w * rbf(1.f + bf2f(mod[4 * 640 + k]))); bb += w * bf2f(mod[3 * 640 + k]); }
  cs1[f] = cs; b1p[f] = bb + bf2f(b1[f]);
}

constexpr int NS = 10, SLOT = 16384;
constexpr float QSCALE = 0.088388347648318440f * 1.4426950408889634f;
constexpr float THR = 40.f;
constexpr int tile_K(int t) { return t == 0 ? 10 : 9 + 2 * t; }
constexpr int tile_V(int t) { return t == 7 ? 25 : 12 + 2 * t; }
constexpr int slot_of(int idx) { return (idx % NS) * SLOT; }
constexpr int HD = 3;
constexpr int lastuse(int idx) { return idx < 10 ? idx : (idx == 10 ? 10 : (idx == 25 ? 18 : ((idx & 1) ? 10 + (idx - 9) / 2 : 11 + (idx - 12) / 2))); }
constexpr int issue_step(int idx) { return idx < NS ? -1 : lastuse(idx - NS) + 1; }
constexpr int need_bar(int idx) {
  if (idx < 10) return idx == 0 ? 0 : idx - 1;
  if (idx == 10) return 9;
  if (idx == 25) return 18;
  return (idx & 1) ? 10 + (idx - 9) / 2 - 1 : 11 + (idx - 12) / 2;
}
struct VmPlan { int wait[19]; };
constexpr VmPlan make_plan() {
  VmPlan p{}; int pos = 0; int tile_end[26] = {}; int h_end[10] = {};
  for (int i = 0; i < 10; ++i) { pos += 2; tile_end[i] = pos; if (i < HD) { pos += 4; h_end[i] = pos; } }
  for (int g = 0; g < 19; ++g) {
    int need = 0;
    for (int idx = 0; idx < 26; ++idx) if (need_bar(idx) <= g && tile_end[idx] > need && issue_step(idx) < g) need = tile_end[idx];
    if (g < 10 && h_end[g] > need) need = h_end[g];
    p.wait[g] = pos - need;
    if (g + HD < 10) { pos += 4; h_end[g + HD] = pos; }
    for (int idx = NS; idx < 26; ++idx) if (issue_step(idx) == g) { pos += 2; tile_end[idx] = pos; }
  }
  return p;
}
constexpr VmPlan PLAN = make_plan();

DI void dma16(unsigned ldsaddr, unsigned voff, __amdgpu_buffer_rsrc_t srd, unsigned soff) {
  asm volatile("s_mov_b32 m0, %0\n\ts_nop 0\n\tbuffer_load_dwordx4 %1, %2, %3 offen lds" ::"s"(ldsaddr), "v"(voff), "s"(srd), "s"(soff) : "memory");
}
template <int OFF> DI void bload16(bf16x8& dst, unsigned voff, __amdgpu_buffer_rsrc_t srd, unsigned soff) {
  asm volatile("buffer_load_dwordx4 %0, %1, %2, %3 offen offset:%4" : "=v"(dst) : "v"(voff), "s"(srd), "s"(soff), "i"(OFF) : "memory");
}
typedef LAS const char* lds_cptr;
typedef short v4i16_t __attribute__((ext_vector_type(4)));
DI s16x4 vtr(lds_cptr p) { return __builtin_bit_cast(s16x4, __builtin_amdgcn_ds_read_tr16_b64_v4i16((LAS v4i16_t*)p)); }
DI bf16x8 trfrag(lds_cptr p0, lds_cptr p1) { const s16x4 lo = vtr(p0), hi = vtr(p1); return __builtin_shufflevector(lo, hi, 0, 1, 2, 3, 4, 5, 6, 7); }
DI bf16x8 rowfrag(lds_cptr p) { return *(LAS const bf16x8*)p; }
DI float max3(float a, float b, float c) { return __builtin_fmaxf(__builtin_fmaxf(a, b), c); }

__global__ __launch_bounds__(512, 2) void k_attn(const u16* __restrict__ hbuf, const u16* __restrict__ wq, const u16* __restrict__ kc, const u16* __restrict__ vc, u16* __restrict__ obuf) {
  extern __shared__ __attribute__((aligned(16))) char lds[];
  const int tid = threadIdx.x, lane = tid & 63, r32 = lane & 31, hi = lane >> 5;
  const int wid = __builtin_amdgcn_readfirstlane(tid >> 6);
  const int xcd = blockIdx.x & 7, idx = blockIdx.x >> 3;
  const int head = (xcd & 3) * 8 + (idx & 7), qt = (xcd >> 2) * 4 + (idx >> 3);
  const unsigned lds0 = (unsigned)(uintptr_t)lds;
  const __amdgpu_buffer_rsrc_t srdW = __builtin_amdgcn_make_buffer_rsrc((void*)(wq + head * DH), 0, (DM - 1) * 8192 + 256, 0x00020000);
  const __amdgpu_buffer_rsrc_t srdK = __builtin_amdgcn_make_buffer_rsrc((void*)(kc + ((size_t)head * SK + (SK - WIN)) * DH), 0, WIN * 256, 0x00020000);
  const __amdgpu_buffer_rsrc_t srdV = __builtin_amdgcn_make_buffer_rsrc((void*)(vc + ((size_t)head * SK + (SK - WIN)) * DH), 0, WIN * 256, 0x00020000);
  const __amdgpu_buffer_rsrc_t srdH = __builtin_amdgcn_make_buffer_rsrc((void*)(hbuf + (size_t)(qt * 8 + wid) * 80 * 256), 0, 80 * 512, 0x00020000);
  const int dr = (lane >> 2) & 7, dx = lane & 3, dc = lane >> 5, dz = (2 * wid + (dr >> 2)) & 3;
  const unsigned voffW = (unsigned)((8 * wid + dr) * 8192 + 64 * dc + 16 * (dx ^ dz));
  const unsigned voffKV = (unsigned)((8 * wid + dr) * 256 + 64 * dc + 16 * (dx ^ dz));
  const unsigned ldsw = lds0 + 2048 * wid;
  auto issue_tile = [&](auto ic) __attribute__((always_inline)) {
    constexpr int i = ic.value; constexpr unsigned so = slot_of(i);
    if constexpr (i < 10) { dma16(ldsw + so, voffW, srdW, i * 64 * 8192); dma16(ldsw + so + 1024, voffW, srdW, i * 64 * 8192 + 128); }
    else {
      constexpr bool isK = (i == 10) || (i != 25 && (i & 1)); constexpr int t = i == 10 ? 0 : (i == 25 ? 7 : (isK ? (i - 9) / 2 : (i - 12) / 2));
      if constexpr (isK) { dma16(ldsw + so, voffKV, srdK, t * 16384); dma16(ldsw + so + 1024, voffKV, srdK, t * 16384 + 128); }
      else { dma16(ldsw + so, voffKV, srdV, t * 16384); dma16(ldsw + so + 1024, voffKV, srdV, t * 16384 + 128); }
    }
  };
  bf16x8 hf[10][4];
  const unsigned voffH = lane * 16;
  auto issue_h = [&](auto ic) __attribute__((always_inline)) {
    constexpr int i = ic.value;
    bload16<0>(hf[i][0], voffH, srdH, i * 4096); bload16<1024>(hf[i][1], voffH, srdH, i * 4096);
    bload16<2048>(hf[i][2], voffH, srdH, i * 4096); bload16<3072>(hf[i][3], voffH, srdH, i * 4096);
  };
  static_for<10>(LAMBDA(ic) { issue_tile(ic); if constexpr (ic.value < HD) issue_h(ic); });

  const int blk = (lane >> 4) & 1, q4 = (lane & 15) >> 2, p4 = lane & 3;
  const lds_cptr L = (lds_cptr)lds;
  lds_cptr wb[2], vb[2], kb_[2];
#pragma unroll
  for (int t = 0; t < 2; ++t) {
    wb[t] = L + 2048 * hi + 64 * (4 * t + q4) + 16 * ((2 * blk + (p4 & 1)) ^ (2 * hi + t)) + 8 * (p4 >> 1);
    vb[t] = L + 64 * (4 * hi + q4) + 16 * ((2 * blk + (p4 & 1)) ^ (2 * t + hi)) + 8 * (p4 >> 1) + 2048 * t;
    kb_[t] = L + 2048 * (r32 >> 3) + 64 * (r32 & 7) + 16 * ((2 * t + hi) ^ ((r32 >> 2) & 3));
  }
  auto wfrag = [&](auto ic) __attribute__((always_inline)) {
    constexpr int M = ic.value, i = M >> 4, ks = (M >> 2) & 3, db = M & 3, o = slot_of(i) + 4096 * ks + 512 * db;
    return trfrag(wb[0] + o, wb[1] + o);
  };
  auto kfrag = [&](auto it, auto ig) __attribute__((always_inline)) {
    constexpr int t = it.value, g = ig.value, kb = g >> 3, s = g & 7;
    return rowfrag(kb_[s & 1] + slot_of(tile_K(t)) + 8192 * kb + 512 * (s >> 1));
  };
  auto vfrag = [&](auto it, auto ig) __attribute__((always_inline)) {
    constexpr int t = it.value, g = ig.value, ks = g >> 2, db = g & 3, o = slot_of(tile_V(t)) + 4096 * ks + 512 * db;
    return trfrag(vb[0] + o, vb[1] + o);
  };
  auto barrier = [&](auto ig) __attribute__((always_inline)) {
    constexpr int g = ig.value;
    if constexpr (g < 10) asm volatile("s_waitcnt vmcnt(%4)\n\ts_barrier" : "+v"(hf[g][0]), "+v"(hf[g][1]), "+v"(hf[g][2]), "+v"(hf[g][3]) : "i"(PLAN.wait[g]) : "memory");
    else asm volatile("s_waitcnt vmcnt(%0)\n\ts_barrier" ::"i"(PLAN.wait[g]) : "memory");
    if constexpr (g + HD < 10) issue_h(std::integral_constant<int, g + HD>{});
    static_for<26 - NS>(LAMBDA(jc) { constexpr int i = NS + jc.value; if constexpr (issue_step(i) == g) issue_tile(std::integral_constant<int, i>{}); });
    SBAR();
  };

  constexpr int PD = 3;
  f32x16 acc[4] = {};
  bf16x8 fr[PD + 1];
  static_for<10>(LAMBDA(ic) {
    constexpr int i = ic.value;
    barrier(ic);
    if constexpr (i == 0) { static_for<PD>(LAMBDA(jc) { fr[jc.value] = wfrag(jc); }); SBAR(); }
    static_for<16>(LAMBDA(mc) {
      constexpr int m = mc.value, M = 16 * i + m;
      if constexpr (M + PD < 160) fr[(M + PD) % (PD + 1)] = wfrag(std::integral_constant<int, M + PD>{});
      acc[m & 3] = mfma32(fr[M % (PD + 1)], hf[i][m >> 2], acc[m & 3]);
      SBAR();
    });
  });
  bf16x8 qf[8];
#pragma unroll
  for (int s = 0; s < 8; ++s) {
    u32x4 w;
#pragma unroll
    for (int e = 0; e < 4; ++e) w[e] = cvtpk(acc[s >> 1][8 * (s & 1) + 2 * e] * QSCALE, acc[s >> 1][8 * (s & 1) + 2 * e + 1] * QSCALE);
    qf[s] = __builtin_bit_cast(bf16x8, w);
  }

  f32x16 o[4] = {}; f32x16 sA[2], sB[2], negm; u32x4 pw[4];
  float mref, lsum = 0.f;
  bf16x8 kf[PD + 1], vf[PD + 1];
  barrier(std::integral_constant<int, 10>{});
  {
    using T0 = std::integral_constant<int, 0>;
    static_for<PD>(LAMBDA(jc) { kf[jc.value] = kfrag(T0{}, jc); }); SBAR();
    const f32x16 zero = {};
    static_for<16>(LAMBDA(gc) {
      constexpr int g = gc.value;
      if constexpr (g + PD < 16) kf[(g + PD) % (PD + 1)] = kfrag(T0{}, std::integral_constant<int, g + PD>{});
      sA[g >> 3] = mfma32(kf[g % (PD + 1)], qf[g & 7], (g & 7) == 0 ? zero : sA[g >> 3]);
      SBAR();
    });
    float a = max3(sA[0][0], sA[0][1], sA[1][0]), b = max3(sA[0][2], sA[0][3], sA[1][1]); a = max3(a, sA[1][2], sA[1][3]);
#pragma unroll
    for (int r = 4; r < 16; r += 4) { a = max3(a, sA[0][r], sA[0][r + 1]); b = max3(b, sA[0][r + 2], sA[0][r + 3]); a = max3(a, sA[1][r], sA[1][r + 1]); b = max3(b, sA[1][r + 2], sA[1][r + 3]); }
    mref = swap_max(__builtin_fmaxf(a, b));
#pragma unroll
    for (int r = 0; r < 16; ++r) { sA[0][r] = __builtin_amdgcn_exp2f(sA[0][r] - mref); sA[1][r] = __builtin_amdgcn_exp2f(sA[1][r] - mref); negm[r] = -mref; }
    PIN(negm);
    static_for<PD>(LAMBDA(jc) { kf[jc.value] = kfrag(std::integral_constant<int, 1>{}, jc); });
    SBAR();
  }
  auto step = [&](auto tc, f32x16(&C)[2], f32x16(&P)[2]) __attribute__((always_inline)) {
    constexpr int t = tc.value;
    barrier(std::integral_constant<int, 10 + t>{});
    float sacc = 0.f;
    using TP = std::integral_constant<int, t - 1>;
    static_for<16>(LAMBDA(gc) {
      constexpr int g = gc.value;
      if constexpr (t < 8) { if constexpr (g + PD < 16) kf[(g + PD) % (PD + 1)] = kfrag(tc, std::integral_constant<int, g + PD>{}); }
      if constexpr (g + PD >= 16) vf[(g + PD - 16) % (PD + 1)] = vfrag(TP{}, std::integral_constant<int, g + PD - 16>{});
      if constexpr (t < 8) C[g >> 3] = mfma32(kf[g % (PD + 1)], qf[g & 7], (g & 7) == 0 ? negm : C[g >> 3]);
      constexpr int ks = g >> 2, w = g & 3, e = 8 * (ks & 1) + 2 * w;
      sacc += P[ks >> 1][e]; sacc += P[ks >> 1][e + 1];
      pw[ks][w] = cvtpk(P[ks >> 1][e], P[ks >> 1][e + 1]);
      PIN(pw[ks]); PIN(sacc);
      SBAR();
    });
    lsum += sacc;
    bool resc = false; float fct = 1.f; float ma, mb;
    static_for<16>(LAMBDA(gc) {
      constexpr int g = gc.value;
      if constexpr (g + PD < 16) vf[(g + PD) % (PD + 1)] = vfrag(TP{}, std::integral_constant<int, g + PD>{});
      else if constexpr (t < 7) kf[(g + PD - 16) % (PD + 1)] = kfrag(std::integral_constant<int, t + 1>{}, std::integral_constant<int, g + PD - 16>{});
      o[g & 3] = mfma32(vf[g % (PD + 1)], __builtin_bit_cast(bf16x8, pw[g >> 2]), o[g & 3]);
      if constexpr (t < 8) {
        if constexpr (g == 0) { ma = max3(C[0][0], C[0][1], C[0][2]); mb = max3(C[0][3], C[0][4], C[0][5]); ma = max3(ma, C[0][6], C[0][7]); mb = max3(mb, C[0][8], C[0][9]); PIN(ma); PIN(mb); }
        if constexpr (g == 1) { ma = max3(ma, C[0][10], C[0][11]); mb = max3(mb, C[0][12], C[0][13]); ma = max3(ma, C[0][14], C[0][15]); mb = max3(mb, C[1][0], C[1][1]); PIN(ma); PIN(mb); }
        if constexpr (g == 2) { ma = max3(ma, C[1][2], C[1][3]); mb = max3(mb, C[1][4], C[1][5]); ma = max3(ma, C[1][6], C[1][7]); mb = max3(mb, C[1][8], C[1][9]); PIN(ma); PIN(mb); }
        if constexpr (g == 3) {
          ma = max3(ma, C[1][10], C[1][11]); mb = max3(mb, C[1][12], C[1][13]); ma = max3(ma, C[1][14], C[1][15]);
          const float rm = swap_max(__builtin_fmaxf(ma, mb));
          if (__builtin_expect(__any(rm > THR), 0)) {
            const float dl = __builtin_fmaxf(rm, 0.f); mref += dl; fct = __builtin_amdgcn_exp2f(-dl); lsum *= fct; resc = true;
#pragma unroll
            for (int r = 0; r < 16; ++r) { C[0][r] -= dl; C[1][r] -= dl; negm[r] = -mref; }
          }
        }
        if constexpr (g >= 4) {
          constexpr int e0 = (32 * (g - 4)) / 12, e1 = (32 * (g - 3)) / 12;
          static_for<e1 - e0>(LAMBDA(ec) { constexpr int e = e0 + ec.value; C[e >> 4][e & 15] = __builtin_amdgcn_exp2f(C[e >> 4][e & 15]); });
          PIN(C[0]); PIN(C[1]);
        }
      }
      SBAR();
    });
    if (t < 8 && resc) {
#pragma unroll
      for (int d = 0; d < 4; ++d)
#pragma unroll
        for (int r = 0; r < 16; ++r) o[d][r] *= fct;
    }
  };
  step(std::integral_constant<int, 1>{}, sB, sA);
  step(std::integral_constant<int, 2>{}, sA, sB);
  step(std::integral_constant<int, 3>{}, sB, sA);
  step(std::integral_constant<int, 4>{}, sA, sB);
  step(std::integral_constant<int, 5>{}, sB, sA);
  step(std::integral_constant<int, 6>{}, sA, sB);
  step(std::integral_constant<int, 7>{}, sB, sA);
  step(std::integral_constant<int, 8>{}, sA, sB);
  const float inv = 1.f / swap_add(lsum);
  const int qrow = qt * 256 + wid * 32 + r32;
  u16* op = obuf + (((size_t)(qrow >> 4) * 512 + head * 16 + hi) * 16 + (qrow & 15)) * 8;
#pragma unroll
  for (int db = 0; db < 4; ++db)
#pragma unroll
    for (int s = 0; s < 2; ++s) {
      u32x4 w;
#pragma unroll
      for (int e = 0; e < 4; ++e) w[e] = cvtpk(o[db][8 * s + 2 * e] * inv, o[db][8 * s + 2 * e + 1] * inv);
      *(u32x4*)(op + (4 * db + 2 * s) * 128) = w;
    }
}

template <int NSTMAX, int KC_A, int KC_B>
DI void gemm5x4(f32x4 (&acc)[5][4], const u16* __restrict__ Abase, const u16* __restrict__ Bbase, int ks0, int nst, int lane) {
  constexpr int DP = 3;
  bf16x8 A[DP + 1][5], B[DP + 1][4];
  const GAS bf16x8* pa = (const GAS bf16x8*)(Abase + (size_t)ks0 * 512 + lane * 8);
  const GAS bf16x8* pb = (const GAS bf16x8*)(Bbase + (size_t)ks0 * 512 + lane * 8);
  auto load = [&](auto bc, int i) __attribute__((always_inline)) {
    constexpr int b = bc.value;
#pragma unroll
    for (int n = 0; n < 5; ++n) A[b][n] = pa[(size_t)n * KC_A * 16 + i * 64];
#pragma unroll
    for (int q = 0; q < 4; ++q) B[b][q] = pb[(size_t)q * KC_B * 16 + i * 64];
  };
  static_for<DP>(LAMBDA(jc) { load(jc, jc.value); });
  SBAR();
  static_for<NSTMAX>(LAMBDA(ic) {
    constexpr int i = ic.value;
    if constexpr (i + DP < NSTMAX) load(std::integral_constant<int, (i + DP) % (DP + 1)>{}, i + DP);
    SBAR();
    if (i < NSTMAX - 1 || i < nst) {
#pragma unroll
      for (int n = 0; n < 5; ++n)
#pragma unroll
        for (int q = 0; q < 4; ++q) acc[n][q] = mfma16(A[i % (DP + 1)][n], B[i % (DP + 1)][q], acc[n][q]);
    }
    SBAR();
  });
}
template <class EPI>
DI void reduce_epilogue(f32x4 (&acc)[5][4], char* lds, int tid, EPI&& epi) {
  const int wid = tid >> 6, lane = tid & 63;
  f32x4* red = (f32x4*)lds;
#pragma unroll
  for (int n = 0; n < 5; ++n)
#pragma unroll
    for (int q = 0; q < 4; ++q) red[((wid * 5 + n) * 4 + q) * 64 + lane] = acc[n][q];
  __syncthreads();
#pragma unroll
  for (int it = 0; it < 3; ++it) {
    const int item = tid + it * 256;
    if (item < 640) {
      const int q = item & 63, c = item >> 6, n = c >> 1, qs = q >> 4, l0 = (q & 15) + 32 * (c & 1);
      f32x4 lo = {0, 0, 0, 0}, hi = {0, 0, 0, 0};
#pragma unroll
      for (int w = 0; w < 4; ++w) { lo += red[((w * 5 + n) * 4 + qs) * 64 + l0]; hi += red[((w * 5 + n) * 4 + qs) * 64 + l0 + 16]; }
      epi(q, c, lo, hi);
    }
  }
}

__global__ __launch_bounds__(256, 1) void k_oproj(const u16* __restrict__ obuf, const u16* __restrict__ wot, const u16* __restrict__ x, const u16* __restrict__ mod,
                                                   u16* __restrict__ x1, float* __restrict__ stat) {
  extern __shared__ __attribute__((aligned(16))) char lds[];
  const int tid = threadIdx.x, lane = tid & 63, wid = __builtin_amdgcn_readfirstlane(tid >> 6);
  const int xcd = blockIdx.x & 7, idx = blockIdx.x >> 3, rt = xcd * 4 + (idx >> 3), j = idx & 7;
  f32x4 acc[5][4] = {};
  gemm5x4<32, 512, 512>(acc, wot + (size_t)(5 * j) * 512 * 128, obuf + (size_t)(4 * rt) * 512 * 128, wid * 32, 32, lane);
  float* part = (float*)(lds + 81920);
  reduce_epilogue(acc, lds, tid, [&](int q, int c, f32x4 lo, f32x4 hi) __attribute__((always_inline)) {
    const int row = rt * 64 + q, n0 = j * 80 + c * 8;
    const u32x4 xv = *(const u32x4*)(x + (size_t)row * DM + n0), gv = *(const u32x4*)(mod + 2 * 640 + n0);
    float v[8] = {lo[0], lo[1], lo[2], lo[3], hi[0], hi[1], hi[2], hi[3]};
    u32x4 o; float s = 0, ss = 0;
#pragma unroll
    for (int e = 0; e < 4; ++e) {
      const float a = bf_lo(xv[e]) + rbf(bf_lo(gv[e]) * rbf(v[2 * e])), b = bf_hi(xv[e]) + rbf(bf_hi(gv[e]) * rbf(v[2 * e + 1]));
      o[e] = cvtpk(a, b);
      const float ra = bf_lo(o[e]), rb = bf_hi(o[e]); s += ra + rb; ss += ra * ra + rb * rb;
    }
    *(u32x4*)(x1 + (((size_t)(row >> 4) * 80 + j * 10 + c) * 16 + (row & 15)) * 8) = o;
    part[(q * 10 + c) * 2] = s; part[(q * 10 + c) * 2 + 1] = ss;
  });
  __syncthreads();
  if (tid < 64) {
    float s = 0, ss = 0;
#pragma unroll
    for (int c = 0; c < 10; ++c) { s += part[(tid * 10 + c) * 2]; ss += part[(tid * 10 + c) * 2 + 1]; }
    *(f32x2*)(stat + ((size_t)(rt * 64 + tid) * 8 + j) * 2) = f32x2{s, ss};
  }
}

__global__ __launch_bounds__(512, 2) void k_ffn1(const u16* __restrict__ x1, const u16* __restrict__ w1t, const float* __restrict__ stat, const float* __restrict__ cs1,
                                                  const float* __restrict__ b1p, u16* __restrict__ hid) {
  extern __shared__ __attribute__((aligned(16))) char lds[];
  const int tid = threadIdx.x, lane = tid & 63, wid = __builtin_amdgcn_readfirstlane(tid >> 6);
  const int xcd = blockIdx.x & 7, idx = blockIdx.x >> 3, rt = xcd * 4 + (idx >> 3), j = idx & 7;
  const int ft0 = j < 4 ? 14 * j : 56 + 13 * (j - 4), nft = j < 4 ? 14 : 13;
  const bool two = wid + 8 < nft;
  {
    const GAS u32x4* src = (const GAS u32x4*)(x1 + (size_t)(4 * rt) * 80 * 128);
    u32x4* dst = (u32x4*)lds;
    u32x4 tmp[10];
#pragma unroll
    for (int i = 0; i < 10; ++i) tmp[i] = src[tid + 512 * i];
#pragma unroll
    for (int i = 0; i < 10; ++i) dst[tid + 512 * i] = tmp[i];
  }
  const GAS bf16x8* pa0 = (const GAS bf16x8*)(w1t + (size_t)(ft0 + wid) * 80 * 128 + lane * 8);
  const GAS bf16x8* pa1 = (const GAS bf16x8*)(w1t + (size_t)(ft0 + (two ? wid + 8 : wid)) * 80 * 128 + lane * 8);
  bf16x8 A0[20], A1[20];
#pragma unroll
  for (int i = 0; i < 20; ++i) { A0[i] = pa0[i * 64]; A1[i] = pa1[i * 64]; }
  float mean[4], rstd[4];
#pragma unroll
  for (int q = 0; q < 4; ++q) {
    const float* sp = stat + (size_t)(rt * 64 + q * 16 + (lane & 15)) * 16; float s = 0, ss = 0;
#pragma unroll
    for (int i = 0; i < 4; ++i) { const f32x4 v = *(const f32x4*)(sp + 4 * i); s += v[0] + v[2]; ss += v[1] + v[3]; }
    mean[q] = s * (1.f / DM); rstd[q] = rsqrtf(__builtin_fmaxf(ss * (1.f / DM) - mean[q] * mean[q], 0.f) + 1e-5f);
  }
  __syncthreads();
  f32x4 acc[2][4] = {};
  const LAS bf16x8* pb = (const LAS bf16x8*)lds + lane;
#pragma unroll
  for (int i = 0; i < 20; ++i)
#pragma unroll
    for (int q = 0; q < 4; ++q) {
      const bf16x8 b = pb[(q * 80 + 4 * i) * 16];
      acc[0][q] = mfma16(A0[i], b, acc[0][q]); acc[1][q] = mfma16(A1[i], b, acc[1][q]);
    }
#pragma unroll
  for (int u = 0; u < 2; ++u) {
    if (u == 1 && !two) break;
    const int ft = ft0 + wid + 8 * u, f0 = ft * 16 + 4 * (lane >> 4);
    const f32x4 cs = *(const f32x4*)(cs1 + f0), bb = *(const f32x4*)(b1p + f0);
#pragma unroll
    for (int q = 0; q < 4; ++q) {
      float v[4];
#pragma unroll
      for (int r = 0; r < 4; ++r) { float p = rstd[q] * (acc[u][q][r] - mean[q] * cs[r]) + bb[r]; p = __builtin_fmaxf(p, 0.f); v[r] = p * p; }
      const u32x2 w = {cvtpk(v[0], v[1]), cvtpk(v[2], v[3])};
      *(u32x2*)(hid + (((size_t)(4 * rt + q) * 216 + ft * 2 + (lane >> 5)) * 16 + (lane & 15)) * 8 + ((lane >> 4) & 1) * 4) = w;
    }
  }
}

__global__ __launch_bounds__(256, 1) void k_ffn2(const u16* __restrict__ hid, const u16* __restrict__ w2t, const u16* __restrict__ x1, const u16* __restrict__ mod,
                                                  const u16* __restrict__ b2, u16* __restrict__ out) {
  extern __shared__ __attribute__((aligned(16))) char lds[];
  const int tid = threadIdx.x, lane = tid & 63, wid = __builtin_amdgcn_readfirstlane(tid >> 6);
  const int xcd = blockIdx.x & 7, idx = blockIdx.x >> 3, rt = xcd * 4 + (idx >> 3), j = idx & 7;
  f32x4 acc[5][4] = {};
  const int ks0 = wid < 2 ? 14 * wid : 28 + 13 * (wid - 2), nst = wid < 2 ? 14 : 13;
  gemm5x4<14, 216, 216>(acc, w2t + (size_t)(5 * j) * 216 * 128, hid + (size_t)(4 * rt) * 216 * 128, ks0, nst, lane);
  reduce_epilogue(acc, lds, tid, [&](int q, int c, f32x4 lo, f32x4 hi) __attribute__((always_inline)) {
    const int row = rt * 64 + q, n0 = j * 80 + c * 8;
    const u32x4 xv = *(const u32x4*)(x1 + (((size_t)(row >> 4) * 80 + j * 10 + c) * 16 + (row & 15)) * 8);
    const u32x4 gv = *(const u32x4*)(mod + 5 * 640 + n0), bv = *(const u32x4*)(b2 + n0);
    float v[8] = {lo[0], lo[1], lo[2], lo[3], hi[0], hi[1], hi[2], hi[3]};
    u32x4 o;
#pragma unroll
    for (int e = 0; e < 4; ++e) {
      const float a = bf_lo(xv[e]) + rbf(bf_lo(gv[e]) * rbf(v[2 * e] + bf_lo(bv[e]))), b = bf_hi(xv[e]) + rbf(bf_hi(gv[e]) * rbf(v[2 * e + 1] + bf_hi(bv[e])));
      o[e] = cvtpk(a, b);
    }
    *(u32x4*)(out + (size_t)row * DM + n0) = o;
  });
}

extern "C" long long* dbg_offsets() { static long long o[5] = {(long long)WS_MOD, (long long)WS_H, (long long)WS_O, (long long)WS_X1, (long long)WS_HID}; return o; }

extern "C" void kernel_launch(void* const* d_in, const int* in_sizes, int n_in, void* d_out, int out_size, void* d_ws, size_t ws_size, hipStream_t stream) {
  const u16 *x = (const u16*)d_in[0], *kc = (const u16*)d_in[1], *vc = (const u16*)d_in[2], *wq = (const u16*)d_in[3], *wo = (const u16*)d_in[4];
  const u16 *w1 = (const u16*)d_in[6], *b1 = (const u16*)d_in[7], *w2 = (const u16*)d_in[8], *b2 = (const u16*)d_in[9], *c = (const u16*)d_in[10], *wmod = (const u16*)d_in[11];
  char* ws = (char*)d_ws;
  u16 *mod = (u16*)(ws + WS_MOD), *h = (u16*)(ws + WS_H), *o = (u16*)(ws + WS_O), *wot = (u16*)(ws + WS_WOT), *w1t = (u16*)(ws + WS_W1T), *w2t = (u16*)(ws + WS_W2T);
  u16 *x1 = (u16*)(ws + WS_X1), *hid = (u16*)(ws + WS_HID);
  float *cs1 = (float*)(ws + WS_CS1), *b1p = (float*)(ws + WS_B1P), *stat = (float*)(ws + WS_STAT);
  (void)hipFuncSetAttribute((const void*)k_attn, hipFuncAttributeMaxDynamicSharedMemorySize, NS * SLOT);
  (void)hipFuncSetAttribute((const void*)k_oproj, hipFuncAttributeMaxDynamicSharedMemorySize, 90112);
  (void)hipFuncSetAttribute((const void*)k_ffn1, hipFuncAttributeMaxDynamicSharedMemorySize, 90112);
  (void)hipFuncSetAttribute((const void*)k_ffn2, hipFuncAttributeMaxDynamicSharedMemorySize, 90112);
  k_mod<<<60, 512, 0, stream>>>(c, wmod, mod);
  k_ln1<<<128, 256, 0, stream>>>(x, mod, h);
  k_retile<<<(4096 * 640 / 8 + 255) / 256, 256, 0, stream>>>(wo, wot, 4096, 640, nullptr);
  k_retile<<<(640 * 1728 / 8 + 255) / 256, 256, 0, stream>>>(w1, w1t, 640, 1728, mod + 4 * 640);
  k_retile<<<(640 * 1728 / 8 + 255) / 256, 256, 0, stream>>>(w2, w2t, 1728, 640, nullptr);
  k_w1sums<<<27, 64, 0, stream>>>(w1, b1, mod, cs1, b1p);
  k_attn<<<256, 512, NS * SLOT, stream>>>(h, wq, kc, vc, o);
  k_oproj<<<256, 256, 90112, stream>>>(o, wot, x, mod, x1, stat);
  k_ffn1<<<256, 512, 90112, stream>>>(x1, w1t, stat, cs1, b1p, hid);
  k_ffn2<<<256, 256, 90112, stream>>>(hid, w2t, x1, mod, b2, (u16*)d_out);
}
```

```cpp
#include <hip/hip_runtime.h>
#include <stdint.h>
#include <utility>

typedef unsigned short u16;
typedef short bf16x8 __attribute__((ext_vector_type(8)));
typedef short s16x4 __attribute__((ext_vector_type(4)));
typedef float f32x16 __attribute__((ext_vector_type(16)));
typedef float f32x4 __attribute__((ext_vector_type(4)));
typedef float f32x2 __attribute__((ext_vector_type(2)));
typedef __bf16 bf16x2 __attribute__((ext_vector_type(2)));
typedef unsigned u32x4 __attribute__((ext_vector_type(4)));
typedef unsigned u32x2 __attribute__((ext_vector_type(2)));
#define DI __device__ __forceinline__
#define GAS __attribute__((address_space(1)))
#define LAS __attribute__((address_space(3)))
#define SBAR() __builtin_amdgcn_sched_barrier(0)
#define PIN(x) asm volatile("" : "+v"(x))

constexpr int SQ = 2048, DM = 640, NH = 32, DH = 128, SK = 2048, WIN = 512, FF = 1728;
constexpr size_t WS_MOD = 0;
constexpr size_t WS_CS1 = 8192;
constexpr size_t WS_B1P = 16384;
constexpr size_t WS_STAT = 32768;
constexpr size_t WS_H = WS_STAT + 2048 * 8 * 2 * 4;
constexpr size_t WS_O = WS_H + (size_t)SQ * DM * 2;
constexpr size_t WS_WOT = WS_O + (size_t)SQ * 4096 * 2;
constexpr size_t WS_W1T = WS_WOT + (size_t)4096 * DM * 2;
constexpr size_t WS_W2T = WS_W1T + (size_t)DM * FF * 2;
constexpr size_t WS_X1 = WS_W2T + (size_t)DM * FF * 2 + 65536;
constexpr size_t WS_HID = WS_X1 + (size_t)SQ * DM * 2;
constexpr size_t WS_H2 = WS_HID + (size_t)SQ * FF * 2 + 65536;
constexpr size_t WS_P = WS_H2 + (size_t)SQ * DM * 2;
constexpr size_t WS_END = WS_P + (size_t)4 * SQ * DM * 2;

#define STAMP_ARG
#define STAMP_FWD
#define STAMP_PASS(k)
#define ST(i)
#define STC(i)
template <int... I, class F> DI void static_for_impl(std::integer_sequence<int, I...>, F&& f) { (f(std::integral_constant<int, I>{}), ...); }
template <int N, class F> DI void static_for(F&& f) { static_for_impl(std::make_integer_sequence<int, N>{}, f); }
#define LAMBDA(ic) [&](auto ic) __attribute__((always_inline))

DI float bf_lo(unsigned w) { return __uint_as_float(w << 16); }
DI float bf_hi(unsigned w) { return __uint_as_float(w & 0xffff0000u); }
DI float bf2f(u16 v) { return __uint_as_float((unsigned)v << 16); }
DI unsigned cvtpk(float lo, float hi) { f32x2 v = {lo, hi}; bf16x2 b = __builtin_convertvector(v, bf16x2); return __builtin_bit_cast(unsigned, b); }
DI u16 f2bf(float x) { return (u16)(cvtpk(x, 0.f) & 0xffffu); }
DI float rbf(float x) { return bf_lo(cvtpk(x, 0.f)); }
DI f32x16 mfma32(bf16x8 a, bf16x8 b, f32x16 c) { return __builtin_amdgcn_mfma_f32_32x32x16_bf16(a, b, c, 0, 0, 0); }
DI f32x4 mfma16(bf16x8 a, bf16x8 b, f32x4 c) { return __builtin_amdgcn_mfma_f32_16x16x32_bf16(a, b, c, 0, 0, 0); }
DI float swap_max(float m) { auto r = __builtin_amdgcn_permlane32_swap(__float_as_uint(m), __float_as_uint(m), false, false); return __builtin_fmaxf(__uint_as_float(r[0]), __uint_as_float(r[1])); }
DI float swap_add(float m) { auto r = __builtin_amdgcn_permlane32_swap(__float_as_uint(m), __float_as_uint(m), false, false); return __uint_as_float(r[0]) + __uint_as_float(r[1]); }

__global__ __launch_bounds__(512) void k_mod(const u16* __restrict__ c, const u16* __restrict__ wmod, u16* __restrict__ mod STAMP_ARG) {
  ST(0);
  __shared__ float red[8][64];
  const int t = threadIdx.x, cc = t & 7, r0 = t >> 3, col0 = blockIdx.x * 64 + cc * 8;
  u32x4 w[10]; float s[10];
#pragma unroll
  for (int i = 0; i < 10; ++i) w[i] = *(const u32x4*)(wmod + (size_t)(r0 + 64 * i) * 3840 + col0);
#pragma unroll
  for (int i = 0; i < 10; ++i) { float x = bf2f(c[r0 + 64 * i]); s[i] = rbf(x / (1.f + __expf(-x))); }
  float a[8] = {};
#pragma unroll
  for (int i = 0; i < 10; ++i)
#pragma unroll
    for (int j = 0; j < 4; ++j) { a[2 * j] += s[i] * bf_lo(w[i][j]); a[2 * j + 1] += s[i] * bf_hi(w[i][j]); }
#pragma unroll
  for (int j = 0; j < 8; ++j) { a[j] += __shfl_xor(a[j], 8); a[j] += __shfl_xor(a[j], 16); a[j] += __shfl_xor(a[j], 32); }
  if ((t & 63) < 8) {
#pragma unroll
    for (int j = 0; j < 8; ++j) red[t >> 6][cc * 8 + j] = a[j];
  }
  __syncthreads();
  if (t < 64) { float v = 0; for (int w8 = 0; w8 < 8; ++w8) v += red[w8][t]; mod[blockIdx.x * 64 + t] = f2bf(v); }
  ST(31);
}

__global__ __launch_bounds__(256) void k_ln1(const u16* __restrict__ x, const u16* __restrict__ mod, u16* __restrict__ h STAMP_ARG) {
  ST(0);
  const int t = threadIdx.x, i = t & 15, row = blockIdx.x * 16 + (t >> 4);
  u32x4 v[5], sa[5], ca[5];
#pragma unroll
  for (int j = 0; j < 5; ++j) v[j] = *(const u32x4*)(x + (size_t)row * DM + (i + 16 * j) * 8);
#pragma unroll
  for (int j = 0; j < 5; ++j) { sa[j] = *(const u32x4*)(mod + (i + 16 * j) * 8); ca[j] = *(const u32x4*)(mod + 640 + (i + 16 * j) * 8); }
  float f[40]; float s = 0;
#pragma unroll
  for (int j = 0; j < 5; ++j)
#pragma unroll
    for (int e = 0; e < 4; ++e) { f[j * 8 + 2 * e] = bf_lo(v[j][e]); f[j * 8 + 2 * e + 1] = bf_hi(v[j][e]); s += f[j * 8 + 2 * e] + f[j * 8 + 2 * e + 1]; }
#pragma unroll
  for (int m = 1; m < 16; m <<= 1) s += __shfl_xor(s, m);
  const float mean = s * (1.f / DM); float q = 0;
#pragma unroll
  for (int e = 0; e < 40; ++e) { f[e] -= mean; q += f[e] * f[e]; }
#pragma unroll
  for (int m = 1; m < 16; m <<= 1) q += __shfl_xor(q, m);
  const float rstd = rsqrtf(q * (1.f / DM) + 1e-5f);
  const int qb = row >> 5, r32 = row & 31;
#pragma unroll
  for (int j = 0; j < 5; ++j) {
    u32x4 o;
#pragma unroll
    for (int e = 0; e < 4; ++e) {
      float lo = f[j * 8 + 2 * e] * rstd * rbf(1.f + bf_lo(ca[j][e])) + bf_lo(sa[j][e]);
      float hi = f[j * 8 + 2 * e + 1] * rstd * rbf(1.f + bf_hi(ca[j][e])) + bf_hi(sa[j][e]);
      o[e] = cvtpk(lo, hi);
    }
    *(u32x4*)(h + (((size_t)qb * 80 + (i + 16 * j)) * 32 + r32) * 8) = o;
  }
  ST(31);
}

__global__ void k_retile(const u16* __restrict__ W, u16* __restrict__ out, int K, int N, const u16* __restrict__ cscale) {
  const int idx = blockIdx.x * blockDim.x + threadIdx.x;
  const int KC = K / 8; if (idx >= (N / 16) * KC * 16) return;
  const int n16 = idx & 15, c = (idx >> 4) % KC, nt = (idx >> 4) / KC;
  float v[8];
  for (int j = 0; j < 8; ++j) { v[j] = bf2f(W[(size_t)(8 * c + j) * N + nt * 16 + n16]); if (cscale) v[j] *= rbf(1.f + bf2f(cscale[8 * c + j])); }
  u32x4 o = {cvtpk(v[0], v[1]), cvtpk(v[2], v[3]), cvtpk(v[4], v[5]), cvtpk(v[6], v[7])};
  *(u32x4*)(out + (size_t)idx * 8) = o;
}

#define A_NS 8
#define A_PD 5
#define O_AUX 16
constexpr int NS = A_NS, SLOT = 16384;
constexpr float QSCALE = 0.088388347648318440f * 1.4426950408889634f;
constexpr float PBIAS = 96.f;
constexpr float PTHR = 30.f;
constexpr int tile_K(int t) { return t == 0 ? 10 : 9 + 2 * t; }
constexpr int tile_V(int t) { return t == 7 ? 25 : 12 + 2 * t; }
constexpr int slot_of(int idx) { return (idx % NS) * SLOT; }
constexpr int lastuse(int idx) { return idx < 10 ? idx : (idx == 10 ? 10 : (idx == 25 ? 18 : ((idx & 1) ? 10 + (idx - 9) / 2 : 11 + (idx - 12) / 2))); }
constexpr int need_bar(int idx) {
  if (idx < 10) return idx == 0 ? 0 : idx - 1;
  if (idx == 10) return 9;
  if (idx == 25) return 17;
  return (idx & 1) ? 10 + (idx - 9) / 2 - 1 : 11 + (idx - 12) / 2;
}
struct Sched { int npro; int pro[16]; int nops[19]; int op[19][12]; int wait[19]; };
constexpr Sched make_sched() {
  Sched p{}; int pos = 0; int tile_end[26] = {}; int h_end[10] = {}; int next_tile = 3;
  for (int i = 0; i < 3; ++i) {
    p.pro[p.npro++] = 2 * i; p.pro[p.npro++] = 2 * i + 1; pos += 2; tile_end[i] = pos;
    if (i < 2) { for (int k = 0; k < 4; ++k) p.pro[p.npro++] = 100 + 4 * i + k; pos += 4; h_end[i] = pos; }
  }
  for (int g = 0; g < 19; ++g) {
    int need = 0;
    for (int idx = 0; idx < next_tile; ++idx) if (need_bar(idx) <= g && tile_end[idx] > need) need = tile_end[idx];
    if (g < 10 && h_end[g] > need) need = h_end[g];
    p.wait[g] = pos - need;
    int n = 0;
    if (g == 0) { for (int k = 0; k < 4; ++k) p.op[g][n++] = 100 + 4 * 2 + k; pos += 4; h_end[2] = pos; }
    const int maxt = g < 7 ? 1 : 2;
    for (int c = 0; c < maxt && next_tile < 26; ++c) {
      if (next_tile >= NS && lastuse(next_tile - NS) >= g) break;
      p.op[g][n++] = 2 * next_tile; p.op[g][n++] = 2 * next_tile + 1; pos += 2; tile_end[next_tile] = pos; ++next_tile;
    }
    if (g + 3 < 10) { for (int k = 0; k < 4; ++k) p.op[g][n++] = 100 + 4 * (g + 3) + k; pos += 4; h_end[g + 3] = pos; }
    p.nops[g] = n;
  }
  for (int idx = 0; idx < 26; ++idx) if (tile_end[idx] == 0) p.wait[0] = -1;
  return p;
}
constexpr Sched SCH = make_sched();
static_assert(SCH.wait[0] >= 0, "schedule incomplete");
constexpr bool sched_ok() {
  int step_of[26] = {}; for (int i = 0; i < 26; ++i) step_of[i] = -1;
  for (int g = 0; g < 19; ++g) for (int j = 0; j < SCH.nops[g]; ++j) if (SCH.op[g][j] < 100) step_of[SCH.op[g][j] >> 1] = g;
  for (int i = 3; i < 26; ++i) if (step_of[i] >= need_bar(i)) return false;
  return true;
}
static_assert(sched_ok(), "a tile is issued too late");

DI void dma16(unsigned ldsaddr, unsigned voff, __amdgpu_buffer_rsrc_t srd, unsigned soff) {
  asm volatile("s_mov_b32 m0, %0\n\ts_nop 0\n\tbuffer_load_dwordx4 %1, %2, %3 offen lds" ::"s"(ldsaddr), "v"(voff), "s"(srd), "s"(soff) : "memory");
}
template <int OFF> DI void bload16(bf16x8& dst, unsigned voff, __amdgpu_buffer_rsrc_t srd, unsigned soff) {
  asm volatile("buffer_load_dwordx4 %0, %1, %2, %3 offen offset:%4" : "=v"(dst) : "v"(voff), "s"(srd), "s"(soff), "i"(OFF) : "memory");
}
typedef LAS const char* lds_cptr;
typedef short v4i16_t __attribute__((ext_vector_type(4)));
DI s16x4 vtr(lds_cptr p) { return __builtin_bit_cast(s16x4, __builtin_amdgcn_ds_read_tr16_b64_v4i16((LAS v4i16_t*)p)); }
DI bf16x8 trfrag(lds_cptr p0, lds_cptr p1) { const s16x4 lo = vtr(p0), hi = vtr(p1); return __builtin_shufflevector(lo, hi, 0, 1, 2, 3, 4, 5, 6, 7); }
DI bf16x8 rowfrag(lds_cptr p) { return *(LAS const bf16x8*)p; }
DI float max3(float a, float b, float c) { return __builtin_fmaxf(__builtin_fmaxf(a, b), c); }

template <int N> DI void wait_bar4(bf16x8 (&b)[4]) {
  asm volatile("s_waitcnt vmcnt(%4)\n\ts_barrier" : "+v"(b[0]), "+v"(b[1]), "+v"(b[2]), "+v"(b[3]) : "i"(N) : "memory");
}
template <int N> DI void wait_bar() { asm volatile("s_waitcnt vmcnt(%0)\n\ts_barrier" ::"i"(N) : "memory"); }
template <int V> using IC = std::integral_constant<int, V>;

__global__ __launch_bounds__(512, 2) void k_attn(const u16* __restrict__ hbuf, const u16* __restrict__ wq, const u16* __restrict__ kc, const u16* __restrict__ vc, u16* __restrict__ obuf STAMP_ARG) {
  ST(0);
  extern __shared__ __attribute__((aligned(16))) char lds[];
  const int tid = threadIdx.x, lane = tid & 63, r32 = lane & 31, hi = lane >> 5;
  const int wid = __builtin_amdgcn_readfirstlane(tid >> 6);
  const int xcd = blockIdx.x & 7, idx = blockIdx.x >> 3;
  const int head = (xcd & 3) * 8 + (idx & 7), qt = (xcd >> 2) * 4 + (idx >> 3);
  const unsigned lds0 = (unsigned)(uintptr_t)lds;
  const __amdgpu_buffer_rsrc_t srdW = __builtin_amdgcn_make_buffer_rsrc((void*)(wq + head * DH), 0, (DM - 1) * 8192 + 256, 0x00020000);
  const __amdgpu_buffer_rsrc_t srdK = __builtin_amdgcn_make_buffer_rsrc((void*)(kc + ((size_t)head * SK + (SK - WIN)) * DH), 0, WIN * 256, 0x00020000);
  const __amdgpu_buffer_rsrc_t srdV = __builtin_amdgcn_make_buffer_rsrc((void*)(vc + ((size_t)head * SK + (SK - WIN)) * DH), 0, WIN * 256, 0x00020000);
  const __amdgpu_buffer_rsrc_t srdH = __builtin_amdgcn_make_buffer_rsrc((void*)(hbuf + (size_t)(qt * 8 + wid) * 80 * 256), 0, 80 * 512, 0x00020000);
  const int dr = (lane >> 2) & 7, dx = lane & 3, dc = lane >> 5, dz = (2 * wid + (dr >> 2)) & 3;
  const unsigned voffW = (unsigned)((8 * wid + dr) * 8192 + 64 * dc + 16 * (dx ^ dz));
  const unsigned voffKV = (unsigned)((8 * wid + dr) * 256 + 64 * dc + 16 * (dx ^ dz));
  const unsigned ldsw = lds0 + 2048 * wid;
  const unsigned voffH = lane * 16;
  bf16x8 hf[10][4];
  auto vmem_op = [&](auto oc) __attribute__((always_inline)) {
    constexpr int op = oc.value;
    if constexpr (op >= 100) { constexpr int i = (op - 100) >> 2, k = (op - 100) & 3; bload16<1024 * k>(hf[i][k], voffH, srdH, i * 4096); }
    else {
      constexpr int i = op >> 1, half = op & 1; constexpr unsigned so = slot_of(i) + 1024 * half;
      if constexpr (i < 10) dma16(ldsw + so, voffW, srdW, i * 64 * 8192 + 128 * half);
      else {
        constexpr bool isK = (i == 10) || (i != 25 && (i & 1)); constexpr int t = i == 10 ? 0 : (i == 25 ? 7 : (isK ? (i - 9) / 2 : (i - 12) / 2));
        dma16(ldsw + so, voffKV, isK ? srdK : srdV, t * 16384 + 128 * half);
      }
    }
  };
  static_for<SCH.npro>(LAMBDA(jc) { vmem_op(IC<SCH.pro[jc.value]>{}); });
  auto vmem_gap = [&](auto gc, auto jc, auto nc) __attribute__((always_inline)) {
    constexpr int g = gc.value, j = jc.value, n = nc.value, no = SCH.nops[g];
    constexpr int i0 = (j * no + n - 1) / n, i1 = ((j + 1) * no + n - 1) / n;
    static_for<i1 - i0>(LAMBDA(uc) { vmem_op(IC<SCH.op[g][i0 + uc.value]>{}); });
  };

  const int blk = (lane >> 4) & 1, q4 = (lane & 15) >> 2, p4 = lane & 3;
  constexpr int NR = (NS * SLOT + 65535) / 65536;
  const lds_cptr L = (lds_cptr)lds;
  lds_cptr wb[NR][2], vb[NR][2], kb_[NR][2];
#pragma unroll
  for (int r = 0; r < NR; ++r)
#pragma unroll
    for (int t = 0; t < 2; ++t) {
      wb[r][t] = L + 65536 * r + 2048 * hi + 64 * (4 * t + q4) + 16 * ((2 * blk + (p4 & 1)) ^ (2 * hi + t)) + 8 * (p4 >> 1);
      vb[r][t] = L + 65536 * r + 64 * (4 * hi + q4) + 16 * ((2 * blk + (p4 & 1)) ^ (2 * t + hi)) + 8 * (p4 >> 1) + 2048 * t;
      kb_[r][t] = L + 65536 * r + 2048 * (r32 >> 3) + 64 * (r32 & 7) + 16 * ((2 * t + hi) ^ ((r32 >> 2) & 3));
      PIN(wb[r][t]);
    }
  constexpr int PD = A_PD, NG = 416;
  bf16x8 fr[PD + 1];
  auto ldfrag_g = [&](auto Gc) __attribute__((always_inline)) {
    constexpr int G = Gc.value;
    if constexpr (G < NG) {
      if constexpr (G < 160) {
        constexpr int i = G >> 4, ks = (G >> 2) & 3, db = G & 3, o = slot_of(i) + 4096 * ks + 512 * db;
        fr[G % (PD + 1)] = trfrag(wb[o >> 16][0] + (o & 65535), wb[o >> 16][1] + (o & 65535));
      } else {
        constexpr int u = G - 176, t = G < 176 ? 0 : (G >= 400 ? 8 : 1 + u / 32), g = G < 176 ? G - 160 : (G >= 400 ? G - 400 + 16 : u % 32);
        if constexpr (g < 16) {
          constexpr int kb = g >> 3, s = g & 7, o = slot_of(tile_K(t)) + 8192 * kb + 512 * (s >> 1);
          fr[G % (PD + 1)] = rowfrag(kb_[o >> 16][s & 1] + (o & 65535));
        } else {
          constexpr int gg = g - 16, ks = gg >> 2, db = gg & 3, o = slot_of(tile_V(t - 1)) + 4096 * ks + 512 * db;
          fr[G % (PD + 1)] = trfrag(vb[o >> 16][0] + (o & 65535), vb[o >> 16][1] + (o & 65535));
        }
      }
    }
  };
  auto barrier = [&](auto gc) __attribute__((always_inline)) {
    constexpr int g = gc.value;
    if constexpr (g < 10) wait_bar4<SCH.wait[g]>(hf[g]); else wait_bar<SCH.wait[g]>();
    ST(1 + g);
    if constexpr (g == 0) STC(28);
    if constexpr (g == 10) STC(29);
    if constexpr (g == 18) STC(30);
    SBAR();
  };

  f32x16 acc[4] = {};
  static_for<10>(LAMBDA(ic) {
    constexpr int i = ic.value;
    barrier(ic);
    if constexpr (i == 0) { static_for<PD>(LAMBDA(jc) { ldfrag_g(jc); }); SBAR(); }
    static_for<16>(LAMBDA(mc) {
      constexpr int m = mc.value, G = 16 * i + m;
      ldfrag_g(IC<G + PD>{});
      vmem_gap(ic, mc, IC<16>{});
      acc[m & 3] = mfma32(fr[G % (PD + 1)], hf[i][m >> 2], acc[m & 3]);
      SBAR();
    });
  });
  bf16x8 qf[8];
#pragma unroll
  for (int s = 0; s < 8; ++s) {
    u32x4 w;
#pragma unroll
    for (int e = 0; e < 4; ++e) w[e] = cvtpk(acc[s >> 1][8 * (s & 1) + 2 * e] * QSCALE, acc[s >> 1][8 * (s & 1) + 2 * e + 1] * QSCALE);
    qf[s] = __builtin_bit_cast(bf16x8, w);
  }
#pragma unroll
  for (int r = 0; r < NR; ++r)
#pragma unroll
    for (int t = 0; t < 2; ++t) { PIN(vb[r][t]); PIN(kb_[r][t]); }

  f32x16 o[4] = {}; f32x16 sA[2], sB[2], negm; u32x4 pw[4];
  float mref, lsum = 0.f;
  barrier(IC<10>{});
  {
    const f32x16 zero = {};
    static_for<16>(LAMBDA(gc) {
      constexpr int g = gc.value, G = 160 + g;
      ldfrag_g(IC<G + PD>{});
      vmem_gap(IC<10>{}, gc, IC<16>{});
      sA[g >> 3] = mfma32(fr[G % (PD + 1)], qf[g & 7], (g & 7) == 0 ? zero : sA[g >> 3]);
      SBAR();
    });
    float a = max3(sA[0][0], sA[0][1], sA[1][0]), b = max3(sA[0][2], sA[0][3], sA[1][1]); a = max3(a, sA[1][2], sA[1][3]);
#pragma unroll
    for (int r = 4; r < 16; r += 4) { a = max3(a, sA[0][r], sA[0][r + 1]); b = max3(b, sA[0][r + 2], sA[0][r + 3]); a = max3(a, sA[1][r], sA[1][r + 1]); b = max3(b, sA[1][r + 2], sA[1][r + 3]); }
    mref = swap_max(__builtin_fmaxf(a, b)) + PBIAS;
#pragma unroll
    for (int r = 0; r < 16; ++r) { sA[0][r] = __builtin_amdgcn_exp2f(sA[0][r] - mref); sA[1][r] = __builtin_amdgcn_exp2f(sA[1][r] - mref); negm[r] = -mref; }
    PIN(negm);
    SBAR();
  }
  auto step = [&](auto tc, f32x16(&C)[2], f32x16(&P)[2]) __attribute__((always_inline)) {
    constexpr int t = tc.value;
    constexpr int G0 = 176 + 32 * (t - 1), GB = t < 8 ? G0 + 16 : 400;
    barrier(IC<10 + t>{});
    float sacc = 0.f;
    static_for<16>(LAMBDA(gc) {
      constexpr int g = gc.value;
      if constexpr (t < 8) {
        ldfrag_g(IC<G0 + g + PD>{});
        vmem_gap(IC<10 + t>{}, gc, IC<32>{});
        C[g >> 3] = mfma32(fr[(G0 + g) % (PD + 1)], qf[g & 7], (g & 7) == 0 ? negm : C[g >> 3]);
      }
      constexpr int ks = g >> 2, w = g & 3, e = 8 * (ks & 1) + 2 * w;
      sacc += P[ks >> 1][e]; sacc += P[ks >> 1][e + 1];
      pw[ks][w] = cvtpk(P[ks >> 1][e], P[ks >> 1][e + 1]);
      PIN(pw[ks]); PIN(sacc);
      SBAR();
    });
    lsum += sacc;
    bool resc = false; float fct = 1.f; float ma, mb;
    static_for<16>(LAMBDA(gc) {
      constexpr int g = gc.value;
      ldfrag_g(IC<GB + g + PD>{});
      if constexpr (t < 8) vmem_gap(IC<10 + t>{}, IC<16 + g>{}, IC<32>{});
      o[g & 3] = mfma32(fr[(GB + g) % (PD + 1)], __builtin_bit_cast(bf16x8, pw[g >> 2]), o[g & 3]);
      if constexpr (t < 8) {
        if constexpr (g == 0) { ma = max3(C[0][0], C[0][1], C[0][2]); mb = max3(C[0][3], C[0][4], C[0][5]); ma = max3(ma, C[0][6], C[0][7]); mb = max3(mb, C[0][8], C[0][9]); PIN(ma); PIN(mb); }
        if constexpr (g == 1) { ma = max3(ma, C[0][10], C[0][11]); mb = max3(mb, C[0][12], C[0][13]); ma = max3(ma, C[0][14], C[0][15]); mb = max3(mb, C[1][0], C[1][1]); PIN(ma); PIN(mb); }
        if constexpr (g == 2) { ma = max3(ma, C[1][2], C[1][3]); mb = max3(mb, C[1][4], C[1][5]); ma = max3(ma, C[1][6], C[1][7]); mb = max3(mb, C[1][8], C[1][9]); PIN(ma); PIN(mb); }
        if constexpr (g == 3) {
          ma = max3(ma, C[1][10], C[1][11]); mb = max3(mb, C[1][12], C[1][13]); ma = max3(ma, C[1][14], C[1][15]);
          const float rm = swap_max(__builtin_fmaxf(ma, mb));
          if (__builtin_expect(__any(rm > PTHR), 0)) {
            const float dl = __builtin_fmaxf(rm + PBIAS, 0.f); mref += dl; fct = __builtin_amdgcn_exp2f(-dl); lsum *= fct; resc = true;
#pragma unroll
            for (int r = 0; r < 16; ++r) { C[0][r] -= dl; C[1][r] -= dl; negm[r] = -mref; }
          }
        }
        if constexpr (g >= 4) {
          constexpr int e0 = (32 * (g - 4)) / 12, e1 = (32 * (g - 3)) / 12;
          static_for<e1 - e0>(LAMBDA(ec) { constexpr int e = e0 + ec.value; C[e >> 4][e & 15] = __builtin_amdgcn_exp2f(C[e >> 4][e & 15]); });
          PIN(C[0]); PIN(C[1]);
        }
      }
      SBAR();
    });
    if (t < 8 && resc) {
#pragma unroll
      for (int d = 0; d < 4; ++d)
#pragma unroll
        for (int r = 0; r < 16; ++r) o[d][r] *= fct;
    }
  };
  step(IC<1>{}, sB, sA);
  step(IC<2>{}, sA, sB);
  step(IC<3>{}, sB, sA);
  step(IC<4>{}, sA, sB);
  step(IC<5>{}, sB, sA);
  step(IC<6>{}, sA, sB);
  step(IC<7>{}, sB, sA);
  step(IC<8>{}, sA, sB);
  ST(20);
  const float inv = 1.f / swap_add(lsum);
  const int qrow = qt * 256 + wid * 32 + r32;
  const __amdgpu_buffer_rsrc_t srdO = __builtin_amdgcn_make_buffer_rsrc((void*)obuf, 0, SQ * 4096 * 2, 0x00020000);
  const unsigned voffO = (((qrow >> 4) * 512 + head * 16 + hi) * 16 + (qrow & 15)) * 16;
#pragma unroll
  for (int db = 0; db < 4; ++db)
#pragma unroll
    for (int s = 0; s < 2; ++s) {
      u32x4 w;
#pragma unroll
      for (int e = 0; e < 4; ++e) w[e] = cvtpk(o[db][8 * s + 2 * e] * inv, o[db][8 * s + 2 * e + 1] * inv);
      __builtin_amdgcn_raw_buffer_store_b128(w, srdO, voffO + (4 * db + 2 * s) * 256, 0, O_AUX);
    }
  ST(21);
  ST(31);
}

#define C_NW 8
#define C_DP 2
#define ST_AUX 16
DI bf16x8 ldfrag(__amdgpu_buffer_rsrc_t r, unsigned voff, unsigned soff) { return __builtin_bit_cast(bf16x8, __builtin_amdgcn_raw_buffer_load_b128(r, voff, soff, 0)); }
DI void st16(__amdgpu_buffer_rsrc_t r, unsigned voff, u32x4 v) { __builtin_amdgcn_raw_buffer_store_b128(v, r, voff, 0, ST_AUX); }
DI void st8(__amdgpu_buffer_rsrc_t r, unsigned voff, u32x2 v) { __builtin_amdgcn_raw_buffer_store_b64(v, r, voff, 0, ST_AUX); }
template <int NSTMAX, int DP, int KC_A, int KC_B>
DI void gemm5x4(f32x4 (&acc)[5][4], __amdgpu_buffer_rsrc_t ra, __amdgpu_buffer_rsrc_t rb, int ks0, int nst, int lane) {
  bf16x8 A[DP + 1][5], B[DP + 1][4];
  const unsigned voff = lane * 16 + ks0 * 1024;
  auto load = [&](auto bc, auto ic) __attribute__((always_inline)) {
    constexpr int b = bc.value, i = ic.value;
    B[b][0] = ldfrag(rb, voff, i * 1024); A[b][0] = ldfrag(ra, voff, i * 1024);
#pragma unroll
    for (int n = 1; n < 4; ++n) { B[b][n] = ldfrag(rb, voff, n * KC_B * 256 + i * 1024); A[b][n] = ldfrag(ra, voff, n * KC_A * 256 + i * 1024); }
    A[b][4] = ldfrag(ra, voff, 4 * KC_A * 256 + i * 1024);
  };
  static_for<(DP < NSTMAX ? DP : NSTMAX)>(LAMBDA(jc) { load(jc, jc); });
  SBAR();
  static_for<NSTMAX>(LAMBDA(ic) {
    constexpr int i = ic.value;
    if constexpr (i + DP < NSTMAX) load(std::integral_constant<int, (i + DP) % (DP + 1)>{}, std::integral_constant<int, i + DP>{});
    SBAR();
    if (i < NSTMAX - 1 || i < nst) {
#pragma unroll
      for (int n = 0; n < 5; ++n)
#pragma unroll
        for (int q = 0; q < 4; ++q) acc[n][q] = mfma16(A[i % (DP + 1)][n], B[i % (DP + 1)][q], acc[n][q]);
    }
    SBAR();
  });
}
template <int NW, class EPI>
DI void reduce_epilogue(f32x4 (&acc)[5][4], char* lds, int tid, EPI&& epi) {
  const int wid = tid >> 6, lane = tid & 63;
  f32x4* red = (f32x4*)lds;
#pragma unroll
  for (int n = 0; n < 5; ++n)
#pragma unroll
    for (int q = 0; q < 4; ++q) red[((wid * 5 + n) * 4 + q) * 64 + lane] = acc[n][q];
  __syncthreads();
  constexpr int NIT = (640 + NW * 64 - 1) / (NW * 64);
  static_for<NIT>(LAMBDA(itc) {
    constexpr int it = itc.value;
    const int item = tid + it * NW * 64;
    if (item < 640) {
      const int q = item & 63, c = item >> 6, n = c >> 1, qs = q >> 4, l0 = (q & 15) + 32 * (c & 1);
      f32x4 lo = {0, 0, 0, 0}, hi = {0, 0, 0, 0};
#pragma unroll
      for (int w = 0; w < NW; ++w) { lo += red[((w * 5 + n) * 4 + qs) * 64 + l0]; hi += red[((w * 5 + n) * 4 + qs) * 64 + l0 + 16]; }
      epi(itc, q, c, lo, hi);
    }
  });
}
constexpr int C_NIT = (640 + C_NW * 64 - 1) / (C_NW * 64);
constexpr int C_LDS = C_NW * 20480 > 90112 ? C_NW * 20480 : 90112;

template <int N> DI void wait_bar8(bf16x8 (&b)[4][2]) {
  asm volatile("s_waitcnt vmcnt(%8)\n\ts_barrier" : "+v"(b[0][0]), "+v"(b[0][1]), "+v"(b[1][0]), "+v"(b[1][1]), "+v"(b[2][0]), "+v"(b[2][1]), "+v"(b[3][0]), "+v"(b[3][1]) : "i"(N) : "memory");
}
template <int NT, int NSTG, int KC_A, int KC_B>
DI void ring_gemm(f32x4 (&acc)[NT][2], __amdgpu_buffer_rsrc_t ra, __amdgpu_buffer_rsrc_t rb, unsigned lds0, const char* lds, int wid, int lane STAMP_ARG) {
  constexpr int SB = 4 * NT * 1024, PA = NT, FR = 4 * NT, PD = 6;
  bf16x8 B[3][4][2];
  const unsigned voff = lane * 16;
  auto issueA1 = [&](auto sc, auto ic) __attribute__((always_inline)) {
    constexpr int s = sc.value, i = ic.value;
    const int p = i * 4 + wid, ks = p / NT, nt = p - ks * NT;
    dma16(lds0 + (s % 4) * SB + p * 1024, voff, ra, nt * KC_A * 256 + (s * 4 + ks) * 1024);
  };
  auto issueB1 = [&](auto sc, auto ic) __attribute__((always_inline)) {
    constexpr int s = sc.value, i = ic.value, ks = i >> 1, q = i & 1;
    bload16<0>(B[s % 3][ks][q], voff, rb, q * KC_B * 256 + (s * 4 + ks) * 1024);
  };
  auto issueA = [&](auto sc) __attribute__((always_inline)) { static_for<PA>(LAMBDA(ic) { issueA1(sc, ic); }); };
  auto issueB = [&](auto sc) __attribute__((always_inline)) { static_for<8>(LAMBDA(ic) { issueB1(sc, ic); }); };
  static_assert(NSTG >= 4, "");
  issueA(std::integral_constant<int, 0>{}); issueB(std::integral_constant<int, 0>{}); issueA(std::integral_constant<int, 1>{});
  const LAS bf16x8* pl = (const LAS bf16x8*)lds + lane;
  bf16x8 A[PD + 1];
  auto rd = [&](auto mc) __attribute__((always_inline)) { constexpr int M = mc.value, s = M / FR, m = M % FR; A[M % (PD + 1)] = pl[((s % 4) * FR + m) * 64]; };
  static_for<NSTG>(LAMBDA(sc) {
    constexpr int s = sc.value;
    constexpr int N = s == 0 ? 0 : ((s + 1 < NSTG ? 8 : 0) + (s + 2 < NSTG ? PA : 0));
    wait_bar8<N>(B[s % 3]);
    ST(2 + s);
    SBAR();
    if constexpr (s == 0) { static_for<PD>(LAMBDA(jc) { rd(jc); }); SBAR(); }
    constexpr int n0 = s == 0 ? 8 + PA : 0;
    constexpr int nB = (s + 2 < NSTG) ? 8 : 0, nA = (s + 3 < NSTG) ? PA : 0, NI = n0 + nB + nA;
    static_for<FR>(LAMBDA(mc) {
      constexpr int m = mc.value, M = s * FR + m, ks = m / NT, nt = m % NT;
      if constexpr (M + PD < NSTG * FR) rd(std::integral_constant<int, M + PD>{});
      constexpr int i0 = (m * NI + FR - 1) / FR, i1 = ((m + 1) * NI + FR - 1) / FR;
      static_for<i1 - i0>(LAMBDA(uc) {
        constexpr int i = i0 + uc.value;
        if constexpr (i < n0) { if constexpr (i < 8) issueB1(std::integral_constant<int, 1>{}, std::integral_constant<int, i>{}); else issueA1(std::integral_constant<int, 2>{}, std::integral_constant<int, i - 8>{}); }
        else if constexpr (i < n0 + nB) issueB1(std::integral_constant<int, s + 2>{}, std::integral_constant<int, i - n0>{});
        else issueA1(std::integral_constant<int, s + 3>{}, std::integral_constant<int, i - n0 - nB>{});
      });
      acc[nt][0] = mfma16(A[M % (PD + 1)], B[s % 3][ks][0], acc[nt][0]); acc[nt][1] = mfma16(A[M % (PD + 1)], B[s % 3][ks][1], acc[nt][1]);
      SBAR();
    });
  });
}

__global__ __launch_bounds__(256, 1) void k_oproj(const u16* __restrict__ obuf, const u16* __restrict__ wot, u16* __restrict__ part STAMP_ARG) {
  ST(0);
  extern __shared__ __attribute__((aligned(16))) char lds[];
  const int tid = threadIdx.x, lane = tid & 63, wid = __builtin_amdgcn_readfirstlane(tid >> 6);
  const int xcd = blockIdx.x & 7, idx = blockIdx.x >> 3;
  const int kq = xcd & 3, rt = (xcd >> 2) * 8 + (idx >> 2), j = idx & 3;
  const __amdgpu_buffer_rsrc_t ra = __builtin_amdgcn_make_buffer_rsrc((void*)(wot + ((size_t)(10 * j) * 512 + kq * 128) * 128), 0, 10 * 512 * 256, 0x00020000);
  const __amdgpu_buffer_rsrc_t rb = __builtin_amdgcn_make_buffer_rsrc((void*)(obuf + ((size_t)(8 * rt + 2 * wid) * 512 + kq * 128) * 128), 0, 2 * 512 * 256, 0x00020000);
  const __amdgpu_buffer_rsrc_t rp = __builtin_amdgcn_make_buffer_rsrc((void*)(part + (size_t)kq * SQ * DM), 0, SQ * DM * 2, 0x00020000);
  f32x4 acc[10][2] = {};
  ring_gemm<10, 8, 512, 512>(acc, ra, rb, (unsigned)(uintptr_t)lds, lds, wid, lane STAMP_FWD);
  ST(1);
#pragma unroll
  for (int nt = 0; nt < 10; ++nt)
#pragma unroll
    for (int q = 0; q < 2; ++q) {
      const u32x2 w = {cvtpk(acc[nt][q][0], acc[nt][q][1]), cvtpk(acc[nt][q][2], acc[nt][q][3])};
      st8(rp, (((8 * rt + 2 * wid + q) * 80 + j * 20 + nt * 2 + (lane >> 5)) * 16 + (lane & 15)) * 16 + ((lane >> 4) & 1) * 8, w);
    }
  ST(31);
}

__global__ __launch_bounds__(640) void k_red(const u16* __restrict__ part, const u16* __restrict__ x, const u16* __restrict__ mod, u16* __restrict__ x1, u16* __restrict__ h2 STAMP_ARG) {
  ST(0);
  __shared__ float red[2][10][8];
  const int tid = threadIdx.x, r = tid & 7, c = tid >> 3, row = blockIdx.x * 8 + r, w = tid >> 6;
  const size_t blk = (((size_t)(row >> 4) * 80 + c) * 16 + (row & 15)) * 8;
  u32x4 p[4];
#pragma unroll
  for (int k = 0; k < 4; ++k) p[k] = *(const u32x4*)(part + (size_t)k * SQ * DM + blk);
  const u32x4 xv = *(const u32x4*)(x + (size_t)row * DM + c * 8);
  const u32x4 gv = *(const u32x4*)(mod + 2 * 640 + c * 8), sbv = *(const u32x4*)(mod + 3 * 640 + c * 8), cbv = *(const u32x4*)(mod + 4 * 640 + c * 8);
  float v[8]; u32x4 o; float s = 0;
#pragma unroll
  for (int e = 0; e < 4; ++e) {
    const float a = (bf_lo(p[0][e]) + bf_lo(p[1][e])) + (bf_lo(p[2][e]) + bf_lo(p[3][e])), b = (bf_hi(p[0][e]) + bf_hi(p[1][e])) + (bf_hi(p[2][e]) + bf_hi(p[3][e]));
    o[e] = cvtpk(bf_lo(xv[e]) + rbf(bf_lo(gv[e]) * rbf(a)), bf_hi(xv[e]) + rbf(bf_hi(gv[e]) * rbf(b)));
    v[2 * e] = bf_lo(o[e]); v[2 * e + 1] = bf_hi(o[e]); s += v[2 * e] + v[2 * e + 1];
  }
  *(u32x4*)(x1 + blk) = o;
  s += __shfl_xor(s, 8); s += __shfl_xor(s, 16); s += __shfl_xor(s, 32);
  if ((tid & 63) < 8) red[0][w][r] = s;
  __syncthreads();
  float tot = 0;
#pragma unroll
  for (int i = 0; i < 10; ++i) tot += red[0][i][r];
  const float mean = tot * (1.f / DM); float q = 0;
#pragma unroll
  for (int e = 0; e < 8; ++e) { v[e] -= mean; q += v[e] * v[e]; }
  q += __shfl_xor(q, 8); q += __shfl_xor(q, 16); q += __shfl_xor(q, 32);
  if ((tid & 63) < 8) red[1][w][r] = q;
  __syncthreads();
  float qt = 0;
#pragma unroll
  for (int i = 0; i < 10; ++i) qt += red[1][i][r];
  const float rstd = rsqrtf(qt * (1.f / DM) + 1e-5f);
  u32x4 hv;
#pragma unroll
  for (int e = 0; e < 4; ++e) hv[e] = cvtpk(v[2 * e] * rstd * rbf(1.f + bf_lo(cbv[e])) + bf_lo(sbv[e]), v[2 * e + 1] * rstd * rbf(1.f + bf_hi(cbv[e])) + bf_hi(sbv[e]));
  *(u32x4*)(h2 + blk) = hv;
  ST(31);
}

__global__ __launch_bounds__(256, 1) void k_ffn1(const u16* __restrict__ h2, const u16* __restrict__ w1t, const u16* __restrict__ b1, u16* __restrict__ hid STAMP_ARG) {
  ST(0);
  extern __shared__ __attribute__((aligned(16))) char lds[];
  const int tid = threadIdx.x, lane = tid & 63, wid = __builtin_amdgcn_readfirstlane(tid >> 6);
  const int xcd = blockIdx.x & 7, idx = blockIdx.x >> 3, rt = xcd * 2 + (idx >> 4), j = idx & 15;
  const int nft = j < 15 ? 7 : 3;
  const __amdgpu_buffer_rsrc_t ra = __builtin_amdgcn_make_buffer_rsrc((void*)(w1t + (size_t)(7 * j) * 80 * 128), 0, nft * 80 * 256, 0x00020000);
  const __amdgpu_buffer_rsrc_t rb = __builtin_amdgcn_make_buffer_rsrc((void*)(h2 + (size_t)(8 * rt + 2 * wid) * 80 * 128), 0, 2 * 80 * 256, 0x00020000);
  const __amdgpu_buffer_rsrc_t rh = __builtin_amdgcn_make_buffer_rsrc((void*)hid, 0, SQ * FF * 2, 0x00020000);
  u32x2 bv[7];
#pragma unroll
  for (int nt = 0; nt < 7; ++nt) bv[nt] = *(const u32x2*)(b1 + (nt < nft ? (7 * j + nt) * 16 + 4 * (lane >> 4) : 0));
  f32x4 acc[7][2] = {};
  ring_gemm<7, 5, 80, 80>(acc, ra, rb, (unsigned)(uintptr_t)lds, lds, wid, lane STAMP_FWD);
  ST(1);
#pragma unroll
  for (int nt = 0; nt < 7; ++nt) {
    if (nt < nft) {
#pragma unroll
      for (int q = 0; q < 2; ++q) {
        const float b0 = bf_lo(bv[nt][0]), b1_ = bf_hi(bv[nt][0]), b2_ = bf_lo(bv[nt][1]), b3 = bf_hi(bv[nt][1]);
        float v0 = __builtin_fmaxf(rbf(acc[nt][q][0]) + b0, 0.f), v1 = __builtin_fmaxf(rbf(acc[nt][q][1]) + b1_, 0.f), v2 = __builtin_fmaxf(rbf(acc[nt][q][2]) + b2_, 0.f), v3 = __builtin_fmaxf(rbf(acc[nt][q][3]) + b3, 0.f);
        v0 = rbf(v0); v1 = rbf(v1); v2 = rbf(v2); v3 = rbf(v3);
        const u32x2 w = {cvtpk(v0 * v0, v1 * v1), cvtpk(v2 * v2, v3 * v3)};
        st8(rh, (((8 * rt + 2 * wid + q) * 216 + (7 * j + nt) * 2 + (lane >> 5)) * 16 + (lane & 15)) * 16 + ((lane >> 4) & 1) * 8, w);
      }
    }
  }
  ST(31);
}

__global__ __launch_bounds__(C_NW * 64, C_NW / 4) void k_ffn2(const u16* __restrict__ hid, const u16* __restrict__ w2t, const u16* __restrict__ x1, const u16* __restrict__ mod,
                                                  const u16* __restrict__ b2, u16* __restrict__ out STAMP_ARG) {
  ST(0);
  extern __shared__ __attribute__((aligned(16))) char lds[];
  const int tid = threadIdx.x, lane = tid & 63, wid = __builtin_amdgcn_readfirstlane(tid >> 6);
  const int xcd = blockIdx.x & 7, idx = blockIdx.x >> 3, rt = xcd * 4 + (idx >> 3), j = idx & 7;
  const __amdgpu_buffer_rsrc_t ra = __builtin_amdgcn_make_buffer_rsrc((void*)(w2t + (size_t)(5 * j) * 216 * 128), 0, 5 * 216 * 256, 0x00020000);
  const __amdgpu_buffer_rsrc_t rb = __builtin_amdgcn_make_buffer_rsrc((void*)(hid + (size_t)(4 * rt) * 216 * 128), 0, 4 * 216 * 256, 0x00020000);
  u32x4 xv[C_NIT], gv[C_NIT], bv[C_NIT];
#pragma unroll
  for (int it = 0; it < C_NIT; ++it) {
    const int item = tid + it * C_NW * 64, q = item & 63, c = item < 640 ? item >> 6 : 0, row = rt * 64 + q;
    xv[it] = *(const u32x4*)(x1 + (((size_t)(row >> 4) * 80 + j * 10 + c) * 16 + (row & 15)) * 8);
    gv[it] = *(const u32x4*)(mod + 5 * 640 + j * 80 + c * 8); bv[it] = *(const u32x4*)(b2 + j * 80 + c * 8);
  }
  f32x4 acc[5][4] = {};
  constexpr int NSTMAX = (54 + C_NW - 1) / C_NW, NBIG = 54 - (NSTMAX - 1) * C_NW;
  const int ks0 = wid < NBIG ? NSTMAX * wid : NBIG * NSTMAX + (NSTMAX - 1) * (wid - NBIG), nst = wid < NBIG ? NSTMAX : NSTMAX - 1;
  gemm5x4<NSTMAX, C_DP, 216, 216>(acc, ra, rb, ks0, nst, lane);
  ST(1);
  reduce_epilogue<C_NW>(acc, lds, tid, [&](auto itc, int q, int c, f32x4 lo, f32x4 hi) __attribute__((always_inline)) {
    constexpr int it = itc.value;
    const int row = rt * 64 + q, n0 = j * 80 + c * 8;
    float v[8] = {lo[0], lo[1], lo[2], lo[3], hi[0], hi[1], hi[2], hi[3]};
    u32x4 o;
#pragma unroll
    for (int e = 0; e < 4; ++e) {
      const float a = bf_lo(xv[it][e]) + rbf(bf_lo(gv[it][e]) * rbf(v[2 * e] + bf_lo(bv[it][e]))), b = bf_hi(xv[it][e]) + rbf(bf_hi(gv[it][e]) * rbf(v[2 * e + 1] + bf_hi(bv[it][e])));
      o[e] = cvtpk(a, b);
    }
    *(u32x4*)(out + (size_t)row * DM + n0) = o;
  });
  ST(31);
}

extern "C" long long* dbg_offsets() { static long long o[5] = {(long long)WS_MOD, (long long)WS_H, (long long)WS_O, (long long)WS_X1, (long long)WS_HID}; return o; }

extern "C" void kernel_launch(void* const* d_in, const int* in_sizes, int n_in, void* d_out, int out_size, void* d_ws, size_t ws_size, hipStream_t stream) {
  const u16 *x = (const u16*)d_in[0], *kc = (const u16*)d_in[1], *vc = (const u16*)d_in[2], *wq = (const u16*)d_in[3], *wo = (const u16*)d_in[4];
  const u16 *w1 = (const u16*)d_in[6], *b1 = (const u16*)d_in[7], *w2 = (const u16*)d_in[8], *b2 = (const u16*)d_in[9], *c = (const u16*)d_in[10], *wmod = (const u16*)d_in[11];
  char* ws = (char*)d_ws;
  u16 *mod = (u16*)(ws + WS_MOD), *h = (u16*)(ws + WS_H), *o = (u16*)(ws + WS_O), *wot = (u16*)(ws + WS_WOT), *w1t = (u16*)(ws + WS_W1T), *w2t = (u16*)(ws + WS_W2T);
  u16 *x1 = (u16*)(ws + WS_X1), *hid = (u16*)(ws + WS_HID);
  u16 *h2 = (u16*)(ws + WS_H2), *part = (u16*)(ws + WS_P);
  (void)hipFuncSetAttribute((const void*)k_attn, hipFuncAttributeMaxDynamicSharedMemorySize, NS * SLOT);
  (void)hipFuncSetAttribute((const void*)k_oproj, hipFuncAttributeMaxDynamicSharedMemorySize, 163840);
  (void)hipFuncSetAttribute((const void*)k_ffn1, hipFuncAttributeMaxDynamicSharedMemorySize, 114688);
  (void)hipFuncSetAttribute((const void*)k_ffn2, hipFuncAttributeMaxDynamicSharedMemorySize, C_LDS);
  k_mod<<<60, 512, 0, stream>>>(c, wmod, mod STAMP_PASS(0));
  k_retile<<<(4096 * 640 / 8 + 255) / 256, 256, 0, stream>>>(wo, wot, 4096, 640, nullptr);
  k_retile<<<(640 * 1728 / 8 + 255) / 256, 256, 0, stream>>>(w1, w1t, 640, 1728, nullptr);
  k_retile<<<(640 * 1728 / 8 + 255) / 256, 256, 0, stream>>>(w2, w2t, 1728, 640, nullptr);
  k_ln1<<<128, 256, 0, stream>>>(x, mod, h STAMP_PASS(1));
  k_attn<<<256, 512, NS * SLOT, stream>>>(h, wq, kc, vc, o STAMP_PASS(2));
  k_oproj<<<256, 256, 163840, stream>>>(o, wot, part STAMP_PASS(3));
  k_red<<<256, 640, 0, stream>>>(part, x, mod, x1, h2 STAMP_PASS(4));
  k_ffn1<<<256, 256, 114688, stream>>>(h2, w1t, b1, hid STAMP_PASS(5));
  k_ffn2<<<256, C_NW * 64, C_LDS, stream>>>(hid, w2t, x1, mod, b2, (u16*)d_out STAMP_PASS(6));
}
```

```cpp
#include <hip/hip_runtime.h>
#include <stdint.h>
#include <utility>

typedef unsigned short u16;
typedef short bf16x8 __attribute__((ext_vector_type(8)));
typedef short s16x4 __attribute__((ext_vector_type(4)));
typedef float f32x16 __attribute__((ext_vector_type(16)));
typedef float f32x4 __attribute__((ext_vector_type(4)));
typedef float f32x2 __attribute__((ext_vector_type(2)));
typedef __bf16 bf16x2 __attribute__((ext_vector_type(2)));
typedef unsigned u32x4 __attribute__((ext_vector_type(4)));
typedef unsigned u32x2 __attribute__((ext_vector_type(2)));
#define DI __device__ __forceinline__
#define GAS __attribute__((address_space(1)))
#define LAS __attribute__((address_space(3)))
#define SBAR() __builtin_amdgcn_sched_barrier(0)
#define PIN(x) asm volatile("" : "+v"(x))

constexpr int SQ = 2048, DM = 640, NH = 32, DH = 128, SK = 2048, WIN = 512, FF = 1728;
constexpr size_t WS_MOD = 0;
constexpr size_t WS_CNT = 8192;
constexpr size_t WS_STAT = 32768;
constexpr size_t WS_H = WS_STAT + 2048 * 8 * 2 * 4;
constexpr size_t WS_O = WS_H + (size_t)SQ * DM * 2;
constexpr size_t WS_WOT = WS_O + (size_t)SQ * 4096 * 2;
constexpr size_t WS_W1T = WS_WOT + (size_t)4096 * DM * 2;
constexpr size_t WS_W2T = WS_W1T + (size_t)DM * FF * 2;
constexpr size_t WS_X1 = WS_W2T + (size_t)DM * FF * 2 + 65536;
constexpr size_t WS_HID = WS_X1 + (size_t)SQ * DM * 2;
constexpr size_t WS_H2 = WS_HID + (size_t)SQ * FF * 2 + 65536;
constexpr size_t WS_P = WS_H2 + (size_t)SQ * DM * 2;
constexpr size_t WS_END = WS_P + (size_t)4 * SQ * DM * 2;

#define STAMP_ARG
#define STAMP_FWD
#define STAMP_PASS(k)
#define ST(i)
#define STC(i)
template <int... I, class F> DI void static_for_impl(std::integer_sequence<int, I...>, F&& f) { (f(std::integral_constant<int, I>{}), ...); }
template <int N, class F> DI void static_for(F&& f) { static_for_impl(std::make_integer_sequence<int, N>{}, f); }
#define LAMBDA(ic) [&](auto ic) __attribute__((always_inline))

DI float bf_lo(unsigned w) { return __uint_as_float(w << 16); }
DI float bf_hi(unsigned w) { return __uint_as_float(w & 0xffff0000u); }
DI float bf2f(u16 v) { return __uint_as_float((unsigned)v << 16); }
DI unsigned cvtpk(float lo, float hi) { f32x2 v = {lo, hi}; bf16x2 b = __builtin_convertvector(v, bf16x2); return __builtin_bit_cast(unsigned, b); }
DI u16 f2bf(float x) { return (u16)(cvtpk(x, 0.f) & 0xffffu); }
DI float rbf(float x) { return bf_lo(cvtpk(x, 0.f)); }
DI f32x16 mfma32(bf16x8 a, bf16x8 b, f32x16 c) { return __builtin_amdgcn_mfma_f32_32x32x16_bf16(a, b, c, 0, 0, 0); }
DI f32x4 mfma16(bf16x8 a, bf16x8 b, f32x4 c) { return __builtin_amdgcn_mfma_f32_16x16x32_bf16(a, b, c, 0, 0, 0); }
DI float swap_max(float m) { auto r = __builtin_amdgcn_permlane32_swap(__float_as_uint(m), __float_as_uint(m), false, false); return __builtin_fmaxf(__uint_as_float(r[0]), __uint_as_float(r[1])); }
DI float swap_add(float m) { auto r = __builtin_amdgcn_permlane32_swap(__float_as_uint(m), __float_as_uint(m), false, false); return __uint_as_float(r[0]) + __uint_as_float(r[1]); }

__global__ __launch_bounds__(512) void k_mod(const u16* __restrict__ c, const u16* __restrict__ wmod, u16* __restrict__ mod STAMP_ARG) {
  ST(0);
  __shared__ float red[8][64];
  const int t = threadIdx.x, cc = t & 7, r0 = t >> 3, col0 = blockIdx.x * 64 + cc * 8;
  u32x4 w[10]; float s[10];
#pragma unroll
  for (int i = 0; i < 10; ++i) w[i] = *(const u32x4*)(wmod + (size_t)(r0 + 64 * i) * 3840 + col0);
#pragma unroll
  for (int i = 0; i < 10; ++i) { float x = bf2f(c[r0 + 64 * i]); s[i] = rbf(x / (1.f + __expf(-x))); }
  float a[8] = {};
#pragma unroll
  for (int i = 0; i < 10; ++i)
#pragma unroll
    for (int j = 0; j < 4; ++j) { a[2 * j] += s[i] * bf_lo(w[i][j]); a[2 * j + 1] += s[i] * bf_hi(w[i][j]); }
#pragma unroll
  for (int j = 0; j < 8; ++j) { a[j] += __shfl_xor(a[j], 8); a[j] += __shfl_xor(a[j], 16); a[j] += __shfl_xor(a[j], 32); }
  if ((t & 63) < 8) {
#pragma unroll
    for (int j = 0; j < 8; ++j) red[t >> 6][cc * 8 + j] = a[j];
  }
  __syncthreads();
  if (t < 64) { float v = 0; for (int w8 = 0; w8 < 8; ++w8) v += red[w8][t]; mod[blockIdx.x * 64 + t] = f2bf(v); }
  ST(31);
}

__global__ __launch_bounds__(256) void k_ln1(const u16* __restrict__ x, const u16* __restrict__ mod, u16* __restrict__ h STAMP_ARG) {
  ST(0);
  const int t = threadIdx.x, i = t & 15, row = blockIdx.x * 16 + (t >> 4);
  u32x4 v[5], sa[5], ca[5];
#pragma unroll
  for (int j = 0; j < 5; ++j) v[j] = *(const u32x4*)(x + (size_t)row * DM + (i + 16 * j) * 8);
#pragma unroll
  for (int j = 0; j < 5; ++j) { sa[j] = *(const u32x4*)(mod + (i + 16 * j) * 8); ca[j] = *(const u32x4*)(mod + 640 + (i + 16 * j) * 8); }
  float f[40]; float s = 0;
#pragma unroll
  for (int j = 0; j < 5; ++j)
#pragma unroll
    for (int e = 0; e < 4; ++e) { f[j * 8 + 2 * e] = bf_lo(v[j][e]); f[j * 8 + 2 * e + 1] = bf_hi(v[j][e]); s += f[j * 8 + 2 * e] + f[j * 8 + 2 * e + 1]; }
#pragma unroll
  for (int m = 1; m < 16; m <<= 1) s += __shfl_xor(s, m);
  const float mean = s * (1.f / DM); float q = 0;
#pragma unroll
  for (int e = 0; e < 40; ++e) { const float d = f[e] - mean; q += d * d; }
#pragma unroll
  for (int m = 1; m < 16; m <<= 1) q += __shfl_xor(q, m);
  const float mb = rbf(mean), rsd = 1.f / rbf(sqrtf(rbf(rbf(q * (1.f / DM)) + 1e-5f)));
#pragma unroll
  for (int e = 0; e < 40; ++e) f[e] = rbf(rbf(f[e] - mb) * rsd);
  const int qb = row >> 5, r32 = row & 31;
#pragma unroll
  for (int j = 0; j < 5; ++j) {
    u32x4 o;
#pragma unroll
    for (int e = 0; e < 4; ++e) {
      float lo = rbf(f[j * 8 + 2 * e] * rbf(1.f + bf_lo(ca[j][e]))) + bf_lo(sa[j][e]);
      float hi = rbf(f[j * 8 + 2 * e + 1] * rbf(1.f + bf_hi(ca[j][e]))) + bf_hi(sa[j][e]);
      o[e] = cvtpk(lo, hi);
    }
    *(u32x4*)(h + (((size_t)qb * 80 + (i + 16 * j)) * 32 + r32) * 8) = o;
  }
  ST(31);
}

#define A_NS 8
#define A_PD 5
#define O_AUX 16
constexpr int NS = A_NS, SLOT = 16384;
constexpr float QSCALE = 0.088388347648318440f * 1.4426950408889634f;
constexpr float PBIAS = 96.f;
constexpr float PTHR = 30.f;
constexpr int tile_K(int t) { return t == 0 ? 10 : 9 + 2 * t; }
constexpr int tile_V(int t) { return t == 7 ? 25 : 12 + 2 * t; }
constexpr int slot_of(int idx) { return (idx % NS) * SLOT; }
constexpr int lastuse(int idx) { return idx < 10 ? idx : (idx == 10 ? 10 : (idx == 25 ? 18 : ((idx & 1) ? 10 + (idx - 9) / 2 : 11 + (idx - 12) / 2))); }
constexpr int need_bar(int idx) {
  if (idx < 10) return idx == 0 ? 0 : idx - 1;
  if (idx == 10) return 9;
  if (idx == 25) return 17;
  return (idx & 1) ? 10 + (idx - 9) / 2 - 1 : 11 + (idx - 12) / 2;
}
constexpr int rt_step(int u) { return u == 0 ? 11 : (u == 1 ? 12 : 16); }
constexpr int rt_slot(int u) { return u == 0 ? NS * SLOT : (u == 1 ? (NS + 1) * SLOT : slot_of(tile_K(5))); }
struct Sched { int npro; int pro[16]; int nops[19]; int op[19][12]; int wait[19]; };
constexpr Sched make_sched() {
  Sched p{}; int pos = 0; int tile_end[26] = {}; int h_end[10] = {}; int next_tile = 3;
  for (int i = 0; i < 3; ++i) {
    p.pro[p.npro++] = 2 * i; p.pro[p.npro++] = 2 * i + 1; pos += 2; tile_end[i] = pos;
    if (i < 2) { for (int k = 0; k < 4; ++k) p.pro[p.npro++] = 100 + 4 * i + k; pos += 4; h_end[i] = pos; }
  }
  for (int g = 0; g < 19; ++g) {
    int need = 0;
    for (int idx = 0; idx < next_tile; ++idx) if (need_bar(idx) <= g && tile_end[idx] > need) need = tile_end[idx];
    if (g < 10 && h_end[g] > need) need = h_end[g];
    p.wait[g] = pos - need;
    int n = 0;
    if (g == 0) { for (int k = 0; k < 4; ++k) p.op[g][n++] = 100 + 4 * 2 + k; pos += 4; h_end[2] = pos; }
    const int maxt = g < 7 ? 1 : 2;
    for (int c = 0; c < maxt && next_tile < 26; ++c) {
      if (next_tile >= NS && lastuse(next_tile - NS) >= g) break;
      p.op[g][n++] = 2 * next_tile; p.op[g][n++] = 2 * next_tile + 1; pos += 2; tile_end[next_tile] = pos; ++next_tile;
    }
    if (g + 3 < 10) { for (int k = 0; k < 4; ++k) p.op[g][n++] = 100 + 4 * (g + 3) + k; pos += 4; h_end[g + 3] = pos; }
    for (int u = 0; u < 3; ++u) if (g == rt_step(u)) { p.op[g][n++] = 200 + 2 * u; p.op[g][n++] = 201 + 2 * u; pos += 2; }
    p.nops[g] = n;
  }
  p.wait[18] = 0;
  for (int idx = 0; idx < 26; ++idx) if (tile_end[idx] == 0) p.wait[0] = -1;
  return p;
}
constexpr Sched SCH = make_sched();
static_assert(SCH.wait[0] >= 0, "schedule incomplete");
constexpr bool sched_ok() {
  int step_of[26] = {}; for (int i = 0; i < 26; ++i) step_of[i] = -1;
  for (int g = 0; g < 19; ++g) for (int j = 0; j < SCH.nops[g]; ++j) if (SCH.op[g][j] < 100) step_of[SCH.op[g][j] >> 1] = g;
  for (int i = 3; i < 26; ++i) if (step_of[i] >= need_bar(i)) return false;
  return true;
}
static_assert(sched_ok(), "a tile is issued too late");

DI void dma16(unsigned ldsaddr, unsigned voff, __amdgpu_buffer_rsrc_t srd, unsigned soff) {
  asm volatile("s_mov_b32 m0, %0\n\ts_nop 0\n\tbuffer_load_dwordx4 %1, %2, %3 offen lds" ::"s"(ldsaddr), "v"(voff), "s"(srd), "s"(soff) : "memory");
}
template <int OFF> DI void bload16(bf16x8& dst, unsigned voff, __amdgpu_buffer_rsrc_t srd, unsigned soff) {
  asm volatile("buffer_load_dwordx4 %0, %1, %2, %3 offen offset:%4" : "=v"(dst) : "v"(voff), "s"(srd), "s"(soff), "i"(OFF) : "memory");
}
typedef LAS const char* lds_cptr;
typedef short v4i16_t __attribute__((ext_vector_type(4)));
DI s16x4 vtr(lds_cptr p) { return __builtin_bit_cast(s16x4, __builtin_amdgcn_ds_read_tr16_b64_v4i16((LAS v4i16_t*)p)); }
DI bf16x8 trfrag(lds_cptr p0, lds_cptr p1) { const s16x4 lo = vtr(p0), hi = vtr(p1); return __builtin_shufflevector(lo, hi, 0, 1, 2, 3, 4, 5, 6, 7); }
DI bf16x8 rowfrag(lds_cptr p) { return *(LAS const bf16x8*)p; }
DI float max3(float a, float b, float c) { return __builtin_fmaxf(__builtin_fmaxf(a, b), c); }

template <int N> DI void wait_bar4(bf16x8 (&b)[4]) {
  asm volatile("s_waitcnt vmcnt(%4)\n\ts_barrier" : "+v"(b[0]), "+v"(b[1]), "+v"(b[2]), "+v"(b[3]) : "i"(N) : "memory");
}
template <int N> DI void wait_bar() { asm volatile("s_waitcnt vmcnt(%0)\n\ts_barrier" ::"i"(N) : "memory"); }
template <int V> using IC = std::integral_constant<int, V>;

__global__ __launch_bounds__(512, 2) void k_attn(const u16* __restrict__ hbuf, const u16* __restrict__ wq, const u16* __restrict__ kc, const u16* __restrict__ vc, u16* __restrict__ obuf,
    const u16* __restrict__ wo, const u16* __restrict__ w1, const u16* __restrict__ w2, u16* __restrict__ wot, u16* __restrict__ w1t, u16* __restrict__ w2t, unsigned* __restrict__ cnt STAMP_ARG) {
  ST(0);
  if (blockIdx.x == 0 && threadIdx.x < 48) cnt[threadIdx.x * 64] = 0u;
  extern __shared__ __attribute__((aligned(16))) char lds[];
  const int tid = threadIdx.x, lane = tid & 63, r32 = lane & 31, hi = lane >> 5;
  const int wid = __builtin_amdgcn_readfirstlane(tid >> 6);
  const int xcd = blockIdx.x & 7, idx = blockIdx.x >> 3;
  const int head = (xcd & 3) * 8 + (idx & 7), qt = (xcd >> 2) * 4 + (idx >> 3);
  const unsigned lds0 = (unsigned)(uintptr_t)lds;
  const __amdgpu_buffer_rsrc_t srdW = __builtin_amdgcn_make_buffer_rsrc((void*)(wq + head * DH), 0, (DM - 1) * 8192 + 256, 0x00020000);
  const __amdgpu_buffer_rsrc_t srdK = __builtin_amdgcn_make_buffer_rsrc((void*)(kc + ((size_t)head * SK + (SK - WIN)) * DH), 0, WIN * 256, 0x00020000);
  const __amdgpu_buffer_rsrc_t srdV = __builtin_amdgcn_make_buffer_rsrc((void*)(vc + ((size_t)head * SK + (SK - WIN)) * DH), 0, WIN * 256, 0x00020000);
  const __amdgpu_buffer_rsrc_t srdH = __builtin_amdgcn_make_buffer_rsrc((void*)(hbuf + (size_t)(qt * 8 + wid) * 80 * 256), 0, 80 * 512, 0x00020000);
  const int dr = (lane >> 2) & 7, dx = lane & 3, dc = lane >> 5, dz = (2 * wid + (dr >> 2)) & 3;
  const unsigned voffW = (unsigned)((8 * wid + dr) * 8192 + 64 * dc + 16 * (dx ^ dz));
  const unsigned voffKV = (unsigned)((8 * wid + dr) * 256 + 64 * dc + 16 * (dx ^ dz));
  const unsigned ldsw = lds0 + 2048 * wid;
  const unsigned voffH = lane * 16;
  bf16x8 hf[10][4];
  struct Unit { const u16* src; u16* dst; int pitch, rows, kt, nt, nsub, kc; };
  auto unit_of = [&](int u) __attribute__((always_inline)) {
    Unit r; const int id = blockIdx.x + 256 * u;
    if (id < 320) { r.src = wo; r.dst = wot; r.pitch = 1280; r.rows = 4096; r.kt = id / 5; r.nt = id - r.kt * 5; r.nsub = 40; r.kc = 512; }
    else if (id < 455) { const int v = id - 320; r.src = w2; r.dst = w2t; r.pitch = 1280; r.rows = 1728; r.kt = v / 5; r.nt = v - r.kt * 5; r.nsub = 40; r.kc = 216; }
    else { const int v = id < 595 ? id - 455 : 0; r.src = w1; r.dst = w1t; r.pitch = 3456; r.rows = id < 595 ? 640 : 0; r.kt = v / 14; r.nt = v - r.kt * 14; r.nsub = id < 595 ? 108 : 0; r.kc = 80; }
    return r;
  };
  auto vmem_op = [&](auto oc) __attribute__((always_inline)) {
    constexpr int op = oc.value;
    if constexpr (op >= 200) {
      constexpr int u = (op - 200) >> 1, half = op & 1;
      const Unit un = unit_of(u);
      const __amdgpu_buffer_rsrc_t srd = __builtin_amdgcn_make_buffer_rsrc((void*)un.src, 0, un.rows * un.pitch, 0x00020000);
      dma16(ldsw + rt_slot(u) + 1024 * half, (unsigned)((8 * wid + dr) * un.pitch + 64 * dc + 16 * (dx ^ dz)), srd, un.kt * 64 * un.pitch + un.nt * 256 + 128 * half);
    } else if constexpr (op >= 100) { constexpr int i = (op - 100) >> 2, k = (op - 100) & 3; bload16<1024 * k>(hf[i][k], voffH, srdH, i * 4096); }
    else {
      constexpr int i = op >> 1, half = op & 1; constexpr unsigned so = slot_of(i) + 1024 * half;
      if constexpr (i < 10) dma16(ldsw + so, voffW, srdW, i * 64 * 8192 + 128 * half);
      else {
        constexpr bool isK = (i == 10) || (i != 25 && (i & 1)); constexpr int t = i == 10 ? 0 : (i == 25 ? 7 : (isK ? (i - 9) / 2 : (i - 12) / 2));
        dma16(ldsw + so, voffKV, isK ? srdK : srdV, t * 16384 + 128 * half);
      }
    }
  };
  static_for<SCH.npro>(LAMBDA(jc) { vmem_op(IC<SCH.pro[jc.value]>{}); });
  auto vmem_gap = [&](auto gc, auto jc, auto nc) __attribute__((always_inline)) {
    constexpr int g = gc.value, j = jc.value, n = nc.value, no = SCH.nops[g];
    constexpr int i0 = (j * no + n - 1) / n, i1 = ((j + 1) * no + n - 1) / n;
    static_for<i1 - i0>(LAMBDA(uc) { vmem_op(IC<SCH.op[g][i0 + uc.value]>{}); });
  };

  const int blk = (lane >> 4) & 1, q4 = (lane & 15) >> 2, p4 = lane & 3;
  constexpr int NR = (NS * SLOT + 65535) / 65536;
  const lds_cptr L = (lds_cptr)lds;
  lds_cptr wb[NR][2], vb[NR][2], kb_[NR][2];
#pragma unroll
  for (int r = 0; r < NR; ++r)
#pragma unroll
    for (int t = 0; t < 2; ++t) {
      wb[r][t] = L + 65536 * r + 2048 * hi + 64 * (4 * t + q4) + 16 * ((2 * blk + (p4 & 1)) ^ (2 * hi + t)) + 8 * (p4 >> 1);
      vb[r][t] = L + 65536 * r + 64 * (4 * hi + q4) + 16 * ((2 * blk + (p4 & 1)) ^ (2 * t + hi)) + 8 * (p4 >> 1) + 2048 * t;
      kb_[r][t] = L + 65536 * r + 2048 * (r32 >> 3) + 64 * (r32 & 7) + 16 * ((2 * t + hi) ^ ((r32 >> 2) & 3));
      PIN(wb[r][t]);
    }
  constexpr int PD = A_PD, NG = 416;
  bf16x8 fr[PD + 1];
  auto ldfrag_g = [&](auto Gc) __attribute__((always_inline)) {
    constexpr int G = Gc.value;
    if constexpr (G < NG) {
      if constexpr (G < 160) {
        constexpr int i = G >> 4, ks = (G >> 2) & 3, db = G & 3, o = slot_of(i) + 4096 * ks + 512 * db;
        fr[G % (PD + 1)] = trfrag(wb[o >> 16][0] + (o & 65535), wb[o >> 16][1] + (o & 65535));
      } else {
        constexpr int u = G - 176, t = G < 176 ? 0 : (G >= 400 ? 8 : 1 + u / 32), g = G < 176 ? G - 160 : (G >= 400 ? G - 400 + 16 : u % 32);
        if constexpr (g < 16) {
          constexpr int kb = g >> 3, s = g & 7, o = slot_of(tile_K(t)) + 8192 * kb + 512 * (s >> 1);
          fr[G % (PD + 1)] = rowfrag(kb_[o >> 16][s & 1] + (o & 65535));
        } else {
          constexpr int gg = g - 16, ks = gg >> 2, db = gg & 3, o = slot_of(tile_V(t - 1)) + 4096 * ks + 512 * db;
          fr[G % (PD + 1)] = trfrag(vb[o >> 16][0] + (o & 65535), vb[o >> 16][1] + (o & 65535));
        }
      }
    }
  };
  auto barrier = [&](auto gc) __attribute__((always_inline)) {
    constexpr int g = gc.value;
    if constexpr (g < 10) wait_bar4<SCH.wait[g]>(hf[g]); else wait_bar<SCH.wait[g]>();
    ST(1 + g);
    if constexpr (g == 0) STC(28);
    if constexpr (g == 10) STC(29);
    if constexpr (g == 18) STC(30);
    SBAR();
  };

  f32x16 acc[4] = {};
  static_for<10>(LAMBDA(ic) {
    constexpr int i = ic.value;
    barrier(ic);
    if constexpr (i == 0) { static_for<PD>(LAMBDA(jc) { ldfrag_g(jc); }); SBAR(); }
    static_for<16>(LAMBDA(mc) {
      constexpr int m = mc.value, G = 16 * i + m;
      ldfrag_g(IC<G + PD>{});
      vmem_gap(ic, mc, IC<16>{});
      acc[m & 3] = mfma32(fr[G % (PD + 1)], hf[i][m >> 2], acc[m & 3]);
      SBAR();
    });
  });
  bf16x8 qf[8];
#pragma unroll
  for (int s = 0; s < 8; ++s) {
    u32x4 w;
#pragma unroll
    for (int e = 0; e < 4; ++e) w[e] = cvtpk(acc[s >> 1][8 * (s & 1) + 2 * e] * QSCALE, acc[s >> 1][8 * (s & 1) + 2 * e + 1] * QSCALE);
    qf[s] = __builtin_bit_cast(bf16x8, w);
  }
#pragma unroll
  for (int r = 0; r < NR; ++r)
#pragma unroll
    for (int t = 0; t < 2; ++t) { PIN(vb[r][t]); PIN(kb_[r][t]); }

  f32x16 o[4] = {}; f32x16 sA[2], sB[2], negm; u32x4 pw[4];
  float mref, lsum = 0.f;
  barrier(IC<10>{});
  {
    const f32x16 zero = {};
    static_for<16>(LAMBDA(gc) {
      constexpr int g = gc.value, G = 160 + g;
      ldfrag_g(IC<G + PD>{});
      vmem_gap(IC<10>{}, gc, IC<16>{});
      sA[g >> 3] = mfma32(fr[G % (PD + 1)], qf[g & 7], (g & 7) == 0 ? zero : sA[g >> 3]);
      SBAR();
    });
    float a = max3(sA[0][0], sA[0][1], sA[1][0]), b = max3(sA[0][2], sA[0][3], sA[1][1]); a = max3(a, sA[1][2], sA[1][3]);
#pragma unroll
    for (int r = 4; r < 16; r += 4) { a = max3(a, sA[0][r], sA[0][r + 1]); b = max3(b, sA[0][r + 2], sA[0][r + 3]); a = max3(a, sA[1][r], sA[1][r + 1]); b = max3(b, sA[1][r + 2], sA[1][r + 3]); }
    mref = swap_max(__builtin_fmaxf(a, b)) + PBIAS;
#pragma unroll
    for (int r = 0; r < 16; ++r) { sA[0][r] = __builtin_amdgcn_exp2f(sA[0][r] - mref); sA[1][r] = __builtin_amdgcn_exp2f(sA[1][r] - mref); negm[r] = -mref; }
    PIN(negm);
    SBAR();
  }
  auto step = [&](auto tc, f32x16(&C)[2], f32x16(&P)[2]) __attribute__((always_inline)) {
    constexpr int t = tc.value;
    constexpr int G0 = 176 + 32 * (t - 1), GB = t < 8 ? G0 + 16 : 400;
    barrier(IC<10 + t>{});
    if constexpr (t == 8) {
      const int g4 = lane >> 4;
#pragma unroll
      for (int u = 0; u < 3; ++u) {
        const Unit un = unit_of(u);
        const int ntg = un.nt * 8 + wid;
        const __amdgpu_buffer_rsrc_t srd = __builtin_amdgcn_make_buffer_rsrc((void*)un.dst, 0, un.nsub * un.kc * 256, 0x00020000);
#pragma unroll
        for (int ks = 0; ks < 2; ++ks) {
          const int chq = 2 * wid + (p4 >> 1);
          const lds_cptr p0 = L + rt_slot(u) + 2048 * (4 * ks + g4) + 512 * (chq >> 2) + 64 * q4 + 16 * ((chq & 3) ^ ((2 * g4) & 3)) + 8 * (p4 & 1);
          const lds_cptr p1 = L + rt_slot(u) + 2048 * (4 * ks + g4) + 512 * (chq >> 2) + 64 * (4 + q4) + 16 * ((chq & 3) ^ ((2 * g4 + 1) & 3)) + 8 * (p4 & 1);
          const bf16x8 f = trfrag(p0, p1);
          __builtin_amdgcn_raw_buffer_store_b128(__builtin_bit_cast(u32x4, f), srd, (ntg * un.kc + un.kt * 8 + 4 * ks) * 256 + lane * 16, 0, O_AUX);
        }
      }
      SBAR();
    }
    float sacc = 0.f;
    static_for<16>(LAMBDA(gc) {
      constexpr int g = gc.value;
      if constexpr (t < 8) {
        ldfrag_g(IC<G0 + g + PD>{});
        vmem_gap(IC<10 + t>{}, gc, IC<32>{});
        C[g >> 3] = mfma32(fr[(G0 + g) % (PD + 1)], qf[g & 7], (g & 7) == 0 ? negm : C[g >> 3]);
      }
      constexpr int ks = g >> 2, w = g & 3, e = 8 * (ks & 1) + 2 * w;
      sacc += P[ks >> 1][e]; sacc += P[ks >> 1][e + 1];
      pw[ks][w] = cvtpk(P[ks >> 1][e], P[ks >> 1][e + 1]);
      PIN(pw[ks]); PIN(sacc);
      SBAR();
    });
    lsum += sacc;
    bool resc = false; float fct = 1.f; float ma, mb;
    static_for<16>(LAMBDA(gc) {
      constexpr int g = gc.value;
      ldfrag_g(IC<GB + g + PD>{});
      if constexpr (t < 8) vmem_gap(IC<10 + t>{}, IC<16 + g>{}, IC<32>{});
      o[g & 3] = mfma32(fr[(GB + g) % (PD + 1)], __builtin_bit_cast(bf16x8, pw[g >> 2]), o[g & 3]);
      if constexpr (t < 8) {
        if constexpr (g == 0) { ma = max3(C[0][0], C[0][1], C[0][2]); mb = max3(C[0][3], C[0][4], C[0][5]); ma = max3(ma, C[0][6], C[0][7]); mb = max3(mb, C[0][8], C[0][9]); PIN(ma); PIN(mb); }
        if constexpr (g == 1) { ma = max3(ma, C[0][10], C[0][11]); mb = max3(mb, C[0][12], C[0][13]); ma = max3(ma, C[0][14], C[0][15]); mb = max3(mb, C[1][0], C[1][1]); PIN(ma); PIN(mb); }
        if constexpr (g == 2) { ma = max3(ma, C[1][2], C[1][3]); mb = max3(mb, C[1][4], C[1][5]); ma = max3(ma, C[1][6], C[1][7]); mb = max3(mb, C[1][8], C[1][9]); PIN(ma); PIN(mb); }
        if constexpr (g == 3) {
          ma = max3(ma, C[1][10], C[1][11]); mb = max3(mb, C[1][12], C[1][13]); ma = max3(ma, C[1][14], C[1][15]);
          const float rm = swap_max(__builtin_fmaxf(ma, mb));
          if (__builtin_expect(__any(rm > PTHR), 0)) {
            const float dl = __builtin_fmaxf(rm + PBIAS, 0.f); mref += dl; fct = __builtin_amdgcn_exp2f(-dl); lsum *= fct; resc = true;
#pragma unroll
            for (int r = 0; r < 16; ++r) { C[0][r] -= dl; C[1][r] -= dl; negm[r] = -mref; }
          }
        }
        if constexpr (g >= 4) {
          constexpr int e0 = (32 * (g - 4)) / 12, e1 = (32 * (g - 3)) / 12;
          static_for<e1 - e0>(LAMBDA(ec) { constexpr int e = e0 + ec.value; C[e >> 4][e & 15] = __builtin_amdgcn_exp2f(C[e >> 4][e & 15]); });
          PIN(C[0]); PIN(C[1]);
        }
      }
      SBAR();
    });
    if (t < 8 && resc) {
#pragma unroll
      for (int d = 0; d < 4; ++d)
#pragma unroll
        for (int r = 0; r < 16; ++r) o[d][r] *= fct;
    }
  };
  step(IC<1>{}, sB, sA);
  step(IC<2>{}, sA, sB);
  step(IC<3>{}, sB, sA);
  step(IC<4>{}, sA, sB);
  step(IC<5>{}, sB, sA);
  step(IC<6>{}, sA, sB);
  step(IC<7>{}, sB, sA);
  step(IC<8>{}, sA, sB);
  ST(20);
  const float inv = 1.f / swap_add(lsum);
  const int qrow = qt * 256 + wid * 32 + r32;
  const __amdgpu_buffer_rsrc_t srdO = __builtin_amdgcn_make_buffer_rsrc((void*)obuf, 0, SQ * 4096 * 2, 0x00020000);
  const unsigned voffO = (((qrow >> 4) * 512 + head * 16 + hi) * 16 + (qrow & 15)) * 16;
#pragma unroll
  for (int db = 0; db < 4; ++db)
#pragma unroll
    for (int s = 0; s < 2; ++s) {
      u32x4 w;
#pragma unroll
      for (int e = 0; e < 4; ++e) w[e] = cvtpk(o[db][8 * s + 2 * e] * inv, o[db][8 * s + 2 * e + 1] * inv);
      __builtin_amdgcn_raw_buffer_store_b128(w, srdO, voffO + (4 * db + 2 * s) * 256, 0, O_AUX);
    }
  ST(21);
  ST(31);
}

constexpr int SC1 = 16;
DI void st16(__amdgpu_buffer_rsrc_t r, unsigned voff, u32x4 v) { __builtin_amdgcn_raw_buffer_store_b128(v, r, voff, 0, SC1); }
DI void st8(__amdgpu_buffer_rsrc_t r, unsigned voff, u32x2 v) { __builtin_amdgcn_raw_buffer_store_b64(v, r, voff, 0, SC1); }
DI u32x4 ld16c(__amdgpu_buffer_rsrc_t r, unsigned voff) { return __builtin_amdgcn_raw_buffer_load_b128(r, voff, 0, SC1); }
DI void bload16c(bf16x8& dst, unsigned voff, __amdgpu_buffer_rsrc_t srd, unsigned soff) {
  asm volatile("buffer_load_dwordx4 %0, %1, %2, %3 offen sc1" : "=v"(dst) : "v"(voff), "s"(srd), "s"(soff) : "memory");
}
template <int N> DI void wait_bar4b(bf16x8 (&b)[2][2]) {
  asm volatile("s_waitcnt vmcnt(%4)\n\ts_barrier" : "+v"(b[0][0]), "+v"(b[0][1]), "+v"(b[1][0]), "+v"(b[1][1]) : "i"(N) : "memory");
}
template <int NQB, int NT, int KS> struct Ring {
  static constexpr int KSP = 8 / NQB, SK = 2 * KSP, NSTG = (KS + SK - 1) / SK, NP = SK * NT, PA = (NP + 7) / 8, SB = PA * 8 * 1024, FR = 2 * NT;
};
template <int NQB, int NT, int KS, int KC_A> struct RingA {
  using R = Ring<NQB, NT, KS>;
  unsigned voff[R::PA]; unsigned ldsw;
  DI void init(unsigned lds0, int wid, int lane) {
    ldsw = lds0 + wid * 1024;
#pragma unroll
    for (int i = 0; i < R::PA; ++i) { const int p = i * 8 + wid, ks = p / NT, nt = p - ks * NT; voff[i] = p < R::NP ? (unsigned)(nt * KC_A * 256 + ks * 1024 + lane * 16) : 0x7ff00000u; }
  }
  template <int S, int I> DI void issue(__amdgpu_buffer_rsrc_t ra) const { dma16(ldsw + ((S % 4) * R::SB + I * 8192), voff[I], ra, S * R::SK * 1024); }
  template <int NPRE> DI void pre(__amdgpu_buffer_rsrc_t ra) const { static_for<NPRE>(LAMBDA(sc) { static_for<R::PA>(LAMBDA(ic) { this->template issue<sc.value, ic.value>(ra); }); }); }
};
template <int NQB, int NT, int KS, int KC_A, int KC_B, int NPRE, int ST0>
DI void ring_gemm(f32x4 (&acc)[NT][2], const RingA<NQB, NT, KS, KC_A>& RA, __amdgpu_buffer_rsrc_t ra, __amdgpu_buffer_rsrc_t rb, const char* lds, int wid, int lane STAMP_ARG) {
  using R = Ring<NQB, NT, KS>;
  constexpr int SK = R::SK, NSTG = R::NSTG, PA = R::PA, FR = R::FR, PD = 5;
  static_assert(NSTG >= 3 && FR > PD + 1 && (NPRE == 0 || NPRE == 4), "");
  const int kq = wid / NQB;
  bf16x8 B[3][2][2];
  const unsigned voffB = lane * 16 + 2 * kq * 1024;
  auto issueA1 = [&](auto sc, auto ic) __attribute__((always_inline)) { RA.template issue<sc.value, ic.value>(ra); };
  auto issueB1 = [&](auto sc, auto ic) __attribute__((always_inline)) {
    constexpr int s = sc.value, i = ic.value, ks = i >> 1, q = i & 1;
    bload16c(B[s % 3][ks][q], voffB, rb, q * KC_B * 256 + (s * SK + ks) * 1024);
  };
  if constexpr (NPRE == 0) static_for<PA>(LAMBDA(ic) { issueA1(IC<0>{}, ic); });
  static_for<4>(LAMBDA(ic) { issueB1(IC<0>{}, ic); });
  if constexpr (NPRE > 0) { static_for<4>(LAMBDA(ic) { issueB1(IC<1>{}, ic); }); wait_bar4b<4>(B[0]); } else wait_bar4b<0>(B[0]);
  ST(ST0);
  const LAS bf16x8* pl = (const LAS bf16x8*)lds + lane + 2 * kq * NT * 64;
  bf16x8 A[PD + 1];
  auto rd = [&](auto mc) __attribute__((always_inline)) { constexpr int M = mc.value, s = M / FR, m = M % FR; A[M % (PD + 1)] = pl[((s % 4) * PA * 8 + m) * 64]; };
  static_for<PD>(LAMBDA(jc) { rd(jc); });
  SBAR();
  static_for<NSTG>(LAMBDA(sc) {
    constexpr int s = sc.value;
    constexpr int n0 = (NPRE == 0 && s == 0) ? PA + 4 : 0;
    constexpr int sa = s + 2;
    constexpr int nA = (sa < NSTG && sa >= NPRE) ? PA : 0, nB = (s + 2 < NSTG) ? 4 : 0, NI = n0 + nA + nB, NGI = FR - PD - 1;
    constexpr bool full = (s + 1) * SK <= KS;
    static_for<FR>(LAMBDA(mc) {
      constexpr int m = mc.value, M = s * FR + m, ks = m / NT, nt = m % NT;
      if constexpr (m == FR - PD && s + 1 < NSTG) wait_bar4b<nA + nB>(B[(s + 1) % 3]);
      if constexpr (M + PD < NSTG * FR) rd(IC<M + PD>{});
      if constexpr (m < NGI) {
        constexpr int i0 = (m * NI + NGI - 1) / NGI, i1 = ((m + 1) * NI + NGI - 1) / NGI;
        static_for<i1 - i0>(LAMBDA(uc) {
          constexpr int i = i0 + uc.value;
          if constexpr (i < n0) { if constexpr (i < PA) issueA1(IC<1>{}, IC<i>{}); else issueB1(IC<1>{}, IC<i - PA>{}); }
          else if constexpr (i - n0 < nA) issueA1(IC<sa>{}, IC<i - n0>{});
          else issueB1(IC<s + 2>{}, IC<i - n0 - nA>{});
        });
      }
      if (full || s * SK + 2 * kq + ks < KS) { acc[nt][0] = mfma16(A[M % (PD + 1)], B[s % 3][ks][0], acc[nt][0]); acc[nt][1] = mfma16(A[M % (PD + 1)], B[s % 3][ks][1], acc[nt][1]); }
      SBAR();
    });
  });
}
DI u32x4 pair16(u32x2 x, u32x2 y) {
  const auto a = __builtin_amdgcn_permlane16_swap(x[0], y[0], false, false), b = __builtin_amdgcn_permlane16_swap(x[1], y[1], false, false);
  return u32x4{a[0], b[0], a[1], b[1]};
}
template <int NT> DI void pair_reduce(f32x4 (&acc)[NT][2], char* lds, int wid, int lane) {
  constexpr int H0 = (NT + 1) / 2;
  f32x4* red = (f32x4*)lds + (size_t)wid * H0 * 2 * 64 + lane;
  const f32x4* in = (const f32x4*)lds + (size_t)(wid ^ 4) * H0 * 2 * 64 + lane;
  __syncthreads();
  if (wid < 4) {
    asm volatile("; lo-send" ::: "memory");
#pragma unroll
    for (int nt = H0; nt < NT; ++nt) { red[((nt - H0) * 2) * 64] = acc[nt][0]; red[((nt - H0) * 2 + 1) * 64] = acc[nt][1]; }
    asm volatile("; lo-sent" ::: "memory");
  } else {
    asm volatile("; hi-send" ::: "memory");
#pragma unroll
    for (int nt = 0; nt < H0; ++nt) { red[(nt * 2) * 64] = acc[nt][0]; red[(nt * 2 + 1) * 64] = acc[nt][1]; }
    asm volatile("; hi-sent" ::: "memory");
  }
  __syncthreads();
  if (wid < 4) {
    asm volatile("; lo-recv" ::: "memory");
#pragma unroll
    for (int nt = 0; nt < H0; ++nt) { acc[nt][0] += in[(nt * 2) * 64]; acc[nt][1] += in[(nt * 2 + 1) * 64]; PIN(acc[nt][0]); PIN(acc[nt][1]); }
    asm volatile("; lo-done" ::: "memory");
  } else {
    asm volatile("; hi-recv" ::: "memory");
#pragma unroll
    for (int nt = H0; nt < NT; ++nt) { acc[nt][0] += in[((nt - H0) * 2) * 64]; acc[nt][1] += in[((nt - H0) * 2 + 1) * 64]; PIN(acc[nt][0]); PIN(acc[nt][1]); }
    asm volatile("; hi-done" ::: "memory");
  }
  __syncthreads();
}

DI void lds_barrier() { asm volatile("s_waitcnt lgkmcnt(0)\n\ts_barrier" ::: "memory"); }
typedef GAS unsigned gu32;
DI void group_arrive(unsigned* cnt) {
  asm volatile("s_waitcnt vmcnt(0)" ::: "memory");
  __syncthreads();
  if (threadIdx.x == 0) __hip_atomic_fetch_add((gu32*)cnt, 1u, __ATOMIC_RELAXED, __HIP_MEMORY_SCOPE_AGENT);
}
DI void group_wait(unsigned* cnt, unsigned want) {
  if (threadIdx.x == 0) {
    for (unsigned spins = 0; spins < (1u << 20); ++spins) {
      if (__hip_atomic_load((gu32*)cnt, __ATOMIC_RELAXED, __HIP_MEMORY_SCOPE_AGENT) >= want) break;
      __builtin_amdgcn_s_sleep(1);
    }
  }
  asm volatile("" ::: "memory");
  __syncthreads();
}

__global__ __launch_bounds__(512, 2) void k_tail(const u16* __restrict__ obuf, const u16* __restrict__ wot, const u16* __restrict__ w1t, const u16* __restrict__ w2t,
                                                  const u16* __restrict__ x, const u16* __restrict__ mod, const u16* __restrict__ b1, const u16* __restrict__ b2,
                                                  u16* part, u16* x1, u16* h2, u16* hid, u16* __restrict__ out, unsigned* cnt STAMP_ARG) {
  ST(0);
  extern __shared__ __attribute__((aligned(16))) char lds[];
  const int tid = threadIdx.x, lane = tid & 63, wid = __builtin_amdgcn_readfirstlane(tid >> 6);
  const unsigned lds0 = (unsigned)(uintptr_t)lds;
  const int xcd = blockIdx.x & 7, idx = blockIdx.x >> 3;
  const int rt = (xcd >> 2) * 8 + (idx >> 2), gi = (xcd & 3) * 4 + (idx & 3);
  unsigned* const c1 = cnt + rt * 64, * const c2 = cnt + (16 + rt) * 64, * const c3 = cnt + (32 + rt) * 64;
  {
    const int kq = gi >> 2, j = gi & 3, qb = wid & 3;
    const __amdgpu_buffer_rsrc_t ra = __builtin_amdgcn_make_buffer_rsrc((void*)(wot + ((size_t)(10 * j) * 512 + kq * 128) * 128), 0, 10 * 512 * 256, 0x00020000);
    const __amdgpu_buffer_rsrc_t rb = __builtin_amdgcn_make_buffer_rsrc((void*)(obuf + ((size_t)(8 * rt + 2 * qb) * 512 + kq * 128) * 128), 0, 2 * 512 * 256, 0x00020000);
    const __amdgpu_buffer_rsrc_t rp = __builtin_amdgcn_make_buffer_rsrc((void*)(part + (size_t)kq * SQ * DM), 0, SQ * DM * 2, 0x00020000);
    f32x4 acc[10][2] = {};
    RingA<4, 10, 32, 512> RA; RA.init(lds0, wid, lane);
    ring_gemm<4, 10, 32, 512, 512, 0, 1>(acc, RA, ra, rb, lds, wid, lane STAMP_FWD);
    ST(2);
    pair_reduce<10>(acc, lds, wid, lane);
    ST(13);
    const int nt0 = wid < 4 ? 0 : 5;
    const unsigned vo = (((8 * rt + 2 * qb + ((lane >> 4) & 1)) * 80 + j * 20 + nt0 * 2 + (lane >> 5)) * 16 + (lane & 15)) * 16;
    auto outp = [&](auto ntc) __attribute__((always_inline)) {
      constexpr int nt = ntc.value, o = nt % 5;
      const u32x2 w0 = {cvtpk(acc[nt][0][0], acc[nt][0][1]), cvtpk(acc[nt][0][2], acc[nt][0][3])}, w1 = {cvtpk(acc[nt][1][0], acc[nt][1][1]), cvtpk(acc[nt][1][2], acc[nt][1][3])};
      st16(rp, vo + o * 2 * 256, pair16(w0, w1));
    };
    if (wid < 4) { asm volatile("; lo-out" ::: "memory"); static_for<5>(LAMBDA(c) { outp(c); }); asm volatile("; lo-end" ::: "memory"); }
    else { asm volatile("; hi-out" ::: "memory"); static_for<5>(LAMBDA(c) { outp(IC<5 + c.value>{}); }); asm volatile("; hi-end" ::: "memory"); }
  }
  ST(14);
  group_arrive(c1);
  ST(3);
  const int nft = gi < 15 ? 7 : 3;
  const __amdgpu_buffer_rsrc_t raD = __builtin_amdgcn_make_buffer_rsrc((void*)(w1t + (size_t)(7 * gi) * 80 * 128), 0, nft * 80 * 256, 0x00020000);
  RingA<4, 7, 20, 80> RD; RD.init(lds0, wid, lane);
  RD.pre<4>(raD);
  {
    float* red = (float*)(lds + 131072);
    const int r = tid & 7, row = rt * 128 + gi * 8 + r, w = tid >> 6;
    const bool two = tid < 128;
    const __amdgpu_buffer_rsrc_t rp = __builtin_amdgcn_make_buffer_rsrc((void*)part, 0, 4 * SQ * DM * 2, 0x00020000);
    const __amdgpu_buffer_rsrc_t rx1 = __builtin_amdgcn_make_buffer_rsrc((void*)x1, 0, SQ * DM * 2, 0x00020000);
    const __amdgpu_buffer_rsrc_t rh2 = __builtin_amdgcn_make_buffer_rsrc((void*)h2, 0, SQ * DM * 2, 0x00020000);
    u32x4 p[2][4], xv[2], gv[2], sbv[2], cbv[2]; unsigned blk[2];
#pragma unroll
    for (int u = 0; u < 2; ++u) {
      const int c = (u == 0 || two) ? (tid >> 3) + 64 * u : (tid >> 3);
      blk[u] = (((row >> 4) * 80 + c) * 16 + (row & 15)) * 16;
      xv[u] = *(const u32x4*)(x + (size_t)row * DM + c * 8);
      gv[u] = *(const u32x4*)(mod + 2 * 640 + c * 8); sbv[u] = *(const u32x4*)(mod + 3 * 640 + c * 8); cbv[u] = *(const u32x4*)(mod + 4 * 640 + c * 8);
    }
    group_wait(c1, 16);
    ST(4);
#pragma unroll
    for (int u = 0; u < 2; ++u)
#pragma unroll
      for (int k = 0; k < 4; ++k) p[u][k] = ld16c(rp, (u == 0 || two) ? blk[u] + k * SQ * DM * 2 : 0x7ffff000u);
    PIN(p[0][0]); PIN(p[1][3]); ST(15);
    float v[2][8]; float s = 0; u32x4 x1v[2];
#pragma unroll
    for (int u = 0; u < 2; ++u) {
#pragma unroll
      for (int e = 0; e < 4; ++e) {
        const float a = (bf_lo(p[u][0][e]) + bf_lo(p[u][1][e])) + (bf_lo(p[u][2][e]) + bf_lo(p[u][3][e])), b = (bf_hi(p[u][0][e]) + bf_hi(p[u][1][e])) + (bf_hi(p[u][2][e]) + bf_hi(p[u][3][e]));
        x1v[u][e] = cvtpk(bf_lo(xv[u][e]) + rbf(bf_lo(gv[u][e]) * rbf(a)), bf_hi(xv[u][e]) + rbf(bf_hi(gv[u][e]) * rbf(b)));
        v[u][2 * e] = bf_lo(x1v[u][e]); v[u][2 * e + 1] = bf_hi(x1v[u][e]);
        if (u == 0 || two) s += v[u][2 * e] + v[u][2 * e + 1];
      }
    }
    s += __shfl_xor(s, 8); s += __shfl_xor(s, 16); s += __shfl_xor(s, 32);
    if (lane < 8) red[w * 8 + r] = s;
    lds_barrier();
    float tot = 0;
#pragma unroll
    for (int i = 0; i < 8; ++i) tot += red[i * 8 + r];
    const float mean = tot * (1.f / DM); float q = 0;
#pragma unroll
    for (int e = 0; e < 8; ++e) { const float d = v[0][e] - mean; q += d * d; }
    if (two) {
#pragma unroll
      for (int e = 0; e < 8; ++e) { const float d = v[1][e] - mean; q += d * d; }
    }
    q += __shfl_xor(q, 8); q += __shfl_xor(q, 16); q += __shfl_xor(q, 32);
    if (lane < 8) red[64 + w * 8 + r] = q;
    lds_barrier();
    float qt = 0;
#pragma unroll
    for (int i = 0; i < 8; ++i) qt += red[64 + i * 8 + r];
    const float mb = rbf(mean), rsd = 1.f / rbf(sqrtf(rbf(rbf(qt * (1.f / DM)) + 1e-5f)));
#pragma unroll
    for (int u = 0; u < 2; ++u) {
      u32x4 hv;
#pragma unroll
      for (int e = 0; e < 4; ++e)
        hv[e] = cvtpk(rbf(rbf(rbf(v[u][2 * e] - mb) * rsd) * rbf(1.f + bf_lo(cbv[u][e]))) + bf_lo(sbv[u][e]), rbf(rbf(rbf(v[u][2 * e + 1] - mb) * rsd) * rbf(1.f + bf_hi(cbv[u][e]))) + bf_hi(sbv[u][e]));
      st16(rh2, (u == 0 || two) ? blk[u] : 0x7ffff000u, hv);
      st16(rx1, (u == 0 || two) ? blk[u] : 0x7ffff000u, x1v[u]);
    }
  }
  ST(16);
  group_arrive(c2);
  ST(5);
  const int jE = gi & 7, rtE = rt * 2 + (gi >> 3);
  const __amdgpu_buffer_rsrc_t raE = __builtin_amdgcn_make_buffer_rsrc((void*)(w2t + (size_t)(5 * jE) * 216 * 128), 0, 5 * 216 * 256, 0x00020000);
  {
    const int qb = wid & 3, nt0 = wid < 4 ? 0 : 4;
    const __amdgpu_buffer_rsrc_t rb = __builtin_amdgcn_make_buffer_rsrc((void*)(h2 + (size_t)(8 * rt + 2 * qb) * 80 * 128), 0, 2 * 80 * 256, 0x00020000);
    const __amdgpu_buffer_rsrc_t rh = __builtin_amdgcn_make_buffer_rsrc((void*)hid, 0, SQ * FF * 2, 0x00020000);
    u32x2 bv[4];
#pragma unroll
    for (int o = 0; o < 4; ++o) bv[o] = *(const u32x2*)(b1 + (nt0 + o < nft ? (7 * gi + nt0 + o) * 16 + 4 * (lane >> 4) : 0));
    f32x4 acc[7][2] = {};
    group_wait(c2, 16);
    ST(6);
    ring_gemm<4, 7, 20, 80, 80, 4, 7>(acc, RD, raD, rb, lds, wid, lane STAMP_FWD);
    ST(8);
    pair_reduce<7>(acc, lds, wid, lane);
    ST(17);
    const unsigned vo = (((8 * rt + 2 * qb + ((lane >> 4) & 1)) * 216 + (7 * gi + nt0) * 2 + (lane >> 5)) * 16 + (lane & 15)) * 16;
    auto outp = [&](auto ntc, auto oc) __attribute__((always_inline)) {
      constexpr int nt = ntc.value, o = oc.value;
      if (nt < nft) {
        const float b0 = bf_lo(bv[o][0]), b1_ = bf_hi(bv[o][0]), b2_ = bf_lo(bv[o][1]), b3 = bf_hi(bv[o][1]);
        u32x2 w[2];
#pragma unroll
        for (int q = 0; q < 2; ++q) {
          const float v0 = rbf(__builtin_fmaxf(rbf(acc[nt][q][0]) + b0, 0.f)), v1 = rbf(__builtin_fmaxf(rbf(acc[nt][q][1]) + b1_, 0.f));
          const float v2 = rbf(__builtin_fmaxf(rbf(acc[nt][q][2]) + b2_, 0.f)), v3 = rbf(__builtin_fmaxf(rbf(acc[nt][q][3]) + b3, 0.f));
          w[q] = u32x2{cvtpk(v0 * v0, v1 * v1), cvtpk(v2 * v2, v3 * v3)};
        }
        st16(rh, vo + o * 2 * 256, pair16(w[0], w[1]));
      }
    };
    if (wid < 4) { asm volatile("; lo-out" ::: "memory"); static_for<4>(LAMBDA(c) { outp(c, c); }); asm volatile("; lo-end" ::: "memory"); }
    else { asm volatile("; hi-out" ::: "memory"); static_for<3>(LAMBDA(c) { outp(IC<4 + c.value>{}, c); }); asm volatile("; hi-end" ::: "memory"); }
  }
  ST(18);
  group_arrive(c3);
  ST(9);
  RingA<2, 5, 54, 216> RE; RE.init(lds0, wid, lane);
  RE.pre<4>(raE);
  {
    const int qb = wid & 1;
    const __amdgpu_buffer_rsrc_t rb = __builtin_amdgcn_make_buffer_rsrc((void*)(hid + (size_t)(4 * rtE + 2 * qb) * 216 * 128), 0, 2 * 216 * 256, 0x00020000);
    const __amdgpu_buffer_rsrc_t rx1 = __builtin_amdgcn_make_buffer_rsrc((void*)x1, 0, SQ * DM * 2, 0x00020000);
    u32x4 xv[2], gv[2], bv[2];
#pragma unroll
    for (int it = 0; it < 2; ++it) {
      const int item = tid + it * 512, q = item & 63, c = item < 640 ? item >> 6 : 0, row = rtE * 64 + q;
      xv[it] = ld16c(rx1, (((row >> 4) * 80 + jE * 10 + c) * 16 + (row & 15)) * 16);
      gv[it] = *(const u32x4*)(mod + 5 * 640 + jE * 80 + c * 8); bv[it] = *(const u32x4*)(b2 + jE * 80 + c * 8);
    }
    f32x4 acc[5][2] = {};
    group_wait(c3, 16);
    ST(10);
    ring_gemm<2, 5, 54, 216, 216, 4, 11>(acc, RE, raE, rb, lds, wid, lane STAMP_FWD);
    ST(12);
    f32x4* red = (f32x4*)lds;
    __syncthreads();
#pragma unroll
    for (int n = 0; n < 5; ++n) { red[((wid * 5 + n) * 2) * 64 + lane] = acc[n][0]; red[((wid * 5 + n) * 2 + 1) * 64 + lane] = acc[n][1]; }
    __syncthreads();
#pragma unroll
    for (int it = 0; it < 2; ++it) {
      const int item = tid + it * 512;
      if (item < 640) {
        const int q = item & 63, c = item >> 6, n = c >> 1, l0 = (q & 15) + 32 * (c & 1), base = (((q >> 5) * 5 + n) * 2 + ((q >> 4) & 1)) * 64 + l0;
        f32x4 lo = {0, 0, 0, 0}, hi = {0, 0, 0, 0};
#pragma unroll
        for (int w = 0; w < 4; ++w) { lo += red[base + w * 2 * 5 * 2 * 64]; hi += red[base + w * 2 * 5 * 2 * 64 + 16]; }
        const float v[8] = {lo[0], lo[1], lo[2], lo[3], hi[0], hi[1], hi[2], hi[3]};
        u32x4 o;
#pragma unroll
        for (int e = 0; e < 4; ++e) {
          const float a = bf_lo(xv[it][e]) + rbf(bf_lo(gv[it][e]) * rbf(rbf(v[2 * e]) + bf_lo(bv[it][e]))), b = bf_hi(xv[it][e]) + rbf(bf_hi(gv[it][e]) * rbf(rbf(v[2 * e + 1]) + bf_hi(bv[it][e])));
          o[e] = cvtpk(a, b);
        }
        *(u32x4*)(out + (size_t)(rtE * 64 + q) * DM + jE * 80 + c * 8) = o;
      }
    }
  }
  ST(31);
}

extern "C" long long* dbg_offsets() { static long long o[5] = {(long long)WS_MOD, (long long)WS_H, (long long)WS_O, (long long)WS_X1, (long long)WS_HID}; return o; }

extern "C" void kernel_launch(void* const* d_in, const int* in_sizes, int n_in, void* d_out, int out_size, void* d_ws, size_t ws_size, hipStream_t stream) {
  const u16 *x = (const u16*)d_in[0], *kc = (const u16*)d_in[1], *vc = (const u16*)d_in[2], *wq = (const u16*)d_in[3], *wo = (const u16*)d_in[4];
  const u16 *w1 = (const u16*)d_in[6], *b1 = (const u16*)d_in[7], *w2 = (const u16*)d_in[8], *b2 = (const u16*)d_in[9], *c = (const u16*)d_in[10], *wmod = (const u16*)d_in[11];
  char* ws = (char*)d_ws;
  u16 *mod = (u16*)(ws + WS_MOD), *h = (u16*)(ws + WS_H), *o = (u16*)(ws + WS_O), *wot = (u16*)(ws + WS_WOT), *w1t = (u16*)(ws + WS_W1T), *w2t = (u16*)(ws + WS_W2T);
  u16 *x1 = (u16*)(ws + WS_X1), *hid = (u16*)(ws + WS_HID);
  u16 *h2 = (u16*)(ws + WS_H2), *part = (u16*)(ws + WS_P);
  (void)hipFuncSetAttribute((const void*)k_attn, hipFuncAttributeMaxDynamicSharedMemorySize, (NS + 2) * SLOT);
  unsigned* cnt = (unsigned*)(ws + WS_CNT);
  (void)hipFuncSetAttribute((const void*)k_tail, hipFuncAttributeMaxDynamicSharedMemorySize, 163840);
  k_mod<<<60, 512, 0, stream>>>(c, wmod, mod STAMP_PASS(0));
  k_ln1<<<128, 256, 0, stream>>>(x, mod, h STAMP_PASS(1));
  k_attn<<<256, 512, (NS + 2) * SLOT, stream>>>(h, wq, kc, vc, o, wo, w1, w2, wot, w1t, w2t, cnt STAMP_PASS(2));
  k_tail<<<256, 512, 163840, stream>>>(o, wot, w1t, w2t, x, mod, b1, b2, part, x1, h2, hid, (u16*)d_out, cnt STAMP_PASS(3));
}
```

```cpp
#include <hip/hip_runtime.h>
#include <stdint.h>
#include <utility>

typedef unsigned short u16;
typedef short bf16x8 __attribute__((ext_vector_type(8)));
typedef short s16x4 __attribute__((ext_vector_type(4)));
typedef float f32x16 __attribute__((ext_vector_type(16)));
typedef float f32x4 __attribute__((ext_vector_type(4)));
typedef float f32x2 __attribute__((ext_vector_type(2)));
typedef __bf16 bf16x2 __attribute__((ext_vector_type(2)));
typedef unsigned u32x4 __attribute__((ext_vector_type(4)));
typedef unsigned u32x2 __attribute__((ext_vector_type(2)));
#define DI __device__ __forceinline__
#define GAS __attribute__((address_space(1)))
#define LAS __attribute__((address_space(3)))
#define SBAR() __builtin_amdgcn_sched_barrier(0)
#define PIN(x) asm volatile("" : "+v"(x))

constexpr int SQ = 2048, DM = 640, NH = 32, DH = 128, SK = 2048, WIN = 512, FF = 1728;
constexpr size_t WS_MOD = 0;
constexpr size_t WS_CNT = 8192;
constexpr size_t WS_SYNC = 32768;
constexpr size_t WS_H = 131072;
constexpr size_t WS_O = WS_H + (size_t)SQ * DM * 2;
constexpr size_t WS_WOT = WS_O + (size_t)SQ * 4096 * 2;
constexpr size_t WS_W1T = WS_WOT + (size_t)4096 * DM * 2;
constexpr size_t WS_W2T = WS_W1T + (size_t)DM * FF * 2;
constexpr size_t WS_X1 = WS_W2T + (size_t)DM * FF * 2 + 65536;
constexpr size_t WS_HID = WS_X1 + (size_t)SQ * DM * 2;
constexpr size_t WS_H2 = WS_HID + (size_t)SQ * FF * 2 + 65536;
constexpr size_t WS_P = WS_H2 + (size_t)SQ * DM * 2;
constexpr size_t WS_END = WS_P + (size_t)4 * SQ * DM * 2;

#define STAMP_ARG
#define STAMP_FWD
#define STAMP_PASS(k)
#define ST(i)
#define STC(i)
template <int... I, class F> DI void static_for_impl(std::integer_sequence<int, I...>, F&& f) { (f(std::integral_constant<int, I>{}), ...); }
template <int N, class F> DI void static_for(F&& f) { static_for_impl(std::make_integer_sequence<int, N>{}, f); }
#define LAMBDA(ic) [&](auto ic) __attribute__((always_inline))

DI float bf_lo(unsigned w) { return __uint_as_float(w << 16); }
DI float bf_hi(unsigned w) { return __uint_as_float(w & 0xffff0000u); }
DI float bf2f(u16 v) { return __uint_as_float((unsigned)v << 16); }
DI unsigned cvtpk(float lo, float hi) { f32x2 v = {lo, hi}; bf16x2 b = __builtin_convertvector(v, bf16x2); return __builtin_bit_cast(unsigned, b); }
DI u16 f2bf(float x) { return (u16)(cvtpk(x, 0.f) & 0xffffu); }
DI float rbf(float x) { return bf_lo(cvtpk(x, 0.f)); }
DI f32x16 mfma32(bf16x8 a, bf16x8 b, f32x16 c) { return __builtin_amdgcn_mfma_f32_32x32x16_bf16(a, b, c, 0, 0, 0); }
DI f32x4 mfma16(bf16x8 a, bf16x8 b, f32x4 c) { return __builtin_amdgcn_mfma_f32_16x16x32_bf16(a, b, c, 0, 0, 0); }
DI float swap_max(float m) { auto r = __builtin_amdgcn_permlane32_swap(__float_as_uint(m), __float_as_uint(m), false, false); return __builtin_fmaxf(__uint_as_float(r[0]), __uint_as_float(r[1])); }
DI float swap_add(float m) { auto r = __builtin_amdgcn_permlane32_swap(__float_as_uint(m), __float_as_uint(m), false, false); return __uint_as_float(r[0]) + __uint_as_float(r[1]); }

DI void lds_barrier() { asm volatile("s_waitcnt lgkmcnt(0)\n\ts_barrier" ::: "memory"); }
template <int CTRL> DI float dpp_mov(float v) { return __builtin_bit_cast(float, __builtin_amdgcn_update_dpp(0, __builtin_bit_cast(int, v), CTRL, 0xf, 0xf, true)); }
template <int M> DI float xor_add(float v) {
  if constexpr (M == 1) return v + dpp_mov<0xB1>(v);
  else if constexpr (M == 2) return v + dpp_mov<0x4E>(v);
  else if constexpr (M == 4) return v + dpp_mov<0x141>(dpp_mov<0x1B>(v));
  else if constexpr (M == 8) return v + dpp_mov<0x128>(v);
  else if constexpr (M == 16) { const auto r = __builtin_amdgcn_permlane16_swap(__float_as_uint(v), __float_as_uint(v), false, false); return __uint_as_float(r[0]) + __uint_as_float(r[1]); }
  else { const auto r = __builtin_amdgcn_permlane32_swap(__float_as_uint(v), __float_as_uint(v), false, false); return __uint_as_float(r[0]) + __uint_as_float(r[1]); }
}
typedef GAS unsigned gu32;
typedef GAS unsigned long long gu64;
#define RLX_AGENT __ATOMIC_RELAXED, __HIP_MEMORY_SCOPE_AGENT
constexpr int FR_NP = 120, FR_NL = 128, FR_N = FR_NP + FR_NL;
constexpr unsigned SPIN_CAP = 1u << 18;
#define FR_SLEEP 4
constexpr int SC1 = 16;
__global__ __launch_bounds__(512) void k_front(const u16* __restrict__ c, const u16* __restrict__ wmod, const u16* __restrict__ x, u16* __restrict__ mod, u16* __restrict__ h,
                                               unsigned* sync  , unsigned* __restrict__ cnt STAMP_ARG) {
  ST(0);
  __shared__ float sm[1280];
  const int t = threadIdx.x, b = blockIdx.x;
  if (b < FR_NP) {
    const int cc = t & 3, r0 = t >> 2, col0 = b * 32 + cc * 8;
    u32x4 w[5];
#pragma unroll
    for (int i = 0; i < 5; ++i) w[i] = *(const u32x4*)(wmod + (size_t)(r0 + 128 * i) * 3840 + col0);
    float* sil = sm + 512;
    const unsigned cv = *(const unsigned*)(c + 2 * (t < 320 ? t : 0));
    unsigned tag = __hip_atomic_load((gu32*)sync, RLX_AGENT) + 1u;
    if (t < 320) {
      const float v0 = bf_lo(cv), v1 = bf_hi(cv);
      const unsigned pk = cvtpk(v0 * __builtin_amdgcn_rcpf(1.f + __expf(-v0)), v1 * __builtin_amdgcn_rcpf(1.f + __expf(-v1)));
      *(f32x2*)(sil + 2 * t) = f32x2{bf_lo(pk), bf_hi(pk)};
    }
    if (b == FR_NP - 1 && t < 48) cnt[t * 64] = 0u;
    PIN(tag);
    ST(4);
    lds_barrier();
    if (t == 0) __hip_atomic_store((gu32*)sync + 64 + b, tag, RLX_AGENT);
    ST(5);
    PIN(w[4]); ST(6);
    float a[8] = {};
#pragma unroll
    for (int i = 0; i < 5; ++i) {
      const float sv = sil[r0 + 128 * i];
#pragma unroll
      for (int j = 0; j < 4; ++j) { a[2 * j] += sv * bf_lo(w[i][j]); a[2 * j + 1] += sv * bf_hi(w[i][j]); }
    }
#pragma unroll
    for (int j = 0; j < 8; ++j) a[j] = xor_add<32>(xor_add<16>(xor_add<8>(xor_add<4>(a[j]))));
    if ((t & 63) < 4) {
#pragma unroll
      for (int j = 0; j < 8; ++j) sm[(t >> 6) * 32 + cc * 8 + j] = a[j];
    }
    lds_barrier();
    ST(3);
    if (t < 128) {
      const int u = t & 15;
      float lo = 0.f, hi = 0.f;
#pragma unroll
      for (int w8 = 0; w8 < 8; ++w8) { lo += sm[w8 * 32 + 2 * u]; hi += sm[w8 * 32 + 2 * u + 1]; }
      const unsigned pk = cvtpk(lo, hi);
      if (b < 40) __hip_atomic_store((gu64*)(sync + 2048 + (t >> 4) * 2048) + b * 16 + u, ((unsigned long long)tag << 32) | pk, RLX_AGENT);
      if (t < 16) *(unsigned*)(mod + b * 32 + 2 * u) = pk;
    }
    if (b == FR_NP - 1 && t < 64) {
      bool ok = false;
      for (unsigned spins = 0; spins < SPIN_CAP && !ok; ++spins) {
        bool all = true;
#pragma unroll
        for (int k = 0; k < 4; ++k) { const int w = t + 64 * k; if (w < FR_N) all &= __hip_atomic_load((gu32*)sync + 64 + w, RLX_AGENT) == tag; }
        ok = __all(all);
        if (!ok) __builtin_amdgcn_s_sleep(2);
      }
      if (t == 0) __hip_atomic_store((gu32*)sync, tag, RLX_AGENT);
    }
  } else {
    if (t >= 256) return;
    const int i = t & 15, row = (b - FR_NP) * 16 + (t >> 4);
    u32x4 v[5];
#pragma unroll
    for (int j = 0; j < 5; ++j) v[j] = *(const u32x4*)(x + (size_t)row * DM + (i + 16 * j) * 8);
    const unsigned tag = __hip_atomic_load((gu32*)sync, RLX_AGENT) + 1u;
    if (t == 0) __hip_atomic_store((gu32*)sync + 64 + b, tag, RLX_AGENT);
    float f[40]; float s = 0;
#pragma unroll
    for (int j = 0; j < 5; ++j)
#pragma unroll
      for (int e = 0; e < 4; ++e) { f[j * 8 + 2 * e] = bf_lo(v[j][e]); f[j * 8 + 2 * e + 1] = bf_hi(v[j][e]); s += f[j * 8 + 2 * e] + f[j * 8 + 2 * e + 1]; }
    s = xor_add<8>(xor_add<4>(xor_add<2>(xor_add<1>(s))));
    const float mean = s * (1.f / DM); float q = 0;
#pragma unroll
    for (int e = 0; e < 40; ++e) { const float d = f[e] - mean; q += d * d; }
    q = xor_add<8>(xor_add<4>(xor_add<2>(xor_add<1>(q))));
    const float mb = rbf(mean), rsd = 1.f / rbf(sqrtf(rbf(rbf(q * (1.f / DM)) + 1e-5f)));
#pragma unroll
    for (int e = 0; e < 40; e += 2) {
      const unsigned d = cvtpk(f[e] - mb, f[e + 1] - mb), n = cvtpk(bf_lo(d) * rsd, bf_hi(d) * rsd);
      f[e] = bf_lo(n); f[e + 1] = bf_hi(n);
    }
    ST(1);
    if (t < 64) {
      const __amdgpu_buffer_rsrc_t rg = __builtin_amdgcn_make_buffer_rsrc((void*)(sync + 2048 + (b & 7) * 2048), 0, 5120, 0x00020000);
      u32x4 gv[5];
      for (unsigned spins = 0; spins < SPIN_CAP; ++spins) {
        bool ok = true;
#pragma unroll
        for (int k = 0; k < 5; ++k) { gv[k] = __builtin_amdgcn_raw_buffer_load_b128(rg, (t + 64 * k) * 16, 0, SC1); ok &= gv[k][1] == tag && gv[k][3] == tag; }
        if (__all(ok)) break;
        __builtin_amdgcn_s_sleep(FR_SLEEP);
      }
#pragma unroll
      for (int k = 0; k < 5; ++k) {
        const int g = (t + 64 * k) * 2;
        float v0 = bf_lo(gv[k][0]), v1 = bf_hi(gv[k][0]), v2 = bf_lo(gv[k][2]), v3 = bf_hi(gv[k][2]);
        if (g >= 320) { const unsigned o = cvtpk(1.f + v0, 1.f + v1), p = cvtpk(1.f + v2, 1.f + v3); v0 = bf_lo(o); v1 = bf_hi(o); v2 = bf_lo(p); v3 = bf_hi(p); }
        *(f32x4*)(sm + 2 * g) = f32x4{v0, v1, v2, v3};
      }
    }
    ST(2);
    __syncthreads();
    const int qb = row >> 5, r32 = row & 31;
#pragma unroll
    for (int j = 0; j < 5; ++j) {
      const int k0 = (i + 16 * j) * 8;
      const f32x4 s0 = *(const f32x4*)(sm + k0), s1 = *(const f32x4*)(sm + k0 + 4), c0 = *(const f32x4*)(sm + 640 + k0), c1 = *(const f32x4*)(sm + 640 + k0 + 4);
      const float sa[8] = {s0[0], s0[1], s0[2], s0[3], s1[0], s1[1], s1[2], s1[3]}, oc[8] = {c0[0], c0[1], c0[2], c0[3], c1[0], c1[1], c1[2], c1[3]};
      u32x4 o;
#pragma unroll
      for (int e = 0; e < 4; ++e) {
        const unsigned m = cvtpk(f[j * 8 + 2 * e] * oc[2 * e], f[j * 8 + 2 * e + 1] * oc[2 * e + 1]);
        o[e] = cvtpk(bf_lo(m) + sa[2 * e], bf_hi(m) + sa[2 * e + 1]);
      }
      *(u32x4*)(h + (((size_t)qb * 80 + (i + 16 * j)) * 32 + r32) * 8) = o;
    }
  }
  ST(31);
}

#define A_NS 8
#define A_PD 5
#define O_AUX 16
constexpr int NS = A_NS, SLOT = 16384;
constexpr float QSCALE = 0.088388347648318440f * 1.4426950408889634f;
constexpr float PBIAS = 96.f;
constexpr float PTHR = 30.f;
constexpr int tile_K(int t) { return t == 0 ? 10 : 9 + 2 * t; }
constexpr int tile_V(int t) { return t == 7 ? 25 : 12 + 2 * t; }
constexpr int slot_of(int idx) { return (idx % NS) * SLOT; }
constexpr int lastuse(int idx) { return idx < 10 ? idx : (idx == 10 ? 10 : (idx == 25 ? 18 : ((idx & 1) ? 10 + (idx - 9) / 2 : 11 + (idx - 12) / 2))); }
constexpr int need_bar(int idx) {
  if (idx < 10) return idx == 0 ? 0 : idx - 1;
  if (idx == 10) return 9;
  if (idx == 25) return 17;
  return (idx & 1) ? 10 + (idx - 9) / 2 - 1 : 11 + (idx - 12) / 2;
}
constexpr int rt_step(int u) { return u == 0 ? 11 : (u == 1 ? 12 : 16); }
constexpr int rt_slot(int u) { return u == 0 ? NS * SLOT : (u == 1 ? (NS + 1) * SLOT : slot_of(tile_K(5))); }
struct Sched { int npro; int pro[16]; int nops[19]; int op[19][12]; int wait[19]; };
constexpr Sched make_sched() {
  Sched p{}; int pos = 0; int tile_end[26] = {}; int h_end[10] = {}; int next_tile = 3;
  for (int i = 0; i < 3; ++i) {
    p.pro[p.npro++] = 2 * i; p.pro[p.npro++] = 2 * i + 1; pos += 2; tile_end[i] = pos;
    if (i < 2) { for (int k = 0; k < 4; ++k) p.pro[p.npro++] = 100 + 4 * i + k; pos += 4; h_end[i] = pos; }
  }
  for (int g = 0; g < 19; ++g) {
    int need = 0;
    for (int idx = 0; idx < next_tile; ++idx) if (need_bar(idx) <= g && tile_end[idx] > need) need = tile_end[idx];
    if (g < 10 && h_end[g] > need) need = h_end[g];
    p.wait[g] = pos - need;
    int n = 0;
    if (g == 0) { for (int k = 0; k < 4; ++k) p.op[g][n++] = 100 + 4 * 2 + k; pos += 4; h_end[2] = pos; }
    const int maxt = g < 7 ? 1 : 2;
    for (int c = 0; c < maxt && next_tile < 26; ++c) {
      if (next_tile >= NS && lastuse(next_tile - NS) >= g) break;
      p.op[g][n++] = 2 * next_tile; p.op[g][n++] = 2 * next_tile + 1; pos += 2; tile_end[next_tile] = pos; ++next_tile;
    }
    if (g + 3 < 10) { for (int k = 0; k < 4; ++k) p.op[g][n++] = 100 + 4 * (g + 3) + k; pos += 4; h_end[g + 3] = pos; }
    for (int u = 0; u < 3; ++u) if (g == rt_step(u)) { p.op[g][n++] = 200 + 2 * u; p.op[g][n++] = 201 + 2 * u; pos += 2; }
    p.nops[g] = n;
  }
  p.wait[18] = 0;
  for (int idx = 0; idx < 26; ++idx) if (tile_end[idx] == 0) p.wait[0] = -1;
  return p;
}
constexpr Sched SCH = make_sched();
static_assert(SCH.wait[0] >= 0, "schedule incomplete");
constexpr bool sched_ok() {
  int step_of[26] = {}; for (int i = 0; i < 26; ++i) step_of[i] = -1;
  for (int g = 0; g < 19; ++g) for (int j = 0; j < SCH.nops[g]; ++j) if (SCH.op[g][j] < 100) step_of[SCH.op[g][j] >> 1] = g;
  for (int i = 3; i < 26; ++i) if (step_of[i] >= need_bar(i)) return false;
  return true;
}
static_assert(sched_ok(), "a tile is issued too late");

DI void dma16(unsigned ldsaddr, unsigned voff, __amdgpu_buffer_rsrc_t srd, unsigned soff) {
  asm volatile("s_mov_b32 m0, %0\n\ts_nop 0\n\tbuffer_load_dwordx4 %1, %2, %3 offen lds" ::"s"(ldsaddr), "v"(voff), "s"(srd), "s"(soff) : "memory");
}
template <int OFF> DI void bload16(bf16x8& dst, unsigned voff, __amdgpu_buffer_rsrc_t srd, unsigned soff) {
  asm volatile("buffer_load_dwordx4 %0, %1, %2, %3 offen offset:%4" : "=v"(dst) : "v"(voff), "s"(srd), "s"(soff), "i"(OFF) : "memory");
}
typedef LAS const char* lds_cptr;
typedef short v4i16_t __attribute__((ext_vector_type(4)));
DI s16x4 vtr(lds_cptr p) { return __builtin_bit_cast(s16x4, __builtin_amdgcn_ds_read_tr16_b64_v4i16((LAS v4i16_t*)p)); }
DI bf16x8 trfrag(lds_cptr p0, lds_cptr p1) { const s16x4 lo = vtr(p0), hi = vtr(p1); return __builtin_shufflevector(lo, hi, 0, 1, 2, 3, 4, 5, 6, 7); }
DI bf16x8 rowfrag(lds_cptr p) { return *(LAS const bf16x8*)p; }
DI float max3(float a, float b, float c) { return __builtin_fmaxf(__builtin_fmaxf(a, b), c); }

template <int N> DI void wait_bar4(bf16x8 (&b)[4]) {
  asm volatile("s_waitcnt vmcnt(%4)\n\ts_barrier" : "+v"(b[0]), "+v"(b[1]), "+v"(b[2]), "+v"(b[3]) : "i"(N) : "memory");
}
template <int N> DI void wait_bar() { asm volatile("s_waitcnt vmcnt(%0)\n\ts_barrier" ::"i"(N) : "memory"); }
template <int V> using IC = std::integral_constant<int, V>;

__global__ __launch_bounds__(512, 2) void k_attn(const u16* __restrict__ hbuf, const u16* __restrict__ wq, const u16* __restrict__ kc, const u16* __restrict__ vc, u16* __restrict__ obuf,
    const u16* __restrict__ wo, const u16* __restrict__ w1, const u16* __restrict__ w2, u16* __restrict__ wot, u16* __restrict__ w1t, u16* __restrict__ w2t STAMP_ARG) {
  ST(0);
  extern __shared__ __attribute__((aligned(16))) char lds[];
  const int tid = threadIdx.x, lane = tid & 63, r32 = lane & 31, hi = lane >> 5;
  const int wid = __builtin_amdgcn_readfirstlane(tid >> 6);
  const int xcd = blockIdx.x & 7, idx = blockIdx.x >> 3;
  const int head = (xcd & 3) * 8 + (idx & 7), qt = (xcd >> 2) * 4 + (idx >> 3);
  const unsigned lds0 = (unsigned)(uintptr_t)lds;
  const __amdgpu_buffer_rsrc_t srdW = __builtin_amdgcn_make_buffer_rsrc((void*)(wq + head * DH), 0, (DM - 1) * 8192 + 256, 0x00020000);
  const __amdgpu_buffer_rsrc_t srdK = __builtin_amdgcn_make_buffer_rsrc((void*)(kc + ((size_t)head * SK + (SK - WIN)) * DH), 0, WIN * 256, 0x00020000);
  const __amdgpu_buffer_rsrc_t srdV = __builtin_amdgcn_make_buffer_rsrc((void*)(vc + ((size_t)head * SK + (SK - WIN)) * DH), 0, WIN * 256, 0x00020000);
  const __amdgpu_buffer_rsrc_t srdH = __builtin_amdgcn_make_buffer_rsrc((void*)(hbuf + (size_t)(qt * 8 + wid) * 80 * 256), 0, 80 * 512, 0x00020000);
  const int dr = (lane >> 2) & 7, dx = lane & 3, dc = lane >> 5, dz = (2 * wid + (dr >> 2)) & 3;
  const unsigned voffW = (unsigned)((8 * wid + dr) * 8192 + 64 * dc + 16 * (dx ^ dz));
  const unsigned voffKV = (unsigned)((8 * wid + dr) * 256 + 64 * dc + 16 * (dx ^ dz));
  const unsigned ldsw = lds0 + 2048 * wid;
  const unsigned voffH = lane * 16;
  bf16x8 hf[10][4];
  struct Unit { const u16* src; u16* dst; int pitch, rows, kt, nt, nsub, kc; };
  auto unit_of = [&](int u) __attribute__((always_inline)) {
    Unit r; const int id = blockIdx.x + 256 * u;
    if (id < 320) { r.src = wo; r.dst = wot; r.pitch = 1280; r.rows = 4096; r.kt = id / 5; r.nt = id - r.kt * 5; r.nsub = 40; r.kc = 512; }
    else if (id < 455) { const int v = id - 320; r.src = w2; r.dst = w2t; r.pitch = 1280; r.rows = 1728; r.kt = v / 5; r.nt = v - r.kt * 5; r.nsub = 40; r.kc = 216; }
    else { const int v = id < 595 ? id - 455 : 0; r.src = w1; r.dst = w1t; r.pitch = 3456; r.rows = id < 595 ? 640 : 0; r.kt = v / 14; r.nt = v - r.kt * 14; r.nsub = id < 595 ? 108 : 0; r.kc = 80; }
    return r;
  };
  auto vmem_op = [&](auto oc) __attribute__((always_inline)) {
    constexpr int op = oc.value;
    if constexpr (op >= 200) {
      constexpr int u = (op - 200) >> 1, half = op & 1;
      const Unit un = unit_of(u);
      const __amdgpu_buffer_rsrc_t srd = __builtin_amdgcn_make_buffer_rsrc((void*)un.src, 0, un.rows * un.pitch, 0x00020000);
      dma16(ldsw + rt_slot(u) + 1024 * half, (unsigned)((8 * wid + dr) * un.pitch + 64 * dc + 16 * (dx ^ dz)), srd, un.kt * 64 * un.pitch + un.nt * 256 + 128 * half);
    } else if constexpr (op >= 100) { constexpr int i = (op - 100) >> 2, k = (op - 100) & 3; bload16<1024 * k>(hf[i][k], voffH, srdH, i * 4096); }
    else {
      constexpr int i = op >> 1, half = op & 1; constexpr unsigned so = slot_of(i) + 1024 * half;
      if constexpr (i < 10) dma16(ldsw + so, voffW, srdW, i * 64 * 8192 + 128 * half);
      else {
        constexpr bool isK = (i == 10) || (i != 25 && (i & 1)); constexpr int t = i == 10 ? 0 : (i == 25 ? 7 : (isK ? (i - 9) / 2 : (i - 12) / 2));
        dma16(ldsw + so, voffKV, isK ? srdK : srdV, t * 16384 + 128 * half);
      }
    }
  };
  static_for<SCH.npro>(LAMBDA(jc) { vmem_op(IC<SCH.pro[jc.value]>{}); });
  auto vmem_gap = [&](auto gc, auto jc, auto nc) __attribute__((always_inline)) {
    constexpr int g = gc.value, j = jc.value, n = nc.value, no = SCH.nops[g];
    constexpr int i0 = (j * no + n - 1) / n, i1 = ((j + 1) * no + n - 1) / n;
    static_for<i1 - i0>(LAMBDA(uc) { vmem_op(IC<SCH.op[g][i0 + uc.value]>{}); });
  };

  const int blk = (lane >> 4) & 1, q4 = (lane & 15) >> 2, p4 = lane & 3;
  constexpr int NR = (NS * SLOT + 65535) / 65536;
  const lds_cptr L = (lds_cptr)lds;
  lds_cptr wb[NR][2], vb[NR][2], kb_[NR][2];
#pragma unroll
  for (int r = 0; r < NR; ++r)
#pragma unroll
    for (int t = 0; t < 2; ++t) {
      wb[r][t] = L + 65536 * r + 2048 * hi + 64 * (4 * t + q4) + 16 * ((2 * blk + (p4 & 1)) ^ (2 * hi + t)) + 8 * (p4 >> 1);
      vb[r][t] = L + 65536 * r + 64 * (4 * hi + q4) + 16 * ((2 * blk + (p4 & 1)) ^ (2 * t + hi)) + 8 * (p4 >> 1) + 2048 * t;
      kb_[r][t] = L + 65536 * r + 2048 * (r32 >> 3) + 64 * (r32 & 7) + 16 * ((2 * t + hi) ^ ((r32 >> 2) & 3));
      PIN(wb[r][t]);
    }
  constexpr int PD = A_PD, NG = 416;
  bf16x8 fr[PD + 1];
  auto ldfrag_g = [&](auto Gc) __attribute__((always_inline)) {
    constexpr int G = Gc.value;
    if constexpr (G < NG) {
      if constexpr (G < 160) {
        constexpr int i = G >> 4, ks = (G >> 2) & 3, db = G & 3, o = slot_of(i) + 4096 * ks + 512 * db;
        fr[G % (PD + 1)] = trfrag(wb[o >> 16][0] + (o & 65535), wb[o >> 16][1] + (o & 65535));
      } else {
        constexpr int u = G - 176, t = G < 176 ? 0 : (G >= 400 ? 8 : 1 + u / 32), g = G < 176 ? G - 160 : (G >= 400 ? G - 400 + 16 : u % 32);
        if constexpr (g < 16) {
          constexpr int kb = g >> 3, s = g & 7, o = slot_of(tile_K(t)) + 8192 * kb + 512 * (s >> 1);
          fr[G % (PD + 1)] = rowfrag(kb_[o >> 16][s & 1] + (o & 65535));
        } else {
          constexpr int gg = g - 16, ks = gg >> 2, db = gg & 3, o = slot_of(tile_V(t - 1)) + 4096 * ks + 512 * db;
          fr[G % (PD + 1)] = trfrag(vb[o >> 16][0] + (o & 65535), vb[o >> 16][1] + (o & 65535));
        }
      }
    }
  };
  auto barrier = [&](auto gc) __attribute__((always_inline)) {
    constexpr int g = gc.value;
    if constexpr (g < 10) wait_bar4<SCH.wait[g]>(hf[g]); else wait_bar<SCH.wait[g]>();
    ST(1 + g);
    if constexpr (g == 0) STC(28);
    if constexpr (g == 10) STC(29);
    if constexpr (g == 18) STC(30);
    SBAR();
  };

  f32x16 acc[4] = {};
  static_for<10>(LAMBDA(ic) {
    constexpr int i = ic.value;
    barrier(ic);
    if constexpr (i == 0) { static_for<PD>(LAMBDA(jc) { ldfrag_g(jc); }); SBAR(); }
    static_for<16>(LAMBDA(mc) {
      constexpr int m = mc.value, G = 16 * i + m;
      ldfrag_g(IC<G + PD>{});
      vmem_gap(ic, mc, IC<16>{});
      acc[m & 3] = mfma32(fr[G % (PD + 1)], hf[i][m >> 2], acc[m & 3]);
      SBAR();
    });
  });
  bf16x8 qf[8];
#pragma unroll
  for (int s = 0; s < 8; ++s) {
    u32x4 w;
#pragma unroll
    for (int e = 0; e < 4; ++e) w[e] = cvtpk(acc[s >> 1][8 * (s & 1) + 2 * e] * QSCALE, acc[s >> 1][8 * (s & 1) + 2 * e + 1] * QSCALE);
    qf[s] = __builtin_bit_cast(bf16x8, w);
  }
#pragma unroll
  for (int r = 0; r < NR; ++r)
#pragma unroll
    for (int t = 0; t < 2; ++t) { PIN(vb[r][t]); PIN(kb_[r][t]); }

  f32x16 o[4] = {}; f32x16 sA[2], sB[2], negm; u32x4 pw[4];
  float mref, lsum = 0.f;
  barrier(IC<10>{});
  {
    const f32x16 zero = {};
    static_for<16>(LAMBDA(gc) {
      constexpr int g = gc.value, G = 160 + g;
      ldfrag_g(IC<G + PD>{});
      vmem_gap(IC<10>{}, gc, IC<16>{});
      sA[g >> 3] = mfma32(fr[G % (PD + 1)], qf[g & 7], (g & 7) == 0 ? zero : sA[g >> 3]);
      SBAR();
    });
    float a = max3(sA[0][0], sA[0][1], sA[1][0]), b = max3(sA[0][2], sA[0][3], sA[1][1]); a = max3(a, sA[1][2], sA[1][3]);
#pragma unroll
    for (int r = 4; r < 16; r += 4) { a = max3(a, sA[0][r], sA[0][r + 1]); b = max3(b, sA[0][r + 2], sA[0][r + 3]); a = max3(a, sA[1][r], sA[1][r + 1]); b = max3(b, sA[1][r + 2], sA[1][r + 3]); }
    mref = swap_max(__builtin_fmaxf(a, b)) + PBIAS;
#pragma unroll
    for (int r = 0; r < 16; ++r) { sA[0][r] = __builtin_amdgcn_exp2f(sA[0][r] - mref); sA[1][r] = __builtin_amdgcn_exp2f(sA[1][r] - mref); negm[r] = -mref; }
    PIN(negm);
    SBAR();
  }
  auto step = [&](auto tc, f32x16(&C)[2], f32x16(&P)[2]) __attribute__((always_inline)) {
    constexpr int t = tc.value;
    constexpr int G0 = 176 + 32 * (t - 1), GB = t < 8 ? G0 + 16 : 400;
    barrier(IC<10 + t>{});
    if constexpr (t == 8) {
      const int g4 = lane >> 4;
#pragma unroll
      for (int u = 0; u < 3; ++u) {
        const Unit un = unit_of(u);
        const int ntg = un.nt * 8 + wid;
        const __amdgpu_buffer_rsrc_t srd = __builtin_amdgcn_make_buffer_rsrc((void*)un.dst, 0, un.nsub * un.kc * 256, 0x00020000);
#pragma unroll
        for (int ks = 0; ks < 2; ++ks) {
          const int chq = 2 * wid + (p4 >> 1);
          const lds_cptr p0 = L + rt_slot(u) + 2048 * (4 * ks + g4) + 512 * (chq >> 2) + 64 * q4 + 16 * ((chq & 3) ^ ((2 * g4) & 3)) + 8 * (p4 & 1);
          const lds_cptr p1 = L + rt_slot(u) + 2048 * (4 * ks + g4) + 512 * (chq >> 2) + 64 * (4 + q4) + 16 * ((chq & 3) ^ ((2 * g4 + 1) & 3)) + 8 * (p4 & 1);
          const bf16x8 f = trfrag(p0, p1);
          __builtin_amdgcn_raw_buffer_store_b128(__builtin_bit_cast(u32x4, f), srd, (ntg * un.kc + un.kt * 8 + 4 * ks) * 256 + lane * 16, 0, O_AUX);
        }
      }
      SBAR();
    }
    float sacc = 0.f;
    static_for<16>(LAMBDA(gc) {
      constexpr int g = gc.value;
      if constexpr (t < 8) {
        ldfrag_g(IC<G0 + g + PD>{});
        vmem_gap(IC<10 + t>{}, gc, IC<32>{});
        C[g >> 3] = mfma32(fr[(G0 + g) % (PD + 1)], qf[g & 7], (g & 7) == 0 ? negm : C[g >> 3]);
      }
      constexpr int ks = g >> 2, w = g & 3, e = 8 * (ks & 1) + 2 * w;
      sacc += P[ks >> 1][e]; sacc += P[ks >> 1][e + 1];
      pw[ks][w] = cvtpk(P[ks >> 1][e], P[ks >> 1][e + 1]);
      PIN(pw[ks]); PIN(sacc);
      SBAR();
    });
    lsum += sacc;
    bool resc = false; float fct = 1.f; float ma, mb;
    static_for<16>(LAMBDA(gc) {
      constexpr int g = gc.value;
      ldfrag_g(IC<GB + g + PD>{});
      if constexpr (t < 8) vmem_gap(IC<10 + t>{}, IC<16 + g>{}, IC<32>{});
      o[g & 3] = mfma32(fr[(GB + g) % (PD + 1)], __builtin_bit_cast(bf16x8, pw[g >> 2]), o[g & 3]);
      if constexpr (t < 8) {
        if constexpr (g == 0) { ma = max3(C[0][0], C[0][1], C[0][2]); mb = max3(C[0][3], C[0][4], C[0][5]); ma = max3(ma, C[0][6], C[0][7]); mb = max3(mb, C[0][8], C[0][9]); PIN(ma); PIN(mb); }
        if constexpr (g == 1) { ma = max3(ma, C[0][10], C[0][11]); mb = max3(mb, C[0][12], C[0][13]); ma = max3(ma, C[0][14], C[0][15]); mb = max3(mb, C[1][0], C[1][1]); PIN(ma); PIN(mb); }
        if constexpr (g == 2) { ma = max3(ma, C[1][2], C[1][3]); mb = max3(mb, C[1][4], C[1][5]); ma = max3(ma, C[1][6], C[1][7]); mb = max3(mb, C[1][8], C[1][9]); PIN(ma); PIN(mb); }
        if constexpr (g == 3) {
          ma = max3(ma, C[1][10], C[1][11]); mb = max3(mb, C[1][12], C[1][13]); ma = max3(ma, C[1][14], C[1][15]);
          const float rm = swap_max(__builtin_fmaxf(ma, mb));
          if (__builtin_expect(__any(rm > PTHR), 0)) {
            const float dl = __builtin_fmaxf(rm + PBIAS, 0.f); mref += dl; fct = __builtin_amdgcn_exp2f(-dl); lsum *= fct; resc = true;
#pragma unroll
            for (int r = 0; r < 16; ++r) { C[0][r] -= dl; C[1][r] -= dl; negm[r] = -mref; }
          }
        }
        if constexpr (g >= 4) {
          constexpr int e0 = (32 * (g - 4)) / 12, e1 = (32 * (g - 3)) / 12;
          static_for<e1 - e0>(LAMBDA(ec) { constexpr int e = e0 + ec.value; C[e >> 4][e & 15] = __builtin_amdgcn_exp2f(C[e >> 4][e & 15]); });
          PIN(C[0]); PIN(C[1]);
        }
      }
      SBAR();
    });
    if (t < 8 && resc) {
#pragma unroll
      for (int d = 0; d < 4; ++d)
#pragma unroll
        for (int r = 0; r < 16; ++r) o[d][r] *= fct;
    }
  };
  step(IC<1>{}, sB, sA);
  step(IC<2>{}, sA, sB);
  step(IC<3>{}, sB, sA);
  step(IC<4>{}, sA, sB);
  step(IC<5>{}, sB, sA);
  step(IC<6>{}, sA, sB);
  step(IC<7>{}, sB, sA);
  step(IC<8>{}, sA, sB);
  ST(20);
  const float inv = 1.f / swap_add(lsum);
  const int qrow = qt * 256 + wid * 32 + r32;
  const __amdgpu_buffer_rsrc_t srdO = __builtin_amdgcn_make_buffer_rsrc((void*)obuf, 0, SQ * 4096 * 2, 0x00020000);
  const unsigned voffO = (((qrow >> 4) * 512 + head * 16 + hi) * 16 + (qrow & 15)) * 16;
#pragma unroll
  for (int db = 0; db < 4; ++db)
#pragma unroll
    for (int s = 0; s < 2; ++s) {
      u32x4 w;
#pragma unroll
      for (int e = 0; e < 4; ++e) w[e] = cvtpk(o[db][8 * s + 2 * e] * inv, o[db][8 * s + 2 * e + 1] * inv);
      __builtin_amdgcn_raw_buffer_store_b128(w, srdO, voffO + (4 * db + 2 * s) * 256, 0, O_AUX);
    }
  ST(21);
  ST(31);
}

DI void st16(__amdgpu_buffer_rsrc_t r, unsigned voff, u32x4 v) { __builtin_amdgcn_raw_buffer_store_b128(v, r, voff, 0, SC1); }
DI void st8(__amdgpu_buffer_rsrc_t r, unsigned voff, u32x2 v) { __builtin_amdgcn_raw_buffer_store_b64(v, r, voff, 0, SC1); }
DI u32x4 ld16c(__amdgpu_buffer_rsrc_t r, unsigned voff) { return __builtin_amdgcn_raw_buffer_load_b128(r, voff, 0, SC1); }
DI void bload16c(bf16x8& dst, unsigned voff, __amdgpu_buffer_rsrc_t srd, unsigned soff) {
  asm volatile("buffer_load_dwordx4 %0, %1, %2, %3 offen sc1" : "=v"(dst) : "v"(voff), "s"(srd), "s"(soff) : "memory");
}
template <int N> DI void wait_bar4b(bf16x8 (&b)[2][2]) {
  asm volatile("s_waitcnt vmcnt(%4)\n\ts_barrier" : "+v"(b[0][0]), "+v"(b[0][1]), "+v"(b[1][0]), "+v"(b[1][1]) : "i"(N) : "memory");
}
template <int NQB, int NT, int KS> struct Ring {
  static constexpr int KSP = 8 / NQB, SK = 2 * KSP, NSTG = (KS + SK - 1) / SK, NP = SK * NT, PA = (NP + 7) / 8, SB = PA * 8 * 1024, FR = 2 * NT;
};
template <int NQB, int NT, int KS, int KC_A> struct RingA {
  using R = Ring<NQB, NT, KS>;
  unsigned voff[R::PA]; unsigned ldsw;
  DI void init(unsigned lds0, int wid, int lane) {
    ldsw = lds0 + wid * 1024;
#pragma unroll
    for (int i = 0; i < R::PA; ++i) { const int p = i * 8 + wid, ks = p / NT, nt = p - ks * NT; voff[i] = p < R::NP ? (unsigned)(nt * KC_A * 256 + ks * 1024 + lane * 16) : 0x7ff00000u; }
  }
  template <int S, int I> DI void issue(__amdgpu_buffer_rsrc_t ra) const { dma16(ldsw + ((S % 4) * R::SB + I * 8192), voff[I], ra, S * R::SK * 1024); }
  template <int NPRE> DI void pre(__amdgpu_buffer_rsrc_t ra) const { static_for<NPRE>(LAMBDA(sc) { static_for<R::PA>(LAMBDA(ic) { this->template issue<sc.value, ic.value>(ra); }); }); }
};
template <int NQB, int NT, int KS, int KC_A, int KC_B, int NPRE, int ST0>
DI void ring_gemm(f32x4 (&acc)[NT][2], const RingA<NQB, NT, KS, KC_A>& RA, __amdgpu_buffer_rsrc_t ra, __amdgpu_buffer_rsrc_t rb, const char* lds, int wid, int lane STAMP_ARG) {
  using R = Ring<NQB, NT, KS>;
  constexpr int SK = R::SK, NSTG = R::NSTG, PA = R::PA, FR = R::FR, PD = 5;
  static_assert(NSTG >= 3 && FR > PD + 1 && (NPRE == 0 || NPRE == 4), "");
  const int kq = wid / NQB;
  bf16x8 B[3][2][2];
  const unsigned voffB = lane * 16 + 2 * kq * 1024;
  auto issueA1 = [&](auto sc, auto ic) __attribute__((always_inline)) { RA.template issue<sc.value, ic.value>(ra); };
  auto issueB1 = [&](auto sc, auto ic) __attribute__((always_inline)) {
    constexpr int s = sc.value, i = ic.value, ks = i >> 1, q = i & 1;
    bload16c(B[s % 3][ks][q], voffB, rb, q * KC_B * 256 + (s * SK + ks) * 1024);
  };
  if constexpr (NPRE == 0) static_for<PA>(LAMBDA(ic) { issueA1(IC<0>{}, ic); });
  static_for<4>(LAMBDA(ic) { issueB1(IC<0>{}, ic); });
  if constexpr (NPRE > 0) { static_for<4>(LAMBDA(ic) { issueB1(IC<1>{}, ic); }); wait_bar4b<4>(B[0]); } else wait_bar4b<0>(B[0]);
  ST(ST0);
  const LAS bf16x8* pl = (const LAS bf16x8*)lds + lane + 2 * kq * NT * 64;
  bf16x8 A[PD + 1];
  auto rd = [&](auto mc) __attribute__((always_inline)) { constexpr int M = mc.value, s = M / FR, m = M % FR; A[M % (PD + 1)] = pl[((s % 4) * PA * 8 + m) * 64]; };
  static_for<PD>(LAMBDA(jc) { rd(jc); });
  SBAR();
  static_for<NSTG>(LAMBDA(sc) {
    constexpr int s = sc.value;
    constexpr int n0 = (NPRE == 0 && s == 0) ? PA + 4 : 0;
    constexpr int sa = s + 2;
    constexpr int nA = (sa < NSTG && sa >= NPRE) ? PA : 0, nB = (s + 2 < NSTG) ? 4 : 0, NI = n0 + nA + nB, NGI = FR - PD - 1;
    constexpr bool full = (s + 1) * SK <= KS;
    static_for<FR>(LAMBDA(mc) {
      constexpr int m = mc.value, M = s * FR + m, ks = m / NT, nt = m % NT;
      if constexpr (m == FR - PD && s + 1 < NSTG) wait_bar4b<nA + nB>(B[(s + 1) % 3]);
      if constexpr (M + PD < NSTG * FR) rd(IC<M + PD>{});
      if constexpr (m < NGI) {
        constexpr int i0 = (m * NI + NGI - 1) / NGI, i1 = ((m + 1) * NI + NGI - 1) / NGI;
        static_for<i1 - i0>(LAMBDA(uc) {
          constexpr int i = i0 + uc.value;
          if constexpr (i < n0) { if constexpr (i < PA) issueA1(IC<1>{}, IC<i>{}); else issueB1(IC<1>{}, IC<i - PA>{}); }
          else if constexpr (i - n0 < nA) issueA1(IC<sa>{}, IC<i - n0>{});
          else issueB1(IC<s + 2>{}, IC<i - n0 - nA>{});
        });
      }
      if (full || s * SK + 2 * kq + ks < KS) { acc[nt][0] = mfma16(A[M % (PD + 1)], B[s % 3][ks][0], acc[nt][0]); acc[nt][1] = mfma16(A[M % (PD + 1)], B[s % 3][ks][1], acc[nt][1]); }
      SBAR();
    });
  });
}
DI u32x4 pair16(u32x2 x, u32x2 y) {
  const auto a = __builtin_amdgcn_permlane16_swap(x[0], y[0], false, false), b = __builtin_amdgcn_permlane16_swap(x[1], y[1], false, false);
  return u32x4{a[0], b[0], a[1], b[1]};
}
template <int NT> DI void pair_reduce(f32x4 (&acc)[NT][2], char* lds, int wid, int lane) {
  constexpr int H0 = (NT + 1) / 2;
  f32x4* red = (f32x4*)lds + (size_t)wid * H0 * 2 * 64 + lane;
  const f32x4* in = (const f32x4*)lds + (size_t)(wid ^ 4) * H0 * 2 * 64 + lane;
  __syncthreads();
  if (wid < 4) {
    asm volatile("; lo-send" ::: "memory");
#pragma unroll
    for (int nt = H0; nt < NT; ++nt) { red[((nt - H0) * 2) * 64] = acc[nt][0]; red[((nt - H0) * 2 + 1) * 64] = acc[nt][1]; }
    asm volatile("; lo-sent" ::: "memory");
  } else {
    asm volatile("; hi-send" ::: "memory");
#pragma unroll
    for (int nt = 0; nt < H0; ++nt) { red[(nt * 2) * 64] = acc[nt][0]; red[(nt * 2 + 1) * 64] = acc[nt][1]; }
    asm volatile("; hi-sent" ::: "memory");
  }
  __syncthreads();
  if (wid < 4) {
    asm volatile("; lo-recv" ::: "memory");
#pragma unroll
    for (int nt = 0; nt < H0; ++nt) { acc[nt][0] += in[(nt * 2) * 64]; acc[nt][1] += in[(nt * 2 + 1) * 64]; PIN(acc[nt][0]); PIN(acc[nt][1]); }
    asm volatile("; lo-done" ::: "memory");
  } else {
    asm volatile("; hi-recv" ::: "memory");
#pragma unroll
    for (int nt = H0; nt < NT; ++nt) { acc[nt][0] += in[((nt - H0) * 2) * 64]; acc[nt][1] += in[((nt - H0) * 2 + 1) * 64]; PIN(acc[nt][0]); PIN(acc[nt][1]); }
    asm volatile("; hi-done" ::: "memory");
  }
  __syncthreads();
}

DI void group_arrive(unsigned* cnt) {
  asm volatile("s_waitcnt vmcnt(0)" ::: "memory");
  __syncthreads();
  if (threadIdx.x == 0) __hip_atomic_fetch_add((gu32*)cnt, 1u, __ATOMIC_RELAXED, __HIP_MEMORY_SCOPE_AGENT);
}
DI void group_wait(unsigned* cnt, unsigned want) {
  if (threadIdx.x == 0) {
    for (unsigned spins = 0; spins < (1u << 20); ++spins) {
      if (__hip_atomic_load((gu32*)cnt, __ATOMIC_RELAXED, __HIP_MEMORY_SCOPE_AGENT) >= want) break;
      __builtin_amdgcn_s_sleep(1);
    }
  }
  asm volatile("" ::: "memory");
  __syncthreads();
}

__global__ __launch_bounds__(512, 2) void k_tail(const u16* __restrict__ obuf, const u16* __restrict__ wot, const u16* __restrict__ w1t, const u16* __restrict__ w2t,
                                                  const u16* __restrict__ x, const u16* __restrict__ mod, const u16* __restrict__ b1, const u16* __restrict__ b2,
                                                  u16* part, u16* x1, u16* h2, u16* hid, u16* __restrict__ out, unsigned* cnt STAMP_ARG) {
  ST(0);
  extern __shared__ __attribute__((aligned(16))) char lds[];
  const int tid = threadIdx.x, lane = tid & 63, wid = __builtin_amdgcn_readfirstlane(tid >> 6);
  const unsigned lds0 = (unsigned)(uintptr_t)lds;
  const int xcd = blockIdx.x & 7, idx = blockIdx.x >> 3;
  const int rt = (xcd >> 2) * 8 + (idx >> 2), gi = (xcd & 3) * 4 + (idx & 3);
  unsigned* const c1 = cnt + rt * 64, * const c2 = cnt + (16 + rt) * 64, * const c3 = cnt + (32 + rt) * 64;
  {
    const int kq = gi >> 2, j = gi & 3, qb = wid & 3;
    const __amdgpu_buffer_rsrc_t ra = __builtin_amdgcn_make_buffer_rsrc((void*)(wot + ((size_t)(10 * j) * 512 + kq * 128) * 128), 0, 10 * 512 * 256, 0x00020000);
    const __amdgpu_buffer_rsrc_t rb = __builtin_amdgcn_make_buffer_rsrc((void*)(obuf + ((size_t)(8 * rt + 2 * qb) * 512 + kq * 128) * 128), 0, 2 * 512 * 256, 0x00020000);
    const __amdgpu_buffer_rsrc_t rp = __builtin_amdgcn_make_buffer_rsrc((void*)(part + (size_t)kq * SQ * DM), 0, SQ * DM * 2, 0x00020000);
    f32x4 acc[10][2] = {};
    RingA<4, 10, 32, 512> RA; RA.init(lds0, wid, lane);
    ring_gemm<4, 10, 32, 512, 512, 0, 1>(acc, RA, ra, rb, lds, wid, lane STAMP_FWD);
    ST(2);
    pair_reduce<10>(acc, lds, wid, lane);
    ST(13);
    const int nt0 = wid < 4 ? 0 : 5;
    const unsigned vo = (((8 * rt + 2 * qb + ((lane >> 4) & 1)) * 80 + j * 20 + nt0 * 2 + (lane >> 5)) * 16 + (lane & 15)) * 16;
    auto outp = [&](auto ntc) __attribute__((always_inline)) {
      constexpr int nt = ntc.value, o = nt % 5;
      const u32x2 w0 = {cvtpk(acc[nt][0][0], acc[nt][0][1]), cvtpk(acc[nt][0][2], acc[nt][0][3])}, w1 = {cvtpk(acc[nt][1][0], acc[nt][1][1]), cvtpk(acc[nt][1][2], acc[nt][1][3])};
      st16(rp, vo + o * 2 * 256, pair16(w0, w1));
    };
    if (wid < 4) { asm volatile("; lo-out" ::: "memory"); static_for<5>(LAMBDA(c) { outp(c); }); asm volatile("; lo-end" ::: "memory"); }
    else { asm volatile("; hi-out" ::: "memory"); static_for<5>(LAMBDA(c) { outp(IC<5 + c.value>{}); }); asm volatile("; hi-end" ::: "memory"); }
  }
  ST(14);
  group_arrive(c1);
  ST(3);
  const int nft = gi < 15 ? 7 : 3;
  const __amdgpu_buffer_rsrc_t raD = __builtin_amdgcn_make_buffer_rsrc((void*)(w1t + (size_t)(7 * gi) * 80 * 128), 0, nft * 80 * 256, 0x00020000);
  RingA<4, 7, 20, 80> RD; RD.init(lds0, wid, lane);
  RD.pre<4>(raD);
  {
    float* red = (float*)(lds + 131072);
    const int r = tid & 7, row = rt * 128 + gi * 8 + r, w = tid >> 6;
    const bool two = tid < 128;
    const __amdgpu_buffer_rsrc_t rp = __builtin_amdgcn_make_buffer_rsrc((void*)part, 0, 4 * SQ * DM * 2, 0x00020000);
    const __amdgpu_buffer_rsrc_t rx1 = __builtin_amdgcn_make_buffer_rsrc((void*)x1, 0, SQ * DM * 2, 0x00020000);
    const __amdgpu_buffer_rsrc_t rh2 = __builtin_amdgcn_make_buffer_rsrc((void*)h2, 0, SQ * DM * 2, 0x00020000);
    u32x4 p[2][4], xv[2], gv[2], sbv[2], cbv[2]; unsigned blk[2];
#pragma unroll
    for (int u = 0; u < 2; ++u) {
      const int c = (u == 0 || two) ? (tid >> 3) + 64 * u : (tid >> 3);
      blk[u] = (((row >> 4) * 80 + c) * 16 + (row & 15)) * 16;
      xv[u] = *(const u32x4*)(x + (size_t)row * DM + c * 8);
      gv[u] = *(const u32x4*)(mod + 2 * 640 + c * 8); sbv[u] = *(const u32x4*)(mod + 3 * 640 + c * 8); cbv[u] = *(const u32x4*)(mod + 4 * 640 + c * 8);
    }
    group_wait(c1, 16);
    ST(4);
#pragma unroll
    for (int u = 0; u < 2; ++u)
#pragma unroll
      for (int k = 0; k < 4; ++k) p[u][k] = ld16c(rp, (u == 0 || two) ? blk[u] + k * SQ * DM * 2 : 0x7ffff000u);
    PIN(p[0][0]); PIN(p[1][3]); ST(15);
    float v[2][8]; float s = 0; u32x4 x1v[2];
#pragma unroll
    for (int u = 0; u < 2; ++u) {
#pragma unroll
      for (int e = 0; e < 4; ++e) {
        const float a = (bf_lo(p[u][0][e]) + bf_lo(p[u][1][e])) + (bf_lo(p[u][2][e]) + bf_lo(p[u][3][e])), b = (bf_hi(p[u][0][e]) + bf_hi(p[u][1][e])) + (bf_hi(p[u][2][e]) + bf_hi(p[u][3][e]));
        x1v[u][e] = cvtpk(bf_lo(xv[u][e]) + rbf(bf_lo(gv[u][e]) * rbf(a)), bf_hi(xv[u][e]) + rbf(bf_hi(gv[u][e]) * rbf(b)));
        v[u][2 * e] = bf_lo(x1v[u][e]); v[u][2 * e + 1] = bf_hi(x1v[u][e]);
        if (u == 0 || two) s += v[u][2 * e] + v[u][2 * e + 1];
      }
    }
    s = xor_add<32>(xor_add<16>(xor_add<8>(s)));
    if (lane < 8) red[w * 8 + r] = s;
    lds_barrier();
    float tot = 0;
#pragma unroll
    for (int i = 0; i < 8; ++i) tot += red[i * 8 + r];
    const float mean = tot * (1.f / DM); float q = 0;
#pragma unroll
    for (int e = 0; e < 8; ++e) { const float d = v[0][e] - mean; q += d * d; }
    if (two) {
#pragma unroll
      for (int e = 0; e < 8; ++e) { const float d = v[1][e] - mean; q += d * d; }
    }
    q = xor_add<32>(xor_add<16>(xor_add<8>(q)));
    if (lane < 8) red[64 + w * 8 + r] = q;
    lds_barrier();
    float qt = 0;
#pragma unroll
    for (int i = 0; i < 8; ++i) qt += red[64 + i * 8 + r];
    const float mb = rbf(mean), rsd = 1.f / rbf(sqrtf(rbf(rbf(qt * (1.f / DM)) + 1e-5f)));
#pragma unroll
    for (int u = 0; u < 2; ++u) {
      u32x4 hv;
#pragma unroll
      for (int e = 0; e < 4; ++e)
        hv[e] = cvtpk(rbf(rbf(rbf(v[u][2 * e] - mb) * rsd) * rbf(1.f + bf_lo(cbv[u][e]))) + bf_lo(sbv[u][e]), rbf(rbf(rbf(v[u][2 * e + 1] - mb) * rsd) * rbf(1.f + bf_hi(cbv[u][e]))) + bf_hi(sbv[u][e]));
      st16(rh2, (u == 0 || two) ? blk[u] : 0x7ffff000u, hv);
      st16(rx1, (u == 0 || two) ? blk[u] : 0x7ffff000u, x1v[u]);
    }
  }
  ST(16);
  group_arrive(c2);
  ST(5);
  const int jE = gi & 7, rtE = rt * 2 + (gi >> 3);
  const __amdgpu_buffer_rsrc_t raE = __builtin_amdgcn_make_buffer_rsrc((void*)(w2t + (size_t)(5 * jE) * 216 * 128), 0, 5 * 216 * 256, 0x00020000);
  {
    const int qb = wid & 3, nt0 = wid < 4 ? 0 : 4;
    const __amdgpu_buffer_rsrc_t rb = __builtin_amdgcn_make_buffer_rsrc((void*)(h2 + (size_t)(8 * rt + 2 * qb) * 80 * 128), 0, 2 * 80 * 256, 0x00020000);
    const __amdgpu_buffer_rsrc_t rh = __builtin_amdgcn_make_buffer_rsrc((void*)hid, 0, SQ * FF * 2, 0x00020000);
    u32x2 bv[4];
#pragma unroll
    for (int o = 0; o < 4; ++o) bv[o] = *(const u32x2*)(b1 + (nt0 + o < nft ? (7 * gi + nt0 + o) * 16 + 4 * (lane >> 4) : 0));
    f32x4 acc[7][2] = {};
    group_wait(c2, 16);
    ST(6);
    ring_gemm<4, 7, 20, 80, 80, 4, 7>(acc, RD, raD, rb, lds, wid, lane STAMP_FWD);
    ST(8);
    pair_reduce<7>(acc, lds, wid, lane);
    ST(17);
    const unsigned vo = (((8 * rt + 2 * qb + ((lane >> 4) & 1)) * 216 + (7 * gi + nt0) * 2 + (lane >> 5)) * 16 + (lane & 15)) * 16;
    auto outp = [&](auto ntc, auto oc) __attribute__((always_inline)) {
      constexpr int nt = ntc.value, o = oc.value;
      if (nt < nft) {
        const float b0 = bf_lo(bv[o][0]), b1_ = bf_hi(bv[o][0]), b2_ = bf_lo(bv[o][1]), b3 = bf_hi(bv[o][1]);
        u32x2 w[2];
#pragma unroll
        for (int q = 0; q < 2; ++q) {
          const float v0 = rbf(__builtin_fmaxf(rbf(acc[nt][q][0]) + b0, 0.f)), v1 = rbf(__builtin_fmaxf(rbf(acc[nt][q][1]) + b1_, 0.f));
          const float v2 = rbf(__builtin_fmaxf(rbf(acc[nt][q][2]) + b2_, 0.f)), v3 = rbf(__builtin_fmaxf(rbf(acc[nt][q][3]) + b3, 0.f));
          w[q] = u32x2{cvtpk(v0 * v0, v1 * v1), cvtpk(v2 * v2, v3 * v3)};
        }
        st16(rh, vo + o * 2 * 256, pair16(w[0], w[1]));
      }
    };
    if (wid < 4) { asm volatile("; lo-out" ::: "memory"); static_for<4>(LAMBDA(c) { outp(c, c); }); asm volatile("; lo-end" ::: "memory"); }
    else { asm volatile("; hi-out" ::: "memory"); static_for<3>(LAMBDA(c) { outp(IC<4 + c.value>{}, c); }); asm volatile("; hi-end" ::: "memory"); }
  }
  ST(18);
  group_arrive(c3);
  ST(9);
  RingA<2, 5, 54, 216> RE; RE.init(lds0, wid, lane);
  RE.pre<4>(raE);
  {
    const int qb = wid & 1;
    const __amdgpu_buffer_rsrc_t rb = __builtin_amdgcn_make_buffer_rsrc((void*)(hid + (size_t)(4 * rtE + 2 * qb) * 216 * 128), 0, 2 * 216 * 256, 0x00020000);
    const __amdgpu_buffer_rsrc_t rx1 = __builtin_amdgcn_make_buffer_rsrc((void*)x1, 0, SQ * DM * 2, 0x00020000);
    u32x4 xv[2], gv[2], bv[2];
#pragma unroll
    for (int it = 0; it < 2; ++it) {
      const int item = tid + it * 512, q = item & 63, c = item < 640 ? item >> 6 : 0, row = rtE * 64 + q;
      xv[it] = ld16c(rx1, (((row >> 4) * 80 + jE * 10 + c) * 16 + (row & 15)) * 16);
      gv[it] = *(const u32x4*)(mod + 5 * 640 + jE * 80 + c * 8); bv[it] = *(const u32x4*)(b2 + jE * 80 + c * 8);
    }
    f32x4 acc[5][2] = {};
    group_wait(c3, 16);
    ST(10);
    ring_gemm<2, 5, 54, 216, 216, 4, 11>(acc, RE, raE, rb, lds, wid, lane STAMP_FWD);
    ST(12);
    f32x4* red = (f32x4*)lds;
    __syncthreads();
#pragma unroll
    for (int n = 0; n < 5; ++n) { red[((wid * 5 + n) * 2) * 64 + lane] = acc[n][0]; red[((wid * 5 + n) * 2 + 1) * 64 + lane] = acc[n][1]; }
    __syncthreads();
#pragma unroll
    for (int it = 0; it < 2; ++it) {
      const int item = tid + it * 512;
      if (item < 640) {
        const int q = item & 63, c = item >> 6, n = c >> 1, l0 = (q & 15) + 32 * (c & 1), base = (((q >> 5) * 5 + n) * 2 + ((q >> 4) & 1)) * 64 + l0;
        f32x4 lo = {0, 0, 0, 0}, hi = {0, 0, 0, 0};
#pragma unroll
        for (int w = 0; w < 4; ++w) { lo += red[base + w * 2 * 5 * 2 * 64]; hi += red[base + w * 2 * 5 * 2 * 64 + 16]; }
        const float v[8] = {lo[0], lo[1], lo[2], lo[3], hi[0], hi[1], hi[2], hi[3]};
        u32x4 o;
#pragma unroll
        for (int e = 0; e < 4; ++e) {
          const float a = bf_lo(xv[it][e]) + rbf(bf_lo(gv[it][e]) * rbf(rbf(v[2 * e]) + bf_lo(bv[it][e]))), b = bf_hi(xv[it][e]) + rbf(bf_hi(gv[it][e]) * rbf(rbf(v[2 * e + 1]) + bf_hi(bv[it][e])));
          o[e] = cvtpk(a, b);
        }
        *(u32x4*)(out + (size_t)(rtE * 64 + q) * DM + jE * 80 + c * 8) = o;
      }
    }
  }
  ST(31);
}

extern "C" long long* dbg_offsets() { static long long o[5] = {(long long)WS_MOD, (long long)WS_H, (long long)WS_O, (long long)WS_X1, (long long)WS_HID}; return o; }

extern "C" void kernel_launch(void* const* d_in, const int* in_sizes, int n_in, void* d_out, int out_size, void* d_ws, size_t ws_size, hipStream_t stream) {
  const u16 *x = (const u16*)d_in[0], *kc = (const u16*)d_in[1], *vc = (const u16*)d_in[2], *wq = (const u16*)d_in[3], *wo = (const u16*)d_in[4];
  const u16 *w1 = (const u16*)d_in[6], *b1 = (const u16*)d_in[7], *w2 = (const u16*)d_in[8], *b2 = (const u16*)d_in[9], *c = (const u16*)d_in[10], *wmod = (const u16*)d_in[11];
  char* ws = (char*)d_ws;
  u16 *mod = (u16*)(ws + WS_MOD), *h = (u16*)(ws + WS_H), *o = (u16*)(ws + WS_O), *wot = (u16*)(ws + WS_WOT), *w1t = (u16*)(ws + WS_W1T), *w2t = (u16*)(ws + WS_W2T);
  u16 *x1 = (u16*)(ws + WS_X1), *hid = (u16*)(ws + WS_HID);
  u16 *h2 = (u16*)(ws + WS_H2), *part = (u16*)(ws + WS_P);
  (void)hipFuncSetAttribute((const void*)k_attn, hipFuncAttributeMaxDynamicSharedMemorySize, (NS + 2) * SLOT);
  unsigned* cnt = (unsigned*)(ws + WS_CNT);
  (void)hipFuncSetAttribute((const void*)k_tail, hipFuncAttributeMaxDynamicSharedMemorySize, 163840);
  k_front<<<FR_N, 512, 0, stream>>>(c, wmod, x, mod, h, (unsigned*)(ws + WS_SYNC), cnt STAMP_PASS(0));
  k_attn<<<256, 512, (NS + 2) * SLOT, stream>>>(h, wq, kc, vc, o, wo, w1, w2, wot, w1t, w2t STAMP_PASS(2));
  k_tail<<<256, 512, 163840, stream>>>(o, wot, w1t, w2t, x, mod, b1, b2, part, x1, h2, hid, (u16*)d_out, cnt STAMP_PASS(3));
}
```

```cpp
#include <hip/hip_runtime.h>
#include <stdint.h>
#include <utility>

typedef unsigned short u16;
typedef short bf16x8 __attribute__((ext_vector_type(8)));
typedef short s16x4 __attribute__((ext_vector_type(4)));
typedef float f32x16 __attribute__((ext_vector_type(16)));
typedef float f32x4 __attribute__((ext_vector_type(4)));
typedef float f32x2 __attribute__((ext_vector_type(2)));
typedef __bf16 bf16x2 __attribute__((ext_vector_type(2)));
typedef unsigned u32x4 __attribute__((ext_vector_type(4)));
typedef unsigned u32x2 __attribute__((ext_vector_type(2)));
#define DI __device__ __forceinline__
#define GAS __attribute__((address_space(1)))
#define LAS __attribute__((address_space(3)))
#define SBAR() __builtin_amdgcn_sched_barrier(0)
#define PIN(x) asm volatile("" : "+v"(x))

constexpr int SQ = 2048, DM = 640, NH = 32, DH = 128, SK = 2048, WIN = 512, FF = 1728;
constexpr size_t WS_MOD = 0;
constexpr size_t WS_CNT = 8192;
constexpr size_t WS_SYNC = 32768;
constexpr size_t WS_H = 131072;
constexpr size_t WS_O = WS_H + (size_t)SQ * DM * 2;
constexpr size_t WS_WOT = WS_O + (size_t)SQ * 4096 * 2;
constexpr size_t WS_W1T = WS_WOT + (size_t)4096 * DM * 2;
constexpr size_t WS_W2T = WS_W1T + (size_t)DM * FF * 2;
constexpr size_t WS_X1 = WS_W2T + (size_t)DM * FF * 2 + 65536;
constexpr size_t WS_HID = WS_X1 + (size_t)SQ * DM * 2;
constexpr size_t WS_H2 = WS_HID + (size_t)SQ * FF * 2 + 65536;
constexpr size_t WS_P = WS_H2 + (size_t)SQ * DM * 2;
constexpr size_t WS_END = WS_P + (size_t)4 * SQ * DM * 2;

#define STAMP_ARG
#define STAMP_FWD
#define STAMP_PASS(k)
#define ST(i)
#define STC(i)
template <int... I, class F> DI void static_for_impl(std::integer_sequence<int, I...>, F&& f) { (f(std::integral_constant<int, I>{}), ...); }
template <int N, class F> DI void static_for(F&& f) { static_for_impl(std::make_integer_sequence<int, N>{}, f); }
#define LAMBDA(ic) [&](auto ic) __attribute__((always_inline))

DI float bf_lo(unsigned w) { return __uint_as_float(w << 16); }
DI float bf_hi(unsigned w) { return __uint_as_float(w & 0xffff0000u); }
DI float bf2f(u16 v) { return __uint_as_float((unsigned)v << 16); }
DI unsigned cvtpk(float lo, float hi) { f32x2 v = {lo, hi}; bf16x2 b = __builtin_convertvector(v, bf16x2); return __builtin_bit_cast(unsigned, b); }
DI u16 f2bf(float x) { return (u16)(cvtpk(x, 0.f) & 0xffffu); }
DI float rbf(float x) { return bf_lo(cvtpk(x, 0.f)); }
DI f32x16 mfma32(bf16x8 a, bf16x8 b, f32x16 c) { return __builtin_amdgcn_mfma_f32_32x32x16_bf16(a, b, c, 0, 0, 0); }
DI f32x4 mfma16(bf16x8 a, bf16x8 b, f32x4 c) { return __builtin_amdgcn_mfma_f32_16x16x32_bf16(a, b, c, 0, 0, 0); }
DI float swap_max(float m) { auto r = __builtin_amdgcn_permlane32_swap(__float_as_uint(m), __float_as_uint(m), false, false); return __builtin_fmaxf(__uint_as_float(r[0]), __uint_as_float(r[1])); }
DI float swap_add(float m) { auto r = __builtin_amdgcn_permlane32_swap(__float_as_uint(m), __float_as_uint(m), false, false); return __uint_as_float(r[0]) + __uint_as_float(r[1]); }

DI void lds_barrier() { asm volatile("s_waitcnt lgkmcnt(0)\n\ts_barrier" ::: "memory"); }
template <int CTRL> DI float dpp_mov(float v) { return __builtin_bit_cast(float, __builtin_amdgcn_update_dpp(0, __builtin_bit_cast(int, v), CTRL, 0xf, 0xf, true)); }
template <int M> DI float xor_add(float v) {
  if constexpr (M == 1) return v + dpp_mov<0xB1>(v);
  else if constexpr (M == 2) return v + dpp_mov<0x4E>(v);
  else if constexpr (M == 4) return v + dpp_mov<0x141>(dpp_mov<0x1B>(v));
  else if constexpr (M == 8) return v + dpp_mov<0x128>(v);
  else if constexpr (M == 16) { const auto r = __builtin_amdgcn_permlane16_swap(__float_as_uint(v), __float_as_uint(v), false, false); return __uint_as_float(r[0]) + __uint_as_float(r[1]); }
  else { const auto r = __builtin_amdgcn_permlane32_swap(__float_as_uint(v), __float_as_uint(v), false, false); return __uint_as_float(r[0]) + __uint_as_float(r[1]); }
}
typedef GAS unsigned gu32;
typedef GAS unsigned long long gu64;
#define RLX_AGENT __ATOMIC_RELAXED, __HIP_MEMORY_SCOPE_AGENT
constexpr int FR_NP = 120, FR_NL = 128, FR_N = FR_NP + FR_NL;
constexpr unsigned SPIN_CAP = 1u << 18;
#define PRE_THR 60
#define POLL_SLEEP 4
#define FR_SLEEP 4
constexpr int SC1 = 16;
__global__ __launch_bounds__(512) void k_front(const u16* __restrict__ c, const u16* __restrict__ wmod, const u16* __restrict__ x, u16* __restrict__ mod, u16* __restrict__ h,
                                               unsigned* sync  , unsigned* __restrict__ cnt STAMP_ARG) {
  ST(0);
  __shared__ float sm[1280];
  const int t = threadIdx.x, b = blockIdx.x;
  if (b < FR_NP) {
    const int cc = t & 3, r0 = t >> 2, col0 = b * 32 + cc * 8;
    u32x4 w[5];
#pragma unroll
    for (int i = 0; i < 5; ++i) w[i] = *(const u32x4*)(wmod + (size_t)(r0 + 128 * i) * 3840 + col0);
    float* sil = sm + 512;
    const unsigned cv = *(const unsigned*)(c + 2 * (t < 320 ? t : 0));
    unsigned tag = __hip_atomic_load((gu32*)sync, RLX_AGENT) + 1u;
    if (t < 320) {
      const float v0 = bf_lo(cv), v1 = bf_hi(cv);
      const unsigned pk = cvtpk(v0 * __builtin_amdgcn_rcpf(1.f + __expf(-v0)), v1 * __builtin_amdgcn_rcpf(1.f + __expf(-v1)));
      *(f32x2*)(sil + 2 * t) = f32x2{bf_lo(pk), bf_hi(pk)};
    }
    if (b == FR_NP - 1 && t < 48) cnt[t * 64] = 0u;
    PIN(tag);
    ST(4);
    lds_barrier();
    if (t == 0) __hip_atomic_store((gu32*)sync + 64 + b, tag, RLX_AGENT);
    ST(5);
    PIN(w[4]); ST(6);
    float a[8] = {};
#pragma unroll
    for (int i = 0; i < 5; ++i) {
      const float sv = sil[r0 + 128 * i];
#pragma unroll
      for (int j = 0; j < 4; ++j) { a[2 * j] += sv * bf_lo(w[i][j]); a[2 * j + 1] += sv * bf_hi(w[i][j]); }
    }
#pragma unroll
    for (int j = 0; j < 8; ++j) a[j] = xor_add<32>(xor_add<16>(xor_add<8>(xor_add<4>(a[j]))));
    if ((t & 63) < 4) {
#pragma unroll
      for (int j = 0; j < 8; ++j) sm[(t >> 6) * 32 + cc * 8 + j] = a[j];
    }
    lds_barrier();
    ST(3);
    if (t < 128) {
      const int u = t & 15;
      float lo = 0.f, hi = 0.f;
#pragma unroll
      for (int w8 = 0; w8 < 8; ++w8) { lo += sm[w8 * 32 + 2 * u]; hi += sm[w8 * 32 + 2 * u + 1]; }
      const bool onep = (b >= 20 && b < 40) || (b >= 80 && b < 100);
      unsigned pk = cvtpk(lo, hi);
      if (onep) pk = cvtpk(1.f + bf_lo(pk), 1.f + bf_hi(pk));
      if (b < 40) __hip_atomic_store((gu64*)(sync + 2048 + (t >> 4) * 2048) + b * 16 + u, ((unsigned long long)tag << 32) | pk, RLX_AGENT);
      if (t < 16) *(unsigned*)(mod + b * 32 + 2 * u) = pk;
    }
    if (b == FR_NP - 1 && t < 64) {
      bool ok = false;
      for (unsigned spins = 0; spins < SPIN_CAP && !ok; ++spins) {
        bool all = true;
#pragma unroll
        for (int k = 0; k < 4; ++k) { const int w = t + 64 * k; if (w < FR_N) all &= __hip_atomic_load((gu32*)sync + 64 + w, RLX_AGENT) == tag; }
        ok = __all(all);
        if (!ok) __builtin_amdgcn_s_sleep(2);
      }
      if (t == 0) __hip_atomic_store((gu32*)sync, tag, RLX_AGENT);
    }
  } else {
    if (t >= 256) return;
    const int i = t & 15, row = (b - FR_NP) * 16 + (t >> 4);
    u32x4 v[5];
#pragma unroll
    for (int j = 0; j < 5; ++j) v[j] = *(const u32x4*)(x + (size_t)row * DM + (i + 16 * j) * 8);
    const unsigned tag = __hip_atomic_load((gu32*)sync, RLX_AGENT) + 1u;
    if (t == 0) __hip_atomic_store((gu32*)sync + 64 + b, tag, RLX_AGENT);
    float f[40]; float s = 0;
#pragma unroll
    for (int j = 0; j < 5; ++j)
#pragma unroll
      for (int e = 0; e < 4; ++e) { f[j * 8 + 2 * e] = bf_lo(v[j][e]); f[j * 8 + 2 * e + 1] = bf_hi(v[j][e]); s += f[j * 8 + 2 * e] + f[j * 8 + 2 * e + 1]; }
    s = xor_add<8>(xor_add<4>(xor_add<2>(xor_add<1>(s))));
    const float mean = s * (1.f / DM); float q = 0;
#pragma unroll
    for (int e = 0; e < 40; ++e) { const float d = f[e] - mean; q += d * d; }
    q = xor_add<8>(xor_add<4>(xor_add<2>(xor_add<1>(q))));
    const float mb = rbf(mean), rsd = 1.f / rbf(sqrtf(rbf(rbf(q * (1.f / DM)) + 1e-5f)));
#pragma unroll
    for (int e = 0; e < 40; e += 2) {
      const unsigned d = cvtpk(f[e] - mb, f[e + 1] - mb), n = cvtpk(bf_lo(d) * rsd, bf_hi(d) * rsd);
      f[e] = bf_lo(n); f[e + 1] = bf_hi(n);
    }
    ST(1);
    if (t < 64) {
      const __amdgpu_buffer_rsrc_t rg = __builtin_amdgcn_make_buffer_rsrc((void*)(sync + 2048 + (b & 7) * 2048), 0, 5120, 0x00020000);
      u32x4 gv[5];
      for (unsigned spins = 0; spins < SPIN_CAP; ++spins) {
        bool ok = true;
#pragma unroll
        for (int k = 0; k < 5; ++k) { gv[k] = __builtin_amdgcn_raw_buffer_load_b128(rg, (t + 64 * k) * 16, 0, SC1); ok &= gv[k][1] == tag && gv[k][3] == tag; }
        if (__all(ok)) break;
        __builtin_amdgcn_s_sleep(FR_SLEEP);
      }
#pragma unroll
      for (int k = 0; k < 5; ++k) {
        const int g = (t + 64 * k) * 2;
        *(f32x4*)(sm + 2 * g) = f32x4{bf_lo(gv[k][0]), bf_hi(gv[k][0]), bf_lo(gv[k][2]), bf_hi(gv[k][2])};
      }
    }
    ST(2);
    __syncthreads();
    const int qb = row >> 5, r32 = row & 31;
#pragma unroll
    for (int j = 0; j < 5; ++j) {
      const int k0 = (i + 16 * j) * 8;
      const f32x4 s0 = *(const f32x4*)(sm + k0), s1 = *(const f32x4*)(sm + k0 + 4), c0 = *(const f32x4*)(sm + 640 + k0), c1 = *(const f32x4*)(sm + 640 + k0 + 4);
      const float sa[8] = {s0[0], s0[1], s0[2], s0[3], s1[0], s1[1], s1[2], s1[3]}, oc[8] = {c0[0], c0[1], c0[2], c0[3], c1[0], c1[1], c1[2], c1[3]};
      u32x4 o;
#pragma unroll
      for (int e = 0; e < 4; ++e) {
        const unsigned m = cvtpk(f[j * 8 + 2 * e] * oc[2 * e], f[j * 8 + 2 * e + 1] * oc[2 * e + 1]);
        o[e] = cvtpk(bf_lo(m) + sa[2 * e], bf_hi(m) + sa[2 * e + 1]);
      }
      *(u32x4*)(h + (((size_t)qb * 80 + (i + 16 * j)) * 32 + r32) * 8) = o;
    }
  }
  ST(31);
}

#define A_NS 8
#define A_PD 5
#define O_AUX 16
constexpr int NS = A_NS, SLOT = 16384;
constexpr float QSCALE = 0.088388347648318440f * 1.4426950408889634f;
constexpr float PBIAS = 96.f;
#define PLIM_V 1073741824.f
constexpr float PLIM = PLIM_V;
constexpr int tile_K(int t) { return t == 0 ? 10 : 9 + 2 * t; }
constexpr int tile_V(int t) { return t == 7 ? 25 : 12 + 2 * t; }
constexpr int slot_of(int idx) { return (idx % NS) * SLOT; }
constexpr int lastuse(int idx) { return idx < 10 ? idx : (idx == 10 ? 10 : (idx == 25 ? 18 : ((idx & 1) ? 10 + (idx - 9) / 2 : 11 + (idx - 12) / 2))); }
constexpr int need_bar(int idx) {
  if (idx < 10) return idx == 0 ? 0 : idx - 1;
  if (idx == 10) return 9;
  if (idx == 25) return 17;
  return (idx & 1) ? 10 + (idx - 9) / 2 - 1 : 11 + (idx - 12) / 2;
}
constexpr int rt_step(int u) { return u == 0 ? 9 : (u == 1 ? 10 : 12); }
constexpr int rt_proc(int u) { return u == 0 ? 11 : (u == 1 ? 12 : 14); }
constexpr int rt_slot(int u) { return (u == 1 ? NS + 1 : NS) * SLOT; }
struct Sched { int npro; int pro[16]; int nops[19]; int op[19][16]; int wait[19]; };
constexpr Sched make_sched() {
  Sched p{}; int pos = 0; int tile_end[26] = {}; int h_end[10] = {}; int u_end[3] = {}; int next_tile = 3;
  for (int i = 0; i < 3; ++i) {
    p.pro[p.npro++] = 2 * i; p.pro[p.npro++] = 2 * i + 1; pos += 2; tile_end[i] = pos;
    if (i < 2) { for (int k = 0; k < 4; ++k) p.pro[p.npro++] = 100 + 4 * i + k; pos += 4; h_end[i] = pos; }
  }
  for (int g = 0; g < 19; ++g) {
    int need = 0;
    for (int idx = 0; idx < next_tile; ++idx) if (need_bar(idx) <= g && tile_end[idx] > need) need = tile_end[idx];
    if (g < 10 && h_end[g] > need) need = h_end[g];
    for (int u = 0; u < 3; ++u) if (rt_proc(u) <= g && u_end[u] > need) need = u_end[u];
    p.wait[g] = pos - need;
    int n = 0;
    for (int u = 0; u < 3; ++u) if (g == rt_proc(u)) { p.op[g][n++] = 300 + 2 * u; p.op[g][n++] = 301 + 2 * u; }
    if (g == 0) { for (int k = 0; k < 4; ++k) p.op[g][n++] = 100 + 4 * 2 + k; pos += 4; h_end[2] = pos; }
    const int maxt = g < 7 ? 1 : 2;
    for (int c = 0; c < maxt && next_tile < 26; ++c) {
      if (next_tile >= NS && lastuse(next_tile - NS) >= g) break;
      p.op[g][n++] = 2 * next_tile; p.op[g][n++] = 2 * next_tile + 1; pos += 2; tile_end[next_tile] = pos; ++next_tile;
    }
    if (g + 3 < 10) { for (int k = 0; k < 4; ++k) p.op[g][n++] = 100 + 4 * (g + 3) + k; pos += 4; h_end[g + 3] = pos; }
    for (int u = 0; u < 3; ++u) if (g == rt_step(u)) { p.op[g][n++] = 200 + 2 * u; p.op[g][n++] = 201 + 2 * u; pos += 2; u_end[u] = pos; }
    for (int u = 0; u < 3; ++u) if (g == rt_proc(u)) { p.op[g][n++] = 310 + 2 * u; p.op[g][n++] = 311 + 2 * u; pos += 2; }
    p.nops[g] = n;
  }
  for (int idx = 0; idx < 26; ++idx) if (tile_end[idx] == 0) p.wait[0] = -1;
  return p;
}
constexpr Sched SCH = make_sched();
static_assert(SCH.wait[0] >= 0, "schedule incomplete");
constexpr bool sched_ok() {
  int step_of[26] = {}; for (int i = 0; i < 26; ++i) step_of[i] = -1;
  for (int g = 0; g < 19; ++g) for (int j = 0; j < SCH.nops[g]; ++j) if (SCH.op[g][j] < 100) step_of[SCH.op[g][j] >> 1] = g;
  for (int i = 3; i < 26; ++i) if (step_of[i] >= need_bar(i)) return false;
  return true;
}
static_assert(sched_ok(), "a tile is issued too late");

DI void dma16(unsigned ldsaddr, unsigned voff, __amdgpu_buffer_rsrc_t srd, unsigned soff) {
  asm volatile("s_mov_b32 m0, %0\n\ts_nop 0\n\tbuffer_load_dwordx4 %1, %2, %3 offen lds" ::"s"(ldsaddr), "v"(voff), "s"(srd), "s"(soff) : "memory");
}
template <int OFF> DI void bload16(bf16x8& dst, unsigned voff, __amdgpu_buffer_rsrc_t srd, unsigned soff) {
  asm volatile("buffer_load_dwordx4 %0, %1, %2, %3 offen offset:%4" : "=v"(dst) : "v"(voff), "s"(srd), "s"(soff), "i"(OFF) : "memory");
}
typedef LAS const char* lds_cptr;
typedef short v4i16_t __attribute__((ext_vector_type(4)));
DI s16x4 vtr(lds_cptr p) { return __builtin_bit_cast(s16x4, __builtin_amdgcn_ds_read_tr16_b64_v4i16((LAS v4i16_t*)p)); }
DI bf16x8 trfrag(lds_cptr p0, lds_cptr p1) { const s16x4 lo = vtr(p0), hi = vtr(p1); return __builtin_shufflevector(lo, hi, 0, 1, 2, 3, 4, 5, 6, 7); }
DI bf16x8 rowfrag(lds_cptr p) { return *(LAS const bf16x8*)p; }
DI float max3(float a, float b, float c) { return __builtin_fmaxf(__builtin_fmaxf(a, b), c); }

template <int N> DI void wait_bar4(bf16x8 (&b)[4]) {
  asm volatile("s_waitcnt vmcnt(%4)\n\ts_barrier" : "+v"(b[0]), "+v"(b[1]), "+v"(b[2]), "+v"(b[3]) : "i"(N) : "memory");
}
template <int N> DI void wait_bar() { asm volatile("s_waitcnt vmcnt(%0)\n\ts_barrier" ::"i"(N) : "memory"); }
template <int V> using IC = std::integral_constant<int, V>;

__global__ __launch_bounds__(512, 2) void k_attn(const u16* __restrict__ hbuf, const u16* __restrict__ wq, const u16* __restrict__ kc, const u16* __restrict__ vc, u16* __restrict__ obuf,
    const u16* __restrict__ wo, const u16* __restrict__ w1, const u16* __restrict__ w2, u16* __restrict__ wot STAMP_ARG) {
  ST(0);
  extern __shared__ __attribute__((aligned(16))) char lds[];
  const int tid = threadIdx.x, lane = tid & 63, r32 = lane & 31, hi = lane >> 5;
  const int wid = __builtin_amdgcn_readfirstlane(tid >> 6);
  const int xcd = blockIdx.x & 7, idx = blockIdx.x >> 3;
  const int head = (xcd & 3) * 8 + (idx & 7), qt = (xcd >> 2) * 4 + (idx >> 3);
  const unsigned lds0 = (unsigned)(uintptr_t)lds;
  const __amdgpu_buffer_rsrc_t srdW = __builtin_amdgcn_make_buffer_rsrc((void*)(wq + head * DH), 0, (DM - 1) * 8192 + 256, 0x00020000);
  const __amdgpu_buffer_rsrc_t srdK = __builtin_amdgcn_make_buffer_rsrc((void*)(kc + ((size_t)head * SK + (SK - WIN)) * DH), 0, WIN * 256, 0x00020000);
  const __amdgpu_buffer_rsrc_t srdV = __builtin_amdgcn_make_buffer_rsrc((void*)(vc + ((size_t)head * SK + (SK - WIN)) * DH), 0, WIN * 256, 0x00020000);
  const __amdgpu_buffer_rsrc_t srdH = __builtin_amdgcn_make_buffer_rsrc((void*)(hbuf + (size_t)(qt * 8 + wid) * 80 * 256), 0, 80 * 512, 0x00020000);
  const int dr = (lane >> 2) & 7, dx = lane & 3, dc = lane >> 5, dz = (2 * wid + (dr >> 2)) & 3;
  const unsigned voffW = (unsigned)((8 * wid + dr) * 8192 + 64 * dc + 16 * (dx ^ dz));
  const unsigned voffKV = (unsigned)((8 * wid + dr) * 256 + 64 * dc + 16 * (dx ^ dz));
  const unsigned ldsw = lds0 + 2048 * wid;
  const unsigned voffH = lane * 16;
  bf16x8 hf[10][4], rtf[2];
  __amdgpu_buffer_rsrc_t rsrc_u[3]; unsigned pitch_u[3], dst_u[3];
  const __amdgpu_buffer_rsrc_t srdT = __builtin_amdgcn_make_buffer_rsrc((void*)wot, 0, (int)(WS_W2T - WS_WOT) + DM * FF * 2, 0x00020000);
  auto vmem_op = [&](auto oc) __attribute__((always_inline)) {
    constexpr int op = oc.value;
    if constexpr (op >= 310) {
      constexpr int u = (op - 310) >> 1, ks = op & 1;
      __builtin_amdgcn_raw_buffer_store_b128(__builtin_bit_cast(u32x4, rtf[ks]), srdT, lane * 16 + ks * 1024, dst_u[u], O_AUX);
    } else if constexpr (op >= 300) {
      constexpr int u = (op - 300) >> 1, ks = op & 1;
      const int g4 = lane >> 4, chq = 2 * wid + ((lane & 3) >> 1), q4_ = (lane & 15) >> 2;
      const lds_cptr p0 = (lds_cptr)lds + rt_slot(u) + 2048 * (4 * ks + g4) + 512 * (chq >> 2) + 64 * q4_ + 16 * ((chq & 3) ^ ((2 * g4) & 3)) + 8 * (lane & 1);
      const lds_cptr p1 = (lds_cptr)lds + rt_slot(u) + 2048 * (4 * ks + g4) + 512 * (chq >> 2) + 64 * (4 + q4_) + 16 * ((chq & 3) ^ ((2 * g4 + 1) & 3)) + 8 * (lane & 1);
      rtf[ks] = trfrag(p0, p1);
    } else if constexpr (op >= 200) {
      constexpr int u = (op - 200) >> 1, half = op & 1;
      dma16(ldsw + rt_slot(u) + 1024 * half, (unsigned)(8 * wid + dr) * pitch_u[u] + (unsigned)(64 * dc + 16 * (dx ^ dz)), rsrc_u[u], 128 * half);
    } else if constexpr (op >= 100) { constexpr int i = (op - 100) >> 2, k = (op - 100) & 3; bload16<1024 * k>(hf[i][k], voffH, srdH, i * 4096); }
    else {
      constexpr int i = op >> 1, half = op & 1; constexpr unsigned so = slot_of(i) + 1024 * half;
      if constexpr (i < 10) dma16(ldsw + so, voffW, srdW, i * 64 * 8192 + 128 * half);
      else {
        constexpr bool isK = (i == 10) || (i != 25 && (i & 1)); constexpr int t = i == 10 ? 0 : (i == 25 ? 7 : (isK ? (i - 9) / 2 : (i - 12) / 2));
        dma16(ldsw + so, voffKV, isK ? srdK : srdV, t * 16384 + 128 * half);
      }
    }
  };
  static_for<SCH.npro>(LAMBDA(jc) { vmem_op(IC<SCH.pro[jc.value]>{}); });
  SBAR();
#pragma unroll
  for (int u = 0; u < 3; ++u) {
    const int id = blockIdx.x + 256 * u;
    const u16* src; int pitch, rows, kt, nt, kc, nsub; unsigned dbase;
    if (id < 320) { src = wo; pitch = 1280; rows = 4096; kt = id / 5; nt = id - kt * 5; kc = 512; nsub = 40; dbase = 0; }
    else if (id < 455) { const int v = id - 320; src = w2; pitch = 1280; rows = 1728; kt = v / 5; nt = v - kt * 5; kc = 216; nsub = 40; dbase = (unsigned)(WS_W2T - WS_WOT); }
    else { const int v = id < 595 ? id - 455 : 0; src = w1; pitch = 3456; rows = id < 595 ? 640 : 0; kt = v / 14; nt = v - kt * 14; kc = 80; nsub = id < 595 ? 108 : 0; dbase = (unsigned)(WS_W1T - WS_WOT); }
    const int first = kt * 64 * pitch + nt * 256;
    rsrc_u[u] = __builtin_amdgcn_make_buffer_rsrc((void*)((const char*)src + first), 0, rows ? rows * pitch - first : 0, 0x00020000);
    pitch_u[u] = pitch;
    dst_u[u] = nt * 8 + wid < nsub ? dbase + ((nt * 8 + wid) * kc + kt * 8) * 256 : 0x7ff00000u;
  }
  auto vmem_gap = [&](auto gc, auto jc, auto nc) __attribute__((always_inline)) {
    constexpr int g = gc.value, j = jc.value, n = nc.value, no = SCH.nops[g];
    constexpr int i0 = (j * no + n - 1) / n, i1 = ((j + 1) * no + n - 1) / n;
    static_for<i1 - i0>(LAMBDA(uc) { vmem_op(IC<SCH.op[g][i0 + uc.value]>{}); });
  };

  const int blk = (lane >> 4) & 1, q4 = (lane & 15) >> 2, p4 = lane & 3;
  constexpr int NR = (NS * SLOT + 65535) / 65536;
  const lds_cptr L = (lds_cptr)lds;
  lds_cptr wb[NR][2], vb[NR][2], kb_[NR][2];
#pragma unroll
  for (int r = 0; r < NR; ++r)
#pragma unroll
    for (int t = 0; t < 2; ++t) {
      wb[r][t] = L + 65536 * r + 2048 * hi + 64 * (4 * t + q4) + 16 * ((2 * blk + (p4 & 1)) ^ (2 * hi + t)) + 8 * (p4 >> 1);
      vb[r][t] = L + 65536 * r + 64 * (4 * hi + q4) + 16 * ((2 * blk + (p4 & 1)) ^ (2 * t + hi)) + 8 * (p4 >> 1) + 2048 * t;
      kb_[r][t] = L + 65536 * r + 2048 * (r32 >> 3) + 64 * (r32 & 7) + 16 * ((2 * t + hi) ^ ((r32 >> 2) & 3));
      PIN(wb[r][t]);
    }
  constexpr int PD = A_PD, NG = 416;
  bf16x8 fr[PD + 1];
  auto ldfrag_g = [&](auto Gc) __attribute__((always_inline)) {
    constexpr int G = Gc.value;
    if constexpr (G < NG) {
      if constexpr (G < 160) {
        constexpr int i = G >> 4, ks = (G >> 2) & 3, db = G & 3, o = slot_of(i) + 4096 * ks + 512 * db;
        fr[G % (PD + 1)] = trfrag(wb[o >> 16][0] + (o & 65535), wb[o >> 16][1] + (o & 65535));
      } else {
        constexpr int u = G - 176, t = G < 176 ? 0 : (G >= 400 ? 8 : 1 + u / 32), g = G < 176 ? G - 160 : (G >= 400 ? G - 400 + 16 : u % 32);
        if constexpr (g < 16) {
          constexpr int kb = g >> 3, s = g & 7, o = slot_of(tile_K(t)) + 8192 * kb + 512 * (s >> 1);
          fr[G % (PD + 1)] = rowfrag(kb_[o >> 16][s & 1] + (o & 65535));
        } else {
          constexpr int gg = g - 16, ks = t == 8 ? (gg & 3) : (gg >> 2), db = t == 8 ? (gg >> 2) : (gg & 3), o = slot_of(tile_V(t - 1)) + 4096 * ks + 512 * db;
          fr[G % (PD + 1)] = trfrag(vb[o >> 16][0] + (o & 65535), vb[o >> 16][1] + (o & 65535));
        }
      }
    }
  };
  auto barrier = [&](auto gc) __attribute__((always_inline)) {
    constexpr int g = gc.value;
    if constexpr (g < 10) wait_bar4<SCH.wait[g]>(hf[g]); else wait_bar<SCH.wait[g]>();
    ST(1 + g);
    if constexpr (g == 0) STC(28);
    if constexpr (g == 10) STC(29);
    if constexpr (g == 18) STC(30);
    SBAR();
  };

  f32x16 acc[4] = {};
  static_for<10>(LAMBDA(ic) {
    constexpr int i = ic.value;
    barrier(ic);
    if constexpr (i == 0) { static_for<PD>(LAMBDA(jc) { ldfrag_g(jc); }); SBAR(); }
    static_for<16>(LAMBDA(mc) {
      constexpr int m = mc.value, G = 16 * i + m;
      ldfrag_g(IC<G + PD>{});
      vmem_gap(ic, mc, IC<16>{});
      acc[m & 3] = mfma32(fr[G % (PD + 1)], hf[i][m >> 2], acc[m & 3]);
      SBAR();
    });
  });
  bf16x8 qf[8];
#pragma unroll
  for (int s = 0; s < 8; ++s) {
    u32x4 w;
#pragma unroll
    for (int e = 0; e < 4; ++e) w[e] = cvtpk(acc[s >> 1][8 * (s & 1) + 2 * e] * QSCALE, acc[s >> 1][8 * (s & 1) + 2 * e + 1] * QSCALE);
    qf[s] = __builtin_bit_cast(bf16x8, w);
  }
#pragma unroll
  for (int r = 0; r < NR; ++r)
#pragma unroll
    for (int t = 0; t < 2; ++t) { PIN(vb[r][t]); PIN(kb_[r][t]); }

  const int qrow = qt * 256 + wid * 32 + r32;
  const __amdgpu_buffer_rsrc_t srdO = __builtin_amdgcn_make_buffer_rsrc((void*)obuf, 0, SQ * 4096 * 2, 0x00020000);
  const unsigned voffO = (((qrow >> 4) * 512 + head * 16 + hi) * 16 + (qrow & 15)) * 16;
  f32x16 o[4] = {}; f32x16 sA[2], sB[2], negm; u32x4 pw[4];
  float mref, lsum = 0.f; bool bad = false;
  barrier(IC<10>{});
  {
    const f32x16 zero = {};
    static_for<16>(LAMBDA(gc) {
      constexpr int g = gc.value, G = 160 + g;
      ldfrag_g(IC<G + PD>{});
      vmem_gap(IC<10>{}, gc, IC<16>{});
      sA[g >> 3] = mfma32(fr[G % (PD + 1)], qf[g & 7], (g & 7) == 0 ? zero : sA[g >> 3]);
      SBAR();
    });
    float a = max3(sA[0][0], sA[0][1], sA[1][0]), b = max3(sA[0][2], sA[0][3], sA[1][1]); a = max3(a, sA[1][2], sA[1][3]);
#pragma unroll
    for (int r = 4; r < 16; r += 4) { a = max3(a, sA[0][r], sA[0][r + 1]); b = max3(b, sA[0][r + 2], sA[0][r + 3]); a = max3(a, sA[1][r], sA[1][r + 1]); b = max3(b, sA[1][r + 2], sA[1][r + 3]); }
    mref = swap_max(__builtin_fmaxf(a, b)) + PBIAS;
#pragma unroll
    for (int r = 0; r < 16; ++r) { sA[0][r] = __builtin_amdgcn_exp2f(sA[0][r] - mref); sA[1][r] = __builtin_amdgcn_exp2f(sA[1][r] - mref); negm[r] = -mref; }
    PIN(negm);
    SBAR();
  }
  auto step = [&](auto tc, f32x16(&C)[2], f32x16(&P)[2]) __attribute__((always_inline)) {
    constexpr int t = tc.value;
    constexpr int G0 = 176 + 32 * (t - 1), GB = t < 8 ? G0 + 16 : 400;
    barrier(IC<10 + t>{});
    float sacc = 0.f;
    static_for<16>(LAMBDA(gc) {
      constexpr int g = gc.value;
      if constexpr (t < 8) {
        ldfrag_g(IC<G0 + g + PD>{});
        vmem_gap(IC<10 + t>{}, gc, IC<32>{});
        C[g >> 3] = mfma32(fr[(G0 + g) % (PD + 1)], qf[g & 7], (g & 7) == 0 ? negm : C[g >> 3]);
      }
      constexpr int ks = g >> 2, w = g & 3, e = 8 * (ks & 1) + 2 * w;
      pw[ks][w] = cvtpk(P[ks >> 1][e], P[ks >> 1][e + 1]);
      sacc += P[ks >> 1][e]; sacc += P[ks >> 1][e + 1];
      PIN(pw[ks]); PIN(sacc);
      SBAR();
    });
    bad |= !(sacc <= PLIM);
    lsum += sacc;
    if constexpr (t < 8) {
      static_for<16>(LAMBDA(gc) {
        constexpr int g = gc.value;
        ldfrag_g(IC<GB + g + PD>{});
        vmem_gap(IC<10 + t>{}, IC<16 + g>{}, IC<32>{});
        o[g & 3] = mfma32(fr[(GB + g) % (PD + 1)], __builtin_bit_cast(bf16x8, pw[g >> 2]), o[g & 3]);
        C[g >> 3][(2 * g) & 15] = __builtin_amdgcn_exp2f(C[g >> 3][(2 * g) & 15]); C[g >> 3][(2 * g + 1) & 15] = __builtin_amdgcn_exp2f(C[g >> 3][(2 * g + 1) & 15]);
        PIN(C[g >> 3]);
        SBAR();
      });
    }
  };
  auto out_db = [&](auto dc, float inv) __attribute__((always_inline)) {
    constexpr int db = dc.value;
#pragma unroll
    for (int s = 0; s < 2; ++s) {
      u32x4 w;
#pragma unroll
      for (int e = 0; e < 4; ++e) w[e] = cvtpk(o[db][8 * s + 2 * e] * inv, o[db][8 * s + 2 * e + 1] * inv);
      __builtin_amdgcn_raw_buffer_store_b128(w, srdO, voffO + (4 * db + 2 * s) * 256, 0, O_AUX);
    }
  };
  step(IC<1>{}, sB, sA);
  step(IC<2>{}, sA, sB);
  step(IC<3>{}, sB, sA);
  step(IC<4>{}, sA, sB);
  step(IC<5>{}, sB, sA);
  step(IC<6>{}, sA, sB);
  step(IC<7>{}, sB, sA);
  step(IC<8>{}, sA, sB);
  if (__builtin_expect(!__any(bad), 1)) {
    const float inv = 1.f / swap_add(lsum);
    static_for<16>(LAMBDA(gc) {
      constexpr int g = gc.value;
      ldfrag_g(IC<400 + g + PD>{});
      o[g >> 2] = mfma32(fr[(400 + g) % (PD + 1)], __builtin_bit_cast(bf16x8, pw[g & 3]), o[g >> 2]);
      if constexpr (g >= 5 && (g & 3) == 1) out_db(IC<(g >> 2) - 1>{}, inv);
      SBAR();
    });
    out_db(IC<3>{}, inv);
  } else {
    const unsigned reg = (unsigned)((wid == 0 ? 0 : wid + 1) * SLOT);
    static_assert(slot_of(tile_V(7)) == SLOT, "");
    const lds_cptr kbs[2] = {kb_[0][0] + reg, kb_[0][1] + reg}, vbs[2] = {vb[0][0] + reg + 8192, vb[0][1] + reg + 8192};
    float m = -__builtin_inff(), l = 0.f;
#pragma unroll
    for (int d = 0; d < 4; ++d) o[d] = f32x16{};
#pragma unroll 1
    for (int kb = 0; kb < WIN / 32; ++kb) {
#pragma unroll
      for (int R = 0; R < 4; ++R) {
        const unsigned vo = (unsigned)((8 * R + dr) * 256 + 64 * dc + 16 * (dx ^ ((2 * R + (dr >> 2)) & 3)));
        const unsigned so = __builtin_amdgcn_readfirstlane(kb * 8192), ld = __builtin_amdgcn_readfirstlane(lds0 + reg + 2048 * R);
        asm volatile("s_nop 4" ::: "memory");
        dma16(ld, vo, srdK, so); dma16(ld + 1024, vo + 128, srdK, so); dma16(ld + 8192, vo, srdV, so); dma16(ld + 9216, vo + 128, srdV, so);
      }
      asm volatile("s_waitcnt vmcnt(0)" ::: "memory");
      f32x16 sc = {};
#pragma unroll
      for (int s = 0; s < 8; ++s) { sc = mfma32(rowfrag(kbs[s & 1] + 512 * (s >> 1)), qf[s], sc); SBAR(); }
      float mx = sc[0];
#pragma unroll
      for (int r = 1; r < 16; ++r) mx = __builtin_fmaxf(mx, sc[r]);
      const float mn = __builtin_fmaxf(m, swap_max(mx)), al = __builtin_amdgcn_exp2f(m - mn);
      m = mn; l *= al;
#pragma unroll
      for (int r = 0; r < 16; ++r) { sc[r] = __builtin_amdgcn_exp2f(sc[r] - mn); l += sc[r]; }
#pragma unroll
      for (int d = 0; d < 4; ++d)
#pragma unroll
        for (int r = 0; r < 16; ++r) o[d][r] *= al;
#pragma unroll
      for (int ks = 0; ks < 2; ++ks) {
        const u32x4 pk = {cvtpk(sc[8 * ks], sc[8 * ks + 1]), cvtpk(sc[8 * ks + 2], sc[8 * ks + 3]), cvtpk(sc[8 * ks + 4], sc[8 * ks + 5]), cvtpk(sc[8 * ks + 6], sc[8 * ks + 7])};
#pragma unroll
        for (int d = 0; d < 4; ++d) { o[d] = mfma32(trfrag(vbs[0] + 4096 * ks + 512 * d, vbs[1] + 4096 * ks + 512 * d), __builtin_bit_cast(bf16x8, pk), o[d]); SBAR(); }
      }
      asm volatile("s_waitcnt lgkmcnt(0)" ::: "memory");
    }
    const float inv = 1.f / swap_add(l);
    static_for<4>(LAMBDA(dc_) { out_db(dc_, inv); });
  }
  ST(21);
  ST(31);
}

DI void st16(__amdgpu_buffer_rsrc_t r, unsigned voff, u32x4 v) { __builtin_amdgcn_raw_buffer_store_b128(v, r, voff, 0, SC1); }
DI void st8(__amdgpu_buffer_rsrc_t r, unsigned voff, u32x2 v) { __builtin_amdgcn_raw_buffer_store_b64(v, r, voff, 0, SC1); }
DI u32x4 ld16c(__amdgpu_buffer_rsrc_t r, unsigned voff) { return __builtin_amdgcn_raw_buffer_load_b128(r, voff, 0, SC1); }
DI void bload16c(bf16x8& dst, unsigned voff, __amdgpu_buffer_rsrc_t srd, unsigned soff) {
  asm volatile("buffer_load_dwordx4 %0, %1, %2, %3 offen sc1" : "=v"(dst) : "v"(voff), "s"(srd), "s"(soff) : "memory");
}
template <int N> DI void wait_bar4b(bf16x8 (&b)[2][2]) {
  asm volatile("s_waitcnt vmcnt(%4)\n\ts_barrier" : "+v"(b[0][0]), "+v"(b[0][1]), "+v"(b[1][0]), "+v"(b[1][1]) : "i"(N) : "memory");
}
template <int NQB, int NT, int KS> struct Ring {
  static constexpr int KSP = 8 / NQB, SK = 2 * KSP, NSTG = (KS + SK - 1) / SK, NP = SK * NT, PA = (NP + 7) / 8, SB = PA * 8 * 1024, FR = 2 * NT;
};
template <int NQB, int NT, int KS, int KC_A, int NSLOT> struct RingA {
  using R = Ring<NQB, NT, KS>;
  static constexpr int NS_ = NSLOT;
  unsigned voff[R::PA]; unsigned ldsw;
  DI void init(unsigned lds0, int wid, int lane) {
    ldsw = lds0 + wid * 1024;
#pragma unroll
    for (int i = 0; i < R::PA; ++i) { const int p = i * 8 + wid, ks = p / NT, nt = p - ks * NT; voff[i] = p < R::NP ? (unsigned)(nt * KC_A * 256 + ks * 1024 + lane * 16) : 0x7ff00000u; }
  }
  template <int S, int I> DI void issue(__amdgpu_buffer_rsrc_t ra) const { dma16(ldsw + ((S % NSLOT) * R::SB + I * 8192), voff[I], ra, S * R::SK * 1024); }
  template <int S0, int S1> DI void pre(__amdgpu_buffer_rsrc_t ra) const { static_for<S1 - S0>(LAMBDA(sc) { static_for<R::PA>(LAMBDA(ic) { this->template issue<S0 + sc.value, ic.value>(ra); }); }); }
};
template <int NQB, int NT, int KS, int KC_A, int KC_B, int NPRE, int NSLOT, int NHOOK, int ST0, class HOOK>
DI void ring_gemm(f32x4 (&acc)[NT][2], const RingA<NQB, NT, KS, KC_A, NSLOT>& RA, __amdgpu_buffer_rsrc_t ra, __amdgpu_buffer_rsrc_t rb, const char* lds, int wid, int lane, HOOK&& hook STAMP_ARG) {
  using R = Ring<NQB, NT, KS>;
  constexpr int SK = R::SK, NSTG = R::NSTG, PA = R::PA, FR = R::FR, PD = 5;
  static_assert(NSTG >= 3 && FR > PD + 1 && (NPRE == 0 || NPRE >= 2), "");
  const int kq = wid / NQB;
  bf16x8 B[3][2][2];
  const unsigned voffB = lane * 16 + 2 * kq * 1024;
  auto issueA1 = [&](auto sc, auto ic) __attribute__((always_inline)) { RA.template issue<sc.value, ic.value>(ra); };
  auto issueB1 = [&](auto sc, auto ic) __attribute__((always_inline)) {
    constexpr int s = sc.value, i = ic.value, ks = i >> 1, q = i & 1;
    bload16c(B[s % 3][ks][q], voffB, rb, q * KC_B * 256 + (s * SK + ks) * 1024);
  };
  if constexpr (NPRE == 0) static_for<PA>(LAMBDA(ic) { issueA1(IC<0>{}, ic); });
  static_for<4>(LAMBDA(ic) { issueB1(IC<0>{}, ic); });
  if constexpr (NPRE > 0) { static_for<4>(LAMBDA(ic) { issueB1(IC<1>{}, ic); }); wait_bar4b<4>(B[0]); } else wait_bar4b<0>(B[0]);
  ST(ST0);
  const LAS bf16x8* pl = (const LAS bf16x8*)lds + lane + 2 * kq * NT * 64;
  bf16x8 A[PD + 1];
  auto rd = [&](auto mc) __attribute__((always_inline)) { constexpr int M = mc.value, s = M / FR, m = M % FR; A[M % (PD + 1)] = pl[((s % NSLOT) * PA * 8 + m) * 64]; };
  static_for<PD>(LAMBDA(jc) { rd(jc); });
  SBAR();
  static_for<NSTG>(LAMBDA(sc) {
    constexpr int s = sc.value;
    constexpr int n0 = (NPRE == 0 && s == 0) ? PA + 4 : 0;
    constexpr int sa = s + 2;
    constexpr int nA = (sa < NSTG && sa >= NPRE) ? PA : 0, nB = (s + 2 < NSTG) ? 4 : 0, NI = n0 + nA + nB, NGI = FR - PD - 1;
    constexpr bool full = (s + 1) * SK <= KS;
    static_for<FR>(LAMBDA(mc) {
      constexpr int m = mc.value, M = s * FR + m, ks = m / NT, nt = m % NT;
      if constexpr (m == FR - PD && s + 1 < NSTG) wait_bar4b<nA + nB>(B[(s + 1) % 3]);
      if constexpr (M + PD < NSTG * FR) rd(IC<M + PD>{});
      if constexpr (m < NGI) {
        constexpr int i0 = (m * NI + NGI - 1) / NGI, i1 = ((m + 1) * NI + NGI - 1) / NGI;
        static_for<i1 - i0>(LAMBDA(uc) {
          constexpr int i = i0 + uc.value;
          if constexpr (i < n0) { if constexpr (i < PA) issueA1(IC<1>{}, IC<i>{}); else issueB1(IC<1>{}, IC<i - PA>{}); }
          else if constexpr (i - n0 < nA) issueA1(IC<sa>{}, IC<i - n0>{});
          else issueB1(IC<s + 2>{}, IC<i - n0 - nA>{});
        });
      }
      if constexpr (NHOOK > 0 && M >= (NSTG - 1) * FR - PD) {
        constexpr int j = M - ((NSTG - 1) * FR - PD), HG = FR + PD - 2, i0 = (j * NHOOK + HG - 1) / HG, i1 = ((j + 1) * NHOOK + HG - 1) / HG;
        if constexpr (j < HG) static_for<i1 - i0>(LAMBDA(uc) { hook(IC<i0 + uc.value>{}); });
      }
      if (full || s * SK + 2 * kq + ks < KS) { acc[nt][0] = mfma16(A[M % (PD + 1)], B[s % 3][ks][0], acc[nt][0]); acc[nt][1] = mfma16(A[M % (PD + 1)], B[s % 3][ks][1], acc[nt][1]); }
      SBAR();
    });
  });
}
DI u32x4 pair16(u32x2 x, u32x2 y) {
  const auto a = __builtin_amdgcn_permlane16_swap(x[0], y[0], false, false), b = __builtin_amdgcn_permlane16_swap(x[1], y[1], false, false);
  return u32x4{a[0], b[0], a[1], b[1]};
}
template <int NT> DI void pair_reduce(f32x4 (&acc)[NT][2], char* lds, int wid, int lane) {
  constexpr int H0 = (NT + 1) / 2;
  f32x4* red = (f32x4*)lds + (size_t)wid * H0 * 2 * 64 + lane;
  const f32x4* in = (const f32x4*)lds + (size_t)(wid ^ 4) * H0 * 2 * 64 + lane;
  lds_barrier();
  if (wid < 4) {
    asm volatile("; lo-send" ::: "memory");
#pragma unroll
    for (int nt = H0; nt < NT; ++nt) { red[((nt - H0) * 2) * 64] = acc[nt][0]; red[((nt - H0) * 2 + 1) * 64] = acc[nt][1]; }
    asm volatile("; lo-sent" ::: "memory");
  } else {
    asm volatile("; hi-send" ::: "memory");
#pragma unroll
    for (int nt = 0; nt < H0; ++nt) { red[(nt * 2) * 64] = acc[nt][0]; red[(nt * 2 + 1) * 64] = acc[nt][1]; }
    asm volatile("; hi-sent" ::: "memory");
  }
  lds_barrier();
  if (wid < 4) {
    asm volatile("; lo-recv" ::: "memory");
#pragma unroll
    for (int nt = 0; nt < H0; ++nt) { acc[nt][0] += in[(nt * 2) * 64]; acc[nt][1] += in[(nt * 2 + 1) * 64]; PIN(acc[nt][0]); PIN(acc[nt][1]); }
    asm volatile("; lo-done" ::: "memory");
  } else {
    asm volatile("; hi-recv" ::: "memory");
#pragma unroll
    for (int nt = H0; nt < NT; ++nt) { acc[nt][0] += in[((nt - H0) * 2) * 64]; acc[nt][1] += in[((nt - H0) * 2 + 1) * 64]; PIN(acc[nt][0]); PIN(acc[nt][1]); }
    asm volatile("; hi-done" ::: "memory");
  }
}

DI void group_arrive(unsigned* cnt) {
  asm volatile("s_waitcnt vmcnt(0)" ::: "memory");
  __syncthreads();
  if (threadIdx.x == 0) __hip_atomic_fetch_add((gu32*)cnt, 1u, __ATOMIC_RELAXED, __HIP_MEMORY_SCOPE_AGENT);
}
DI void group_wait(unsigned* cnt, unsigned want) {
  if (threadIdx.x == 0) {
    gu32* p = (gu32*)cnt;
    unsigned a = __hip_atomic_load(p, RLX_AGENT); __builtin_amdgcn_s_sleep(POLL_SLEEP);
    unsigned b = __hip_atomic_load(p, RLX_AGENT); __builtin_amdgcn_s_sleep(POLL_SLEEP);
    unsigned c = __hip_atomic_load(p, RLX_AGENT); __builtin_amdgcn_s_sleep(POLL_SLEEP);
    unsigned d = __hip_atomic_load(p, RLX_AGENT);
    for (unsigned spins = 0; spins < SPIN_CAP; ++spins) {
      if (a >= want) break;
      a = __hip_atomic_load(p, RLX_AGENT); __builtin_amdgcn_s_sleep(POLL_SLEEP);
      if (b >= want) break;
      b = __hip_atomic_load(p, RLX_AGENT); __builtin_amdgcn_s_sleep(POLL_SLEEP);
      if (c >= want) break;
      c = __hip_atomic_load(p, RLX_AGENT); __builtin_amdgcn_s_sleep(POLL_SLEEP);
      if (d >= want) break;
      d = __hip_atomic_load(p, RLX_AGENT); __builtin_amdgcn_s_sleep(POLL_SLEEP);
    }
  }
  asm volatile("" ::: "memory");
  __syncthreads();
}

__global__ __launch_bounds__(512, 2) void k_tail(const u16* __restrict__ obuf, const u16* __restrict__ wot, const u16* __restrict__ w1t, const u16* __restrict__ w2t,
                                                  const u16* __restrict__ x, const u16* __restrict__ mod, const u16* __restrict__ b1, const u16* __restrict__ b2,
                                                  u16* part, u16* x1, u16* h2, u16* hid, u16* __restrict__ out, unsigned* cnt STAMP_ARG) {
  ST(0);
  extern __shared__ __attribute__((aligned(16))) char lds[];
  const int tid = threadIdx.x, lane = tid & 63, wid = __builtin_amdgcn_readfirstlane(tid >> 6);
  const unsigned lds0 = (unsigned)(uintptr_t)lds;
  const int xcd = blockIdx.x & 7, idx = blockIdx.x >> 3;
  const int rt = (xcd >> 2) * 8 + (idx >> 2), gi = (xcd & 3) * 4 + (idx & 3);
  unsigned* const c1 = cnt + rt * 64, * const c2 = cnt + (16 + rt) * 64, * const c3 = cnt + (32 + rt) * 64;
  const int nft = gi < 15 ? 7 : 3;
  const __amdgpu_buffer_rsrc_t raD = __builtin_amdgcn_make_buffer_rsrc((void*)(w1t + (size_t)(7 * gi) * 80 * 128), 0, nft * 80 * 256, 0x00020000);
  RingA<4, 7, 20, 80, 5> RD; RD.init(lds0, wid, lane);
  {
    const int kq = gi >> 2, j = gi & 3, qb = wid & 3;
    const __amdgpu_buffer_rsrc_t ra = __builtin_amdgcn_make_buffer_rsrc((void*)(wot + ((size_t)(10 * j) * 512 + kq * 128) * 128), 0, 10 * 512 * 256, 0x00020000);
    const __amdgpu_buffer_rsrc_t rb = __builtin_amdgcn_make_buffer_rsrc((void*)(obuf + ((size_t)(8 * rt + 2 * qb) * 512 + kq * 128) * 128), 0, 2 * 512 * 256, 0x00020000);
    const __amdgpu_buffer_rsrc_t rp = __builtin_amdgcn_make_buffer_rsrc((void*)(part + (size_t)kq * SQ * DM), 0, SQ * DM * 2, 0x00020000);
    f32x4 acc[10][2] = {};
    RingA<4, 10, 32, 512, 4> RA; RA.init(lds0, wid, lane);
    ring_gemm<4, 10, 32, 512, 512, 0, 4, 8, 1>(acc, RA, ra, rb, lds, wid, lane, [&](auto ic) __attribute__((always_inline)) { RD.template issue<ic.value / 4, ic.value % 4>(raD); } STAMP_FWD);
    ST(2);
    pair_reduce<10>(acc, lds + 81920, wid, lane);
    ST(13);
    const int nt0 = wid < 4 ? 0 : 5;
    const unsigned vo = (((8 * rt + 2 * qb + ((lane >> 4) & 1)) * 80 + j * 20 + nt0 * 2 + (lane >> 5)) * 16 + (lane & 15)) * 16;
    auto outp = [&](auto ntc) __attribute__((always_inline)) {
      constexpr int nt = ntc.value, o = nt % 5;
      const u32x2 w0 = {cvtpk(acc[nt][0][0], acc[nt][0][1]), cvtpk(acc[nt][0][2], acc[nt][0][3])}, w1 = {cvtpk(acc[nt][1][0], acc[nt][1][1]), cvtpk(acc[nt][1][2], acc[nt][1][3])};
      st16(rp, vo + o * 2 * 256, pair16(w0, w1));
    };
    if (wid < 4) { asm volatile("; lo-out" ::: "memory"); static_for<5>(LAMBDA(c) { outp(c); }); asm volatile("; lo-end" ::: "memory"); }
    else { asm volatile("; hi-out" ::: "memory"); static_for<5>(LAMBDA(c) { outp(IC<5 + c.value>{}); }); asm volatile("; hi-end" ::: "memory"); }
  }
  ST(14);
  group_arrive(c1);
  ST(3);
  {
    float* red = (float*)(lds + 98304);
    const int r = tid & 7, row = rt * 128 + gi * 8 + r;
    const bool two = wid < 2;
    const __amdgpu_buffer_rsrc_t rp = __builtin_amdgcn_make_buffer_rsrc((void*)part, 0, 4 * SQ * DM * 2, 0x00020000);
    const __amdgpu_buffer_rsrc_t rx1 = __builtin_amdgcn_make_buffer_rsrc((void*)x1, 0, SQ * DM * 2, 0x00020000);
    const __amdgpu_buffer_rsrc_t rh2 = __builtin_amdgcn_make_buffer_rsrc((void*)h2, 0, SQ * DM * 2, 0x00020000);
    u32x4 p[2][4], xv[2], gv[2], sbv[2], cbv[2]; unsigned blk[2];
#pragma unroll
    for (int u = 0; u < 2; ++u) {
      const int c = (u == 0 || two) ? (tid >> 3) + 64 * u : (tid >> 3);
      blk[u] = (((row >> 4) * 80 + c) * 16 + (row & 15)) * 16;
      xv[u] = *(const u32x4*)(x + (size_t)row * DM + c * 8);
      gv[u] = *(const u32x4*)(mod + 2 * 640 + c * 8); sbv[u] = *(const u32x4*)(mod + 3 * 640 + c * 8); cbv[u] = *(const u32x4*)(mod + 4 * 640 + c * 8);
    }
    RD.pre<2, 3>(raD);
    group_wait(c1, 16);
    ST(4);
#pragma unroll
    for (int k = 0; k < 4; ++k) p[0][k] = ld16c(rp, blk[0] + k * SQ * DM * 2);
    if (two) {
#pragma unroll
      for (int k = 0; k < 4; ++k) p[1][k] = ld16c(rp, blk[1] + k * SQ * DM * 2);
    }
    float v[2][8]; float s = 0; u32x4 x1v[2];
    auto resid = [&](auto uc) __attribute__((always_inline)) {
      constexpr int u = uc.value;
#pragma unroll
      for (int e = 0; e < 4; ++e) {
        const float a = (bf_lo(p[u][0][e]) + bf_lo(p[u][1][e])) + (bf_lo(p[u][2][e]) + bf_lo(p[u][3][e])), b = (bf_hi(p[u][0][e]) + bf_hi(p[u][1][e])) + (bf_hi(p[u][2][e]) + bf_hi(p[u][3][e]));
        x1v[u][e] = cvtpk(__builtin_fmaf(bf_lo(gv[u][e]), a, bf_lo(xv[u][e])), __builtin_fmaf(bf_hi(gv[u][e]), b, bf_hi(xv[u][e])));
        v[u][2 * e] = bf_lo(x1v[u][e]); v[u][2 * e + 1] = bf_hi(x1v[u][e]); s += v[u][2 * e] + v[u][2 * e + 1];
      }
    };
    resid(IC<0>{});
    if (two) resid(IC<1>{});
    ST(15);
    s = xor_add<32>(xor_add<16>(xor_add<8>(s)));
    if (lane < 8) red[wid * 8 + r] = s;
    lds_barrier();
    float tot = 0;
#pragma unroll
    for (int i = 0; i < 8; ++i) tot += red[i * 8 + r];
    const float mean = tot * (1.f / DM); float q = 0;
#pragma unroll
    for (int e = 0; e < 8; ++e) { v[0][e] -= mean; q += v[0][e] * v[0][e]; }
    if (two) {
#pragma unroll
      for (int e = 0; e < 8; ++e) { v[1][e] -= mean; q += v[1][e] * v[1][e]; }
    }
    q = xor_add<32>(xor_add<16>(xor_add<8>(q)));
    if (lane < 8) red[64 + wid * 8 + r] = q;
    lds_barrier();
    float qt = 0;
#pragma unroll
    for (int i = 0; i < 8; ++i) qt += red[64 + i * 8 + r];
    const float rstd = __builtin_amdgcn_rsqf(qt * (1.f / DM) + 1e-5f);
    auto norm = [&](auto uc) __attribute__((always_inline)) {
      constexpr int u = uc.value;
      u32x4 hv;
#pragma unroll
      for (int e = 0; e < 4; ++e)
        hv[e] = cvtpk(__builtin_fmaf(v[u][2 * e] * rstd, bf_lo(cbv[u][e]), bf_lo(sbv[u][e])), __builtin_fmaf(v[u][2 * e + 1] * rstd, bf_hi(cbv[u][e]), bf_hi(sbv[u][e])));
      st16(rh2, blk[u], hv); st16(rx1, blk[u], x1v[u]);
    };
    norm(IC<0>{});
    if (two) norm(IC<1>{});
  }
  ST(16);
  group_arrive(c2);
  ST(5);
  const int jE = gi & 7, rtE = rt * 2 + (gi >> 3);
  const __amdgpu_buffer_rsrc_t raE = __builtin_amdgcn_make_buffer_rsrc((void*)(w2t + (size_t)(5 * jE) * 216 * 128), 0, 5 * 216 * 256, 0x00020000);
  RingA<2, 5, 54, 216, 4> RE; RE.init(lds0, wid, lane);
  {
    const int qb = wid & 3, nt0 = wid < 4 ? 0 : 4;
    const __amdgpu_buffer_rsrc_t rb = __builtin_amdgcn_make_buffer_rsrc((void*)(h2 + (size_t)(8 * rt + 2 * qb) * 80 * 128), 0, 2 * 80 * 256, 0x00020000);
    const __amdgpu_buffer_rsrc_t rh = __builtin_amdgcn_make_buffer_rsrc((void*)hid, 0, SQ * FF * 2, 0x00020000);
    u32x2 bv[4];
#pragma unroll
    for (int o = 0; o < 4; ++o) bv[o] = *(const u32x2*)(b1 + (nt0 + o < nft ? (7 * gi + nt0 + o) * 16 + 4 * (lane >> 4) : 0));
    f32x4 acc[7][2] = {};
    group_wait(c2, 16);
    ST(6);
    ring_gemm<4, 7, 20, 80, 80, 3, 5, 10, 7>(acc, RD, raD, rb, lds, wid, lane, [&](auto ic) __attribute__((always_inline)) { RE.template issue<ic.value / 5, ic.value % 5>(raE); } STAMP_FWD);
    ST(8);
    pair_reduce<7>(acc, lds + 98304, wid, lane);
    ST(17);
    const unsigned vo = (((8 * rt + 2 * qb + ((lane >> 4) & 1)) * 216 + (7 * gi + nt0) * 2 + (lane >> 5)) * 16 + (lane & 15)) * 16;
    auto outp = [&](auto ntc, auto oc) __attribute__((always_inline)) {
      constexpr int nt = ntc.value, o = oc.value;
      if (nt < nft) {
        const float b0 = bf_lo(bv[o][0]), b1_ = bf_hi(bv[o][0]), b2_ = bf_lo(bv[o][1]), b3 = bf_hi(bv[o][1]);
        u32x2 w[2];
#pragma unroll
        for (int q = 0; q < 2; ++q) {
          const float v0 = rbf(__builtin_fmaxf(rbf(acc[nt][q][0]) + b0, 0.f)), v1 = rbf(__builtin_fmaxf(rbf(acc[nt][q][1]) + b1_, 0.f));
          const float v2 = rbf(__builtin_fmaxf(rbf(acc[nt][q][2]) + b2_, 0.f)), v3 = rbf(__builtin_fmaxf(rbf(acc[nt][q][3]) + b3, 0.f));
          w[q] = u32x2{cvtpk(v0 * v0, v1 * v1), cvtpk(v2 * v2, v3 * v3)};
        }
        st16(rh, vo + o * 2 * 256, pair16(w[0], w[1]));
      }
    };
    if (wid < 4) { asm volatile("; lo-out" ::: "memory"); static_for<4>(LAMBDA(c) { outp(c, c); }); asm volatile("; lo-end" ::: "memory"); }
    else { asm volatile("; hi-out" ::: "memory"); static_for<3>(LAMBDA(c) { outp(IC<4 + c.value>{}, c); }); asm volatile("; hi-end" ::: "memory"); }
  }
  ST(18);
  group_arrive(c3);
  ST(9);
  {
    const int qb = wid & 1;
    const __amdgpu_buffer_rsrc_t rb = __builtin_amdgcn_make_buffer_rsrc((void*)(hid + (size_t)(4 * rtE + 2 * qb) * 216 * 128), 0, 2 * 216 * 256, 0x00020000);
    const __amdgpu_buffer_rsrc_t rx1 = __builtin_amdgcn_make_buffer_rsrc((void*)x1, 0, SQ * DM * 2, 0x00020000);
    u32x4 xv[2], gv[2], bv[2];
#pragma unroll
    for (int it = 0; it < 2; ++it) {
      const int item = tid + it * 512, q = item & 63, c = item < 640 ? item >> 6 : 0, row = rtE * 64 + q;
      xv[it] = ld16c(rx1, (((row >> 4) * 80 + jE * 10 + c) * 16 + (row & 15)) * 16);
      gv[it] = *(const u32x4*)(mod + 5 * 640 + jE * 80 + c * 8); bv[it] = *(const u32x4*)(b2 + jE * 80 + c * 8);
    }
    f32x4 acc[5][2] = {};
    RE.pre<2, 3>(raE);
    group_wait(c3, 16);
    ST(10);
    ring_gemm<2, 5, 54, 216, 216, 3, 4, 0, 11>(acc, RE, raE, rb, lds, wid, lane, [&](auto) {} STAMP_FWD);
    ST(12);
    f32x4* red = (f32x4*)lds;
    constexpr int HI = (3 * 40960 - 4 * 10240) / 16;
    {
      f32x4* mine = red + (wid * 10) * 64 + (wid >= 4 ? HI : 0) + lane;
#pragma unroll
      for (int n = 0; n < 5; ++n) { mine[(n * 2) * 64] = acc[n][0]; mine[(n * 2 + 1) * 64] = acc[n][1]; }
    }
    lds_barrier();
#pragma unroll
    for (int it = 0; it < 2; ++it) {
      const int item = tid + it * 512;
      if (item < 640) {
        const int q = item & 63, c = item >> 6, n = c >> 1, l0 = (q & 15) + 32 * (c & 1), base = (((q >> 5) * 5 + n) * 2 + ((q >> 4) & 1)) * 64 + l0;
        f32x4 lo = {0, 0, 0, 0}, hi = {0, 0, 0, 0};
#pragma unroll
        for (int w = 0; w < 4; ++w) { const int o_ = base + w * 2 * 5 * 2 * 64 + (w >= 2 ? HI : 0); lo += red[o_]; hi += red[o_ + 16]; }
        const float v[8] = {lo[0], lo[1], lo[2], lo[3], hi[0], hi[1], hi[2], hi[3]};
        u32x4 o;
#pragma unroll
        for (int e = 0; e < 4; ++e) {
          const float a = bf_lo(xv[it][e]) + rbf(bf_lo(gv[it][e]) * rbf(rbf(v[2 * e]) + bf_lo(bv[it][e]))), b = bf_hi(xv[it][e]) + rbf(bf_hi(gv[it][e]) * rbf(rbf(v[2 * e + 1]) + bf_hi(bv[it][e])));
          o[e] = cvtpk(a, b);
        }
        *(u32x4*)(out + (size_t)(rtE * 64 + q) * DM + jE * 80 + c * 8) = o;
      }
    }
  }
  ST(31);
}

extern "C" long long* dbg_offsets() { static long long o[5] = {(long long)WS_MOD, (long long)WS_H, (long long)WS_O, (long long)WS_X1, (long long)WS_HID}; return o; }

extern "C" void kernel_launch(void* const* d_in, const int* in_sizes, int n_in, void* d_out, int out_size, void* d_ws, size_t ws_size, hipStream_t stream) {
  const u16 *x = (const u16*)d_in[0], *kc = (const u16*)d_in[1], *vc = (const u16*)d_in[2], *wq = (const u16*)d_in[3], *wo = (const u16*)d_in[4];
  const u16 *w1 = (const u16*)d_in[6], *b1 = (const u16*)d_in[7], *w2 = (const u16*)d_in[8], *b2 = (const u16*)d_in[9], *c = (const u16*)d_in[10], *wmod = (const u16*)d_in[11];
  char* ws = (char*)d_ws;
  u16 *mod = (u16*)(ws + WS_MOD), *h = (u16*)(ws + WS_H), *o = (u16*)(ws + WS_O), *wot = (u16*)(ws + WS_WOT), *w1t = (u16*)(ws + WS_W1T), *w2t = (u16*)(ws + WS_W2T);
  u16 *x1 = (u16*)(ws + WS_X1), *hid = (u16*)(ws + WS_HID);
  u16 *h2 = (u16*)(ws + WS_H2), *part = (u16*)(ws + WS_P);
  (void)hipFuncSetAttribute((const void*)k_attn, hipFuncAttributeMaxDynamicSharedMemorySize, (NS + 2) * SLOT);
  unsigned* cnt = (unsigned*)(ws + WS_CNT);
  (void)hipFuncSetAttribute((const void*)k_tail, hipFuncAttributeMaxDynamicSharedMemorySize, 163840);
  k_front<<<FR_N, 512, 0, stream>>>(c, wmod, x, mod, h, (unsigned*)(ws + WS_SYNC), cnt STAMP_PASS(0));
  k_attn<<<256, 512, (NS + 2) * SLOT, stream>>>(h, wq, kc, vc, o, wo, w1, w2, wot STAMP_PASS(2));
  k_tail<<<256, 512, 163840, stream>>>(o, wot, w1t, w2t, x, mod, b1, b2, part, x1, h2, hid, (u16*)d_out, cnt STAMP_PASS(3));
}
```

```cpp
#include <hip/hip_runtime.h>
#include <stdint.h>
#include <utility>

typedef unsigned short u16;
typedef short bf16x8 __attribute__((ext_vector_type(8)));
typedef short s16x4 __attribute__((ext_vector_type(4)));
typedef float f32x16 __attribute__((ext_vector_type(16)));
typedef float f32x4 __attribute__((ext_vector_type(4)));
typedef float f32x2 __attribute__((ext_vector_type(2)));
typedef __bf16 bf16x2 __attribute__((ext_vector_type(2)));
typedef unsigned u32x4 __attribute__((ext_vector_type(4)));
typedef unsigned u32x2 __attribute__((ext_vector_type(2)));
#define DI __device__ __forceinline__
#define GAS __attribute__((address_space(1)))
#define LAS __attribute__((address_space(3)))
#define SBAR() __builtin_amdgcn_sched_barrier(0)
#define PIN(x) asm volatile("" : "+v"(x))

constexpr int SQ = 2048, DM = 640, NH = 32, DH = 128, SK = 2048, WIN = 512, FF = 1728;
constexpr size_t WS_MOD = 0;
constexpr size_t WS_CNT = 8192;
constexpr size_t WS_SYNC = 32768;
constexpr size_t WS_H = 131072;
constexpr size_t WS_O = WS_H + (size_t)SQ * DM * 2;
constexpr size_t WS_WOT = WS_O + (size_t)SQ * 4096 * 2;
constexpr size_t WS_W1T = WS_WOT + (size_t)4096 * DM * 2;
constexpr size_t WS_W2T = WS_W1T + (size_t)DM * FF * 2;
constexpr size_t WS_X1 = WS_W2T + (size_t)DM * FF * 2 + 65536;
constexpr size_t WS_HID = WS_X1 + (size_t)SQ * DM * 2;
constexpr size_t WS_H2 = WS_HID + (size_t)SQ * FF * 2 + 65536;
constexpr size_t WS_P = WS_H2 + (size_t)SQ * DM * 2;
constexpr size_t WS_END = WS_P + (size_t)4 * SQ * DM * 2;

template <int... I, class F> DI void static_for_impl(std::integer_sequence<int, I...>, F&& f) { (f(std::integral_constant<int, I>{}), ...); }
template <int N, class F> DI void static_for(F&& f) { static_for_impl(std::make_integer_sequence<int, N>{}, f); }
#define LAMBDA(ic) [&](auto ic) __attribute__((always_inline))

DI float bf_lo(unsigned w) { return __uint_as_float(w << 16); }
DI float bf_hi(unsigned w) { return __uint_as_float(w & 0xffff0000u); }
DI float bf2f(u16 v) { return __uint_as_float((unsigned)v << 16); }
DI unsigned cvtpk(float lo, float hi) { f32x2 v = {lo, hi}; bf16x2 b = __builtin_convertvector(v, bf16x2); return __builtin_bit_cast(unsigned, b); }
DI u16 f2bf(float x) { return (u16)(cvtpk(x, 0.f) & 0xffffu); }
DI float rbf(float x) { return bf_lo(cvtpk(x, 0.f)); }
DI f32x16 mfma32(bf16x8 a, bf16x8 b, f32x16 c) { return __builtin_amdgcn_mfma_f32_32x32x16_bf16(a, b, c, 0, 0, 0); }
DI f32x4 mfma16(bf16x8 a, bf16x8 b, f32x4 c) { return __builtin_amdgcn_mfma_f32_16x16x32_bf16(a, b, c, 0, 0, 0); }
DI float swap_max(float m) { auto r = __builtin_amdgcn_permlane32_swap(__float_as_uint(m), __float_as_uint(m), false, false); return __builtin_fmaxf(__uint_as_float(r[0]), __uint_as_float(r[1])); }
DI float swap_add(float m) { auto r = __builtin_amdgcn_permlane32_swap(__float_as_uint(m), __float_as_uint(m), false, false); return __uint_as_float(r[0]) + __uint_as_float(r[1]); }

DI void lds_barrier() { asm volatile("s_waitcnt lgkmcnt(0)\n\ts_barrier" ::: "memory"); }
template <int CTRL> DI float dpp_mov(float v) { return __builtin_bit_cast(float, __builtin_amdgcn_update_dpp(0, __builtin_bit_cast(int, v), CTRL, 0xf, 0xf, true)); }
template <int M> DI float xor_add(float v) {
  if constexpr (M == 1) return v + dpp_mov<0xB1>(v);
  else if constexpr (M == 2) return v + dpp_mov<0x4E>(v);
  else if constexpr (M == 4) return v + dpp_mov<0x141>(dpp_mov<0x1B>(v));
  else if constexpr (M == 8) return v + dpp_mov<0x128>(v);
  else if constexpr (M == 16) { const auto r = __builtin_amdgcn_permlane16_swap(__float_as_uint(v), __float_as_uint(v), false, false); return __uint_as_float(r[0]) + __uint_as_float(r[1]); }
  else { const auto r = __builtin_amdgcn_permlane32_swap(__float_as_uint(v), __float_as_uint(v), false, false); return __uint_as_float(r[0]) + __uint_as_float(r[1]); }
}
typedef GAS unsigned gu32;
typedef GAS unsigned long long gu64;
#define RLX_AGENT __ATOMIC_RELAXED, __HIP_MEMORY_SCOPE_AGENT
constexpr int FR_NP = 120, FR_NL = 128, FR_N = FR_NP + FR_NL;
constexpr unsigned SPIN_CAP = 1u << 18;
#define H_AUX 0
#define POLL_SLEEP 4
#define FR_SLEEP 4
constexpr int SC1 = 16;
__global__ __launch_bounds__(512) void k_front(const u16* __restrict__ c, const u16* __restrict__ wmod, const u16* __restrict__ x, u16* __restrict__ mod, u16* __restrict__ h,
                                               unsigned* sync  , unsigned* __restrict__ cnt) {
  __shared__ float sm[1280];
  const int t = threadIdx.x, b = blockIdx.x;
  if (b < FR_NP) {
    const int cc = t & 3, r0 = t >> 2, col0 = b * 32 + cc * 8;
    u32x4 w[5];
#pragma unroll
    for (int i = 0; i < 5; ++i) w[i] = *(const u32x4*)(wmod + (size_t)(r0 + 128 * i) * 3840 + col0);
    float* sil = sm + 512;
    const unsigned cv = *(const unsigned*)(c + 2 * (t < 320 ? t : 0));
    unsigned tag = __hip_atomic_load((gu32*)sync, RLX_AGENT) + 1u;
    if (t < 320) {
      const float v0 = bf_lo(cv), v1 = bf_hi(cv);
      const unsigned pk = cvtpk(v0 * __builtin_amdgcn_rcpf(1.f + __expf(-v0)), v1 * __builtin_amdgcn_rcpf(1.f + __expf(-v1)));
      *(f32x2*)(sil + 2 * t) = f32x2{bf_lo(pk), bf_hi(pk)};
    }
    if (b == FR_NP - 1 && t < 48) cnt[t * 64] = 0u;
    PIN(tag);
    lds_barrier();
    if (t == 0) __hip_atomic_store((gu32*)sync + 64 + b, tag, RLX_AGENT);
    PIN(w[4]);
    float a[8] = {};
#pragma unroll
    for (int i = 0; i < 5; ++i) {
      const float sv = sil[r0 + 128 * i];
#pragma unroll
      for (int j = 0; j < 4; ++j) { a[2 * j] += sv * bf_lo(w[i][j]); a[2 * j + 1] += sv * bf_hi(w[i][j]); }
    }
#pragma unroll
    for (int j = 0; j < 8; ++j) a[j] = xor_add<32>(xor_add<16>(xor_add<8>(xor_add<4>(a[j]))));
    if ((t & 63) < 4) {
#pragma unroll
      for (int j = 0; j < 8; ++j) sm[(t >> 6) * 32 + cc * 8 + j] = a[j];
    }
    lds_barrier();
    if (t < 128) {
      const int u = t & 15;
      float lo = 0.f, hi = 0.f;
#pragma unroll
      for (int w8 = 0; w8 < 8; ++w8) { lo += sm[w8 * 32 + 2 * u]; hi += sm[w8 * 32 + 2 * u + 1]; }
      const bool onep = (b >= 20 && b < 40) || (b >= 80 && b < 100);
      unsigned pk = cvtpk(lo, hi);
      if (onep) pk = cvtpk(1.f + bf_lo(pk), 1.f + bf_hi(pk));
      if (b < 40) __hip_atomic_store((gu64*)(sync + 2048 + (t >> 4) * 2048) + b * 16 + u, ((unsigned long long)tag << 32) | pk, RLX_AGENT);
      if (t < 16) *(unsigned*)(mod + b * 32 + 2 * u) = pk;
    }
    if (b == FR_NP - 1 && t < 64) {
      bool ok = false;
      for (unsigned spins = 0; spins < SPIN_CAP && !ok; ++spins) {
        bool all = true;
#pragma unroll
        for (int k = 0; k < 4; ++k) { const int w = t + 64 * k; if (w < FR_N) all &= __hip_atomic_load((gu32*)sync + 64 + w, RLX_AGENT) == tag; }
        ok = __all(all);
        if (!ok) __builtin_amdgcn_s_sleep(2);
      }
      if (t == 0) __hip_atomic_store((gu32*)sync, tag, RLX_AGENT);
    }
  } else {
    if (t >= 256) return;
    const int i = t & 15, row = (b - FR_NP) * 16 + (t >> 4);
    u32x4 v[5];
#pragma unroll
    for (int j = 0; j < 5; ++j) v[j] = *(const u32x4*)(x + (size_t)row * DM + (i + 16 * j) * 8);
    const unsigned tag = __hip_atomic_load((gu32*)sync, RLX_AGENT) + 1u;
    if (t == 0) __hip_atomic_store((gu32*)sync + 64 + b, tag, RLX_AGENT);
    float f[40]; float s = 0;
#pragma unroll
    for (int j = 0; j < 5; ++j)
#pragma unroll
      for (int e = 0; e < 4; ++e) { f[j * 8 + 2 * e] = bf_lo(v[j][e]); f[j * 8 + 2 * e + 1] = bf_hi(v[j][e]); s += f[j * 8 + 2 * e] + f[j * 8 + 2 * e + 1]; }
    s = xor_add<8>(xor_add<4>(xor_add<2>(xor_add<1>(s))));
    const float mean = s * (1.f / DM); float q = 0;
#pragma unroll
    for (int e = 0; e < 40; ++e) { const float d = f[e] - mean; q += d * d; }
    q = xor_add<8>(xor_add<4>(xor_add<2>(xor_add<1>(q))));
    const float mb = rbf(mean), rsd = 1.f / rbf(sqrtf(rbf(rbf(q * (1.f / DM)) + 1e-5f)));
#pragma unroll
    for (int e = 0; e < 40; e += 2) {
      const unsigned d = cvtpk(f[e] - mb, f[e + 1] - mb), n = cvtpk(bf_lo(d) * rsd, bf_hi(d) * rsd);
      f[e] = bf_lo(n); f[e + 1] = bf_hi(n);
    }
    if (t < 64) {
      const __amdgpu_buffer_rsrc_t rg = __builtin_amdgcn_make_buffer_rsrc((void*)(sync + 2048 + (b & 7) * 2048), 0, 5120, 0x00020000);
      u32x4 gv[5];
      for (unsigned spins = 0; spins < SPIN_CAP; ++spins) {
        bool ok = true;
#pragma unroll
        for (int k = 0; k < 5; ++k) { gv[k] = __builtin_amdgcn_raw_buffer_load_b128(rg, (t + 64 * k) * 16, 0, SC1); ok &= gv[k][1] == tag && gv[k][3] == tag; }
        if (__all(ok)) break;
        __builtin_amdgcn_s_sleep(FR_SLEEP);
      }
#pragma unroll
      for (int k = 0; k < 5; ++k) {
        const int g = (t + 64 * k) * 2;
        *(f32x4*)(sm + 2 * g) = f32x4{bf_lo(gv[k][0]), bf_hi(gv[k][0]), bf_lo(gv[k][2]), bf_hi(gv[k][2])};
      }
    }
    __syncthreads();
#pragma unroll
    for (int j = 0; j < 5; ++j) {
      const int k0 = (i + 16 * j) * 8;
      const f32x4 s0 = *(const f32x4*)(sm + k0), s1 = *(const f32x4*)(sm + k0 + 4), c0 = *(const f32x4*)(sm + 640 + k0), c1 = *(const f32x4*)(sm + 640 + k0 + 4);
      const float sa[8] = {s0[0], s0[1], s0[2], s0[3], s1[0], s1[1], s1[2], s1[3]}, oc[8] = {c0[0], c0[1], c0[2], c0[3], c1[0], c1[1], c1[2], c1[3]};
      u32x4 o;
#pragma unroll
      for (int e = 0; e < 4; ++e) {
        const unsigned m = cvtpk(f[j * 8 + 2 * e] * oc[2 * e], f[j * 8 + 2 * e + 1] * oc[2 * e + 1]);
        o[e] = cvtpk(bf_lo(m) + sa[2 * e], bf_hi(m) + sa[2 * e + 1]);
      }
      __builtin_amdgcn_raw_buffer_store_b128(o, __builtin_amdgcn_make_buffer_rsrc((void*)h, 0, SQ * DM * 2, 0x00020000), (((row >> 4) * 80 + (i + 16 * j)) * 16 + (row & 15)) * 16, 0, H_AUX);
    }
  }
}

DI u32x4 pair16(u32x2 x, u32x2 y) {
  const auto a = __builtin_amdgcn_permlane16_swap(x[0], y[0], false, false), b = __builtin_amdgcn_permlane16_swap(x[1], y[1], false, false);
  return u32x4{a[0], b[0], a[1], b[1]};
}
#define A_NS 8
#define A_PD 5
#define O_AUX 16
constexpr int NS = A_NS, SLOT = 16384;
constexpr float QSCALE = 0.088388347648318440f * 1.4426950408889634f;
constexpr float PBIAS = 96.f;
#define PLIM_V 1073741824.f
constexpr float PLIM = PLIM_V;
constexpr int tile_K(int t) { return t == 0 ? 10 : 9 + 2 * t; }
constexpr int tile_V(int t) { return t == 7 ? 25 : 12 + 2 * t; }
constexpr int slot_of(int idx) { return (idx % NS) * SLOT; }
constexpr int lastuse(int idx) { return idx < 10 ? idx : (idx == 10 ? 10 : (idx == 25 ? 18 : ((idx & 1) ? 10 + (idx - 9) / 2 : 11 + (idx - 12) / 2))); }
constexpr int need_bar(int idx) {
  if (idx < 10) return idx == 0 ? 0 : idx - 1;
  if (idx == 10) return 9;
  if (idx == 25) return 17;
  return (idx & 1) ? 10 + (idx - 9) / 2 - 1 : 11 + (idx - 12) / 2;
}
constexpr int rt_step(int u) { return u == 0 ? 9 : (u == 1 ? 10 : 12); }
constexpr int rt_proc(int u) { return u == 0 ? 11 : (u == 1 ? 12 : 14); }
constexpr int rt_slot(int u) { return (u == 1 ? NS + 1 : NS) * SLOT; }
struct Sched { int npro; int pro[16]; int nops[19]; int op[19][16]; int wait[19]; };
constexpr Sched make_sched() {
  Sched p{}; int pos = 0; int tile_end[26] = {}; int h_end[10] = {}; int u_end[3] = {}; int next_tile = 3;
  auto pro_tile = [&](int i) { p.pro[p.npro++] = 2 * i; p.pro[p.npro++] = 2 * i + 1; pos += 2; tile_end[i] = pos; };
  auto pro_h = [&](int i) { for (int k = 0; k < 4; ++k) p.pro[p.npro++] = 100 + 4 * i + k; pos += 4; h_end[i] = pos; };
  pro_tile(0); pro_h(0); pro_tile(1); pro_h(1); pro_tile(2);
  for (int g = 0; g < 19; ++g) {
    int need = 0;
    for (int idx = 0; idx < next_tile; ++idx) if (need_bar(idx) <= g && tile_end[idx] > need) need = tile_end[idx];
    if (g < 10 && h_end[g] > need) need = h_end[g];
    for (int u = 0; u < 3; ++u) if (rt_proc(u) <= g && u_end[u] > need) need = u_end[u];
    p.wait[g] = pos - need;
    int n = 0;
    for (int u = 0; u < 3; ++u) if (g == rt_proc(u)) { p.op[g][n++] = 300 + 2 * u; p.op[g][n++] = 301 + 2 * u; }
    if (g == 0) { for (int k = 0; k < 4; ++k) p.op[g][n++] = 100 + 4 * 2 + k; pos += 4; h_end[2] = pos; }
    const int maxt = g < 7 ? 1 : 2;
    for (int c = 0; c < maxt && next_tile < 26; ++c) {
      if (next_tile >= NS && lastuse(next_tile - NS) >= g) break;
      p.op[g][n++] = 2 * next_tile; p.op[g][n++] = 2 * next_tile + 1; pos += 2; tile_end[next_tile] = pos; ++next_tile;
    }
    if (g + 3 < 10) { for (int k = 0; k < 4; ++k) p.op[g][n++] = 100 + 4 * (g + 3) + k; pos += 4; h_end[g + 3] = pos; }
    for (int u = 0; u < 3; ++u) if (g == rt_step(u)) { p.op[g][n++] = 200 + 2 * u; p.op[g][n++] = 201 + 2 * u; pos += 2; u_end[u] = pos; }
    for (int u = 0; u < 3; ++u) if (g == rt_proc(u)) { p.op[g][n++] = 310 + 2 * u; p.op[g][n++] = 311 + 2 * u; pos += 2; }
    p.nops[g] = n;
  }
  for (int idx = 0; idx < 26; ++idx) if (tile_end[idx] == 0) p.wait[0] = -1;
  return p;
}
constexpr Sched SCH = make_sched();
static_assert(SCH.wait[0] >= 0, "schedule incomplete");
constexpr bool sched_ok() {
  int step_of[26] = {}; for (int i = 0; i < 26; ++i) step_of[i] = -1;
  for (int g = 0; g < 19; ++g) for (int j = 0; j < SCH.nops[g]; ++j) if (SCH.op[g][j] < 100) step_of[SCH.op[g][j] >> 1] = g;
  for (int i = 3; i < 26; ++i) if (step_of[i] >= need_bar(i)) return false;
  return true;
}
static_assert(sched_ok(), "a tile is issued too late");

DI void dma16(unsigned ldsaddr, unsigned voff, __amdgpu_buffer_rsrc_t srd, unsigned soff) {
  asm volatile("s_mov_b32 m0, %0\n\ts_nop 0\n\tbuffer_load_dwordx4 %1, %2, %3 offen lds" ::"s"(ldsaddr), "v"(voff), "s"(srd), "s"(soff) : "memory");
}
template <int OFF> DI void bload16(bf16x8& dst, unsigned voff, __amdgpu_buffer_rsrc_t srd, unsigned soff) {
  asm volatile("buffer_load_dwordx4 %0, %1, %2, %3 offen offset:%4" : "=v"(dst) : "v"(voff), "s"(srd), "s"(soff), "i"(OFF) : "memory");
}
typedef LAS const char* lds_cptr;
typedef short v4i16_t __attribute__((ext_vector_type(4)));
DI s16x4 vtr(lds_cptr p) { return __builtin_bit_cast(s16x4, __builtin_amdgcn_ds_read_tr16_b64_v4i16((LAS v4i16_t*)p)); }
DI bf16x8 trfrag(lds_cptr p0, lds_cptr p1) { const s16x4 lo = vtr(p0), hi = vtr(p1); return __builtin_shufflevector(lo, hi, 0, 1, 2, 3, 4, 5, 6, 7); }
DI bf16x8 rowfrag(lds_cptr p) { return *(LAS const bf16x8*)p; }
DI float max3(float a, float b, float c) { return __builtin_fmaxf(__builtin_fmaxf(a, b), c); }

template <int N> DI void wait_bar4(bf16x8 (&b)[4]) {
  asm volatile("s_waitcnt vmcnt(%4)\n\ts_barrier" : "+v"(b[0]), "+v"(b[1]), "+v"(b[2]), "+v"(b[3]) : "i"(N) : "memory");
}
template <int N> DI void wait_bar() { asm volatile("s_waitcnt vmcnt(%0)\n\ts_barrier" ::"i"(N) : "memory"); }
template <int V> using IC = std::integral_constant<int, V>;

__global__ __launch_bounds__(512, 2) void k_attn(const u16* __restrict__ hbuf, const u16* __restrict__ wq, const u16* __restrict__ kc, const u16* __restrict__ vc, u16* __restrict__ obuf,
    const u16* __restrict__ wo, const u16* __restrict__ w1, const u16* __restrict__ w2, u16* __restrict__ wot) {
  extern __shared__ __attribute__((aligned(16))) char lds[];
  const int tid = threadIdx.x, lane = tid & 63, r32 = lane & 31, hi = lane >> 5;
  const int wid = __builtin_amdgcn_readfirstlane(tid >> 6);
  const int xcd = blockIdx.x & 7, idx = blockIdx.x >> 3;
  const int head = (xcd & 3) * 8 + (idx & 7), qt = (xcd >> 2) * 4 + (idx >> 3);
  const unsigned lds0 = (unsigned)(uintptr_t)lds;
  const __amdgpu_buffer_rsrc_t srdW = __builtin_amdgcn_make_buffer_rsrc((void*)(wq + head * DH), 0, (DM - 1) * 8192 + 256, 0x00020000);
  const __amdgpu_buffer_rsrc_t srdK = __builtin_amdgcn_make_buffer_rsrc((void*)(kc + ((size_t)head * SK + (SK - WIN)) * DH), 0, WIN * 256, 0x00020000);
  const __amdgpu_buffer_rsrc_t srdV = __builtin_amdgcn_make_buffer_rsrc((void*)(vc + ((size_t)head * SK + (SK - WIN)) * DH), 0, WIN * 256, 0x00020000);
  const __amdgpu_buffer_rsrc_t srdH = __builtin_amdgcn_make_buffer_rsrc((void*)(hbuf + (size_t)(qt * 16 + 2 * wid) * 80 * 128), 0, 2 * 80 * 256, 0x00020000);
  const int dr = (lane >> 2) & 7, dx = lane & 3, dc = lane >> 5, dz = (2 * wid + (dr >> 2)) & 3;
  const unsigned voffW = (unsigned)((8 * wid + dr) * 8192 + 64 * dc + 16 * (dx ^ dz));
  const unsigned voffKV = (unsigned)((8 * wid + dr) * 256 + 64 * dc + 16 * (dx ^ dz));
  const unsigned ldsw = lds0 + 2048 * wid;
  const unsigned voffH = lane * 16;
  bf16x8 hf[10][4], rtf[2];
  __amdgpu_buffer_rsrc_t rsrc_u[3]; unsigned pitch_u[3], dst_u[3];
  const __amdgpu_buffer_rsrc_t srdT = __builtin_amdgcn_make_buffer_rsrc((void*)wot, 0, (int)(WS_W2T - WS_WOT) + DM * FF * 2, 0x00020000);
  auto vmem_op = [&](auto oc) __attribute__((always_inline)) {
    constexpr int op = oc.value;
    if constexpr (op >= 310) {
      constexpr int u = (op - 310) >> 1, ks = op & 1;
      __builtin_amdgcn_raw_buffer_store_b128(__builtin_bit_cast(u32x4, rtf[ks]), srdT, lane * 16 + ks * 1024, dst_u[u], O_AUX);
    } else if constexpr (op >= 300) {
      constexpr int u = (op - 300) >> 1, ks = op & 1;
      const int g4 = lane >> 4, chq = 2 * wid + ((lane & 3) >> 1), q4_ = (lane & 15) >> 2;
      const lds_cptr p0 = (lds_cptr)lds + rt_slot(u) + 2048 * (4 * ks + g4) + 512 * (chq >> 2) + 64 * q4_ + 16 * ((chq & 3) ^ ((2 * g4) & 3)) + 8 * (lane & 1);
      const lds_cptr p1 = (lds_cptr)lds + rt_slot(u) + 2048 * (4 * ks + g4) + 512 * (chq >> 2) + 64 * (4 + q4_) + 16 * ((chq & 3) ^ ((2 * g4 + 1) & 3)) + 8 * (lane & 1);
      rtf[ks] = trfrag(p0, p1);
    } else if constexpr (op >= 200) {
      constexpr int u = (op - 200) >> 1, half = op & 1;
      dma16(ldsw + rt_slot(u) + 1024 * half, (unsigned)(8 * wid + dr) * pitch_u[u] + (unsigned)(64 * dc + 16 * (dx ^ dz)), rsrc_u[u], 128 * half);
    } else if constexpr (op >= 100) { constexpr int i = (op - 100) >> 2, k = (op - 100) & 3; bload16<1024 * (k >> 1)>(hf[i][k], voffH, srdH, i * 2048 + (k & 1) * 20480); }
    else {
      constexpr int i = op >> 1, half = op & 1; constexpr unsigned so = slot_of(i) + 1024 * half;
      if constexpr (i < 10) dma16(ldsw + so, voffW, srdW, i * 64 * 8192 + 128 * half);
      else {
        constexpr bool isK = (i == 10) || (i != 25 && (i & 1)); constexpr int t = i == 10 ? 0 : (i == 25 ? 7 : (isK ? (i - 9) / 2 : (i - 12) / 2));
        dma16(ldsw + so, voffKV, isK ? srdK : srdV, t * 16384 + 128 * half);
      }
    }
  };
  static_for<SCH.npro>(LAMBDA(jc) { vmem_op(IC<SCH.pro[jc.value]>{}); });
  SBAR();
#pragma unroll
  for (int u = 0; u < 3; ++u) {
    const int id = blockIdx.x + 256 * u;
    const u16* src; int pitch, rows, kt, nt, kc, nsub; unsigned dbase;
    if (id < 320) { src = wo; pitch = 1280; rows = 4096; kt = id / 5; nt = id - kt * 5; kc = 512; nsub = 40; dbase = 0; }
    else if (id < 455) { const int v = id - 320; src = w2; pitch = 1280; rows = 1728; kt = v / 5; nt = v - kt * 5; kc = 216; nsub = 40; dbase = (unsigned)(WS_W2T - WS_WOT); }
    else { const int v = id < 595 ? id - 455 : 0; src = w1; pitch = 3456; rows = id < 595 ? 640 : 0; kt = v / 14; nt = v - kt * 14; kc = 80; nsub = id < 595 ? 108 : 0; dbase = (unsigned)(WS_W1T - WS_WOT); }
    const int first = kt * 64 * pitch + nt * 256;
    rsrc_u[u] = __builtin_amdgcn_make_buffer_rsrc((void*)((const char*)src + first), 0, rows ? rows * pitch - first : 0, 0x00020000);
    pitch_u[u] = pitch;
    dst_u[u] = nt * 8 + wid < nsub ? dbase + ((nt * 8 + wid) * kc + kt * 8) * 256 : 0x7ff00000u;
  }
  auto vmem_gap = [&](auto gc, auto jc, auto nc) __attribute__((always_inline)) {
    constexpr int g = gc.value, j = jc.value, n = nc.value, no = SCH.nops[g];
    constexpr int i0 = (j * no + n - 1) / n, i1 = ((j + 1) * no + n - 1) / n;
    static_for<i1 - i0>(LAMBDA(uc) { vmem_op(IC<SCH.op[g][i0 + uc.value]>{}); });
  };

  const int blk = (lane >> 4) & 1, q4 = (lane & 15) >> 2, p4 = lane & 3;
  constexpr int NR = (NS * SLOT + 65535) / 65536;
  const lds_cptr L = (lds_cptr)lds;
  lds_cptr wb[NR][4], vb[NR][2], kb_[NR][2];
#pragma unroll
  for (int r = 0; r < NR; ++r)
#pragma unroll
    for (int t = 0; t < 2; ++t) {
#pragma unroll
      for (int par = 0; par < 2; ++par) {
        const int g4 = lane >> 4;
        wb[r][2 * par + t] = L + 65536 * r + 2048 * g4 + 64 * (4 * t + q4) + 16 * ((2 * par + (p4 >> 1)) ^ ((2 * g4 + t) & 3)) + 8 * (p4 & 1);
        PIN(wb[r][2 * par + t]);
      }
      vb[r][t] = L + 65536 * r + 64 * (4 * hi + q4) + 16 * ((2 * blk + (p4 & 1)) ^ (2 * t + hi)) + 8 * (p4 >> 1) + 2048 * t;
      kb_[r][t] = L + 65536 * r + 2048 * (r32 >> 3) + 64 * (r32 & 7) + 16 * ((2 * t + hi) ^ ((r32 >> 2) & 3));
    }
  constexpr int PD = A_PD, NG = 416;
  bf16x8 fr[PD + 1];
  auto ldfrag_g = [&](auto Gc) __attribute__((always_inline)) {
    constexpr int G = Gc.value;
    if constexpr (G < NG) {
      if constexpr (G < 160) {
        constexpr int i = G >> 4, ks = (G >> 3) & 1, dt = G & 7, o = slot_of(i) + 8192 * ks + 512 * (dt >> 1);
        fr[G % (PD + 1)] = trfrag(wb[o >> 16][2 * (dt & 1)] + (o & 65535), wb[o >> 16][2 * (dt & 1) + 1] + (o & 65535));
      } else {
        constexpr int u = G - 176, t = G < 176 ? 0 : (G >= 400 ? 8 : 1 + u / 32), g = G < 176 ? G - 160 : (G >= 400 ? G - 400 + 16 : u % 32);
        if constexpr (g < 16) {
          constexpr int kb = g >> 3, s = g & 7, o = slot_of(tile_K(t)) + 8192 * kb + 512 * (s >> 1);
          fr[G % (PD + 1)] = rowfrag(kb_[o >> 16][s & 1] + (o & 65535));
        } else {
          constexpr int gg = g - 16, ks = t == 8 ? (gg & 3) : (gg >> 2), db = t == 8 ? (gg >> 2) : (gg & 3), o = slot_of(tile_V(t - 1)) + 4096 * ks + 512 * db;
          fr[G % (PD + 1)] = trfrag(vb[o >> 16][0] + (o & 65535), vb[o >> 16][1] + (o & 65535));
        }
      }
    }
  };
  auto barrier = [&](auto gc) __attribute__((always_inline)) {
    constexpr int g = gc.value;
    if constexpr (g < 10) wait_bar4<SCH.wait[g]>(hf[g]); else wait_bar<SCH.wait[g]>();
    SBAR();
  };

  f32x4 acc[8][2] = {};
  bf16x8 qf[8];
  auto q_conv = [&](auto sc) __attribute__((always_inline)) {
    constexpr int s = sc.value;
    const u32x2 x0 = {cvtpk(acc[s][0][0] * QSCALE, acc[s][0][1] * QSCALE), cvtpk(acc[s][0][2] * QSCALE, acc[s][0][3] * QSCALE)};
    const u32x2 x1 = {cvtpk(acc[s][1][0] * QSCALE, acc[s][1][1] * QSCALE), cvtpk(acc[s][1][2] * QSCALE, acc[s][1][3] * QSCALE)};
    qf[s] = __builtin_bit_cast(bf16x8, pair16(x0, x1));
    PIN(qf[s]);
  };
  static_for<10>(LAMBDA(ic) {
    constexpr int i = ic.value;
    barrier(ic);
    if constexpr (i == 0) { static_for<PD>(LAMBDA(jc) { ldfrag_g(jc); }); SBAR(); }
    static_for<16>(LAMBDA(mc) {
      constexpr int m = mc.value, G = 16 * i + m;
      ldfrag_g(IC<G + PD>{});
      vmem_gap(ic, mc, IC<16>{});
      acc[m & 7][0] = mfma16(fr[G % (PD + 1)], hf[i][2 * (m >> 3)], acc[m & 7][0]);
      acc[m & 7][1] = mfma16(fr[G % (PD + 1)], hf[i][2 * (m >> 3) + 1], acc[m & 7][1]);
      if constexpr (i == 9 && m >= 10) q_conv(IC<m - 10>{});
      SBAR();
    });
  });
  q_conv(IC<6>{}); q_conv(IC<7>{});
#pragma unroll
  for (int r = 0; r < NR; ++r)
#pragma unroll
    for (int t = 0; t < 2; ++t) { PIN(vb[r][t]); PIN(kb_[r][t]); }

  const int qrow = qt * 256 + wid * 32 + r32;
  const __amdgpu_buffer_rsrc_t srdO = __builtin_amdgcn_make_buffer_rsrc((void*)obuf, 0, SQ * 4096 * 2, 0x00020000);
  const unsigned voffO = (((qrow >> 4) * 512 + head * 16 + hi) * 16 + (qrow & 15)) * 16;
  f32x16 o[4] = {}; f32x16 sA[2], sB[2], negm; u32x4 pw[4];
  float mref, lsum = 0.f; bool bad = false;
  barrier(IC<10>{});
  {
    const f32x16 zero = {};
    static_for<16>(LAMBDA(gc) {
      constexpr int g = gc.value, G = 160 + g;
      ldfrag_g(IC<G + PD>{});
      vmem_gap(IC<10>{}, gc, IC<16>{});
      sA[g >> 3] = mfma32(fr[G % (PD + 1)], qf[g & 7], (g & 7) == 0 ? zero : sA[g >> 3]);
      SBAR();
    });
    float a = max3(sA[0][0], sA[0][1], sA[1][0]), b = max3(sA[0][2], sA[0][3], sA[1][1]); a = max3(a, sA[1][2], sA[1][3]);
#pragma unroll
    for (int r = 4; r < 16; r += 4) { a = max3(a, sA[0][r], sA[0][r + 1]); b = max3(b, sA[0][r + 2], sA[0][r + 3]); a = max3(a, sA[1][r], sA[1][r + 1]); b = max3(b, sA[1][r + 2], sA[1][r + 3]); }
    mref = swap_max(__builtin_fmaxf(a, b)) + PBIAS;
#pragma unroll
    for (int r = 0; r < 16; ++r) negm[r] = -mref;
    PIN(negm);
    sA[0][0] = __builtin_amdgcn_exp2f(sA[0][0] - mref); sA[0][1] = __builtin_amdgcn_exp2f(sA[0][1] - mref);
    SBAR();
  }
  auto step = [&](auto tc, f32x16(&C)[2], f32x16(&P)[2]) __attribute__((always_inline)) {
    constexpr int t = tc.value;
    constexpr int G0 = 176 + 32 * (t - 1), GB = t < 8 ? G0 + 16 : 400;
    barrier(IC<10 + t>{});
    float sacc = 0.f;
    static_for<16>(LAMBDA(gc) {
      constexpr int g = gc.value;
      if constexpr (t < 8) {
        ldfrag_g(IC<G0 + g + PD>{});
        vmem_gap(IC<10 + t>{}, gc, IC<32>{});
        C[g >> 3] = mfma32(fr[(G0 + g) % (PD + 1)], qf[g & 7], (g & 7) == 0 ? negm : C[g >> 3]);
      }
      if constexpr (t == 1 && g < 15) {
        constexpr int e1 = 2 * (g + 1);
        P[e1 >> 4][e1 & 15] = __builtin_amdgcn_exp2f(P[e1 >> 4][e1 & 15] - mref); P[e1 >> 4][(e1 & 15) + 1] = __builtin_amdgcn_exp2f(P[e1 >> 4][(e1 & 15) + 1] - mref);
        PIN(P[e1 >> 4]);
      }
      constexpr int ks = g >> 2, w = g & 3, e = 8 * (ks & 1) + 2 * w;
      pw[ks][w] = cvtpk(P[ks >> 1][e], P[ks >> 1][e + 1]);
      sacc += P[ks >> 1][e]; sacc += P[ks >> 1][e + 1];
      PIN(pw[ks]); PIN(sacc);
      SBAR();
    });
    bad |= !(sacc <= PLIM);
    lsum += sacc;
    if constexpr (t < 8) {
      static_for<16>(LAMBDA(gc) {
        constexpr int g = gc.value;
        ldfrag_g(IC<GB + g + PD>{});
        vmem_gap(IC<10 + t>{}, IC<16 + g>{}, IC<32>{});
        o[g & 3] = mfma32(fr[(GB + g) % (PD + 1)], __builtin_bit_cast(bf16x8, pw[g >> 2]), o[g & 3]);
        C[g >> 3][(2 * g) & 15] = __builtin_amdgcn_exp2f(C[g >> 3][(2 * g) & 15]); C[g >> 3][(2 * g + 1) & 15] = __builtin_amdgcn_exp2f(C[g >> 3][(2 * g + 1) & 15]);
        PIN(C[g >> 3]);
        SBAR();
      });
    }
  };
  auto out_db = [&](auto dc, float inv) __attribute__((always_inline)) {
    constexpr int db = dc.value;
#pragma unroll
    for (int s = 0; s < 2; ++s) {
      u32x4 w;
#pragma unroll
      for (int e = 0; e < 4; ++e) w[e] = cvtpk(o[db][8 * s + 2 * e] * inv, o[db][8 * s + 2 * e + 1] * inv);
      __builtin_amdgcn_raw_buffer_store_b128(w, srdO, voffO + (4 * db + 2 * s) * 256, 0, O_AUX);
    }
  };
  step(IC<1>{}, sB, sA);
  step(IC<2>{}, sA, sB);
  step(IC<3>{}, sB, sA);
  step(IC<4>{}, sA, sB);
  step(IC<5>{}, sB, sA);
  step(IC<6>{}, sA, sB);
  step(IC<7>{}, sB, sA);
  step(IC<8>{}, sA, sB);
  if (__builtin_expect(!__any(bad), 1)) {
    const float inv = 1.f / swap_add(lsum);
    static_for<16>(LAMBDA(gc) {
      constexpr int g = gc.value;
      ldfrag_g(IC<400 + g + PD>{});
      o[g >> 2] = mfma32(fr[(400 + g) % (PD + 1)], __builtin_bit_cast(bf16x8, pw[g & 3]), o[g >> 2]);
      if constexpr (g >= 5 && (g & 3) == 1) out_db(IC<(g >> 2) - 1>{}, inv);
      SBAR();
    });
    out_db(IC<3>{}, inv);
  } else {
    const unsigned reg = (unsigned)((wid == 0 ? 0 : wid + 1) * SLOT);
    static_assert(slot_of(tile_V(7)) == SLOT, "");
    const lds_cptr kbs[2] = {kb_[0][0] + reg, kb_[0][1] + reg}, vbs[2] = {vb[0][0] + reg + 8192, vb[0][1] + reg + 8192};
    float m = -__builtin_inff(), l = 0.f;
#pragma unroll
    for (int d = 0; d < 4; ++d) o[d] = f32x16{};
#pragma unroll 1
    for (int kb = 0; kb < WIN / 32; ++kb) {
#pragma unroll
      for (int R = 0; R < 4; ++R) {
        const unsigned vo = (unsigned)((8 * R + dr) * 256 + 64 * dc + 16 * (dx ^ ((2 * R + (dr >> 2)) & 3)));
        const unsigned so = __builtin_amdgcn_readfirstlane(kb * 8192), ld = __builtin_amdgcn_readfirstlane(lds0 + reg + 2048 * R);
        asm volatile("s_nop 4" ::: "memory");
        dma16(ld, vo, srdK, so); dma16(ld + 1024, vo + 128, srdK, so); dma16(ld + 8192, vo, srdV, so); dma16(ld + 9216, vo + 128, srdV, so);
      }
      asm volatile("s_waitcnt vmcnt(0)" ::: "memory");
      f32x16 sc = {};
#pragma unroll
      for (int s = 0; s < 8; ++s) { sc = mfma32(rowfrag(kbs[s & 1] + 512 * (s >> 1)), qf[s], sc); SBAR(); }
      float mx = sc[0];
#pragma unroll
      for (int r = 1; r < 16; ++r) mx = __builtin_fmaxf(mx, sc[r]);
      const float mn = __builtin_fmaxf(m, swap_max(mx)), al = __builtin_amdgcn_exp2f(m - mn);
      m = mn; l *= al;
#pragma unroll
      for (int r = 0; r < 16; ++r) { sc[r] = __builtin_amdgcn_exp2f(sc[r] - mn); l += sc[r]; }
#pragma unroll
      for (int d = 0; d < 4; ++d)
#pragma unroll
        for (int r = 0; r < 16; ++r) o[d][r] *= al;
#pragma unroll
      for (int ks = 0; ks < 2; ++ks) {
        const u32x4 pk = {cvtpk(sc[8 * ks], sc[8 * ks + 1]), cvtpk(sc[8 * ks + 2], sc[8 * ks + 3]), cvtpk(sc[8 * ks + 4], sc[8 * ks + 5]), cvtpk(sc[8 * ks + 6], sc[8 * ks + 7])};
#pragma unroll
        for (int d = 0; d < 4; ++d) { o[d] = mfma32(trfrag(vbs[0] + 4096 * ks + 512 * d, vbs[1] + 4096 * ks + 512 * d), __builtin_bit_cast(bf16x8, pk), o[d]); SBAR(); }
      }
      asm volatile("s_waitcnt lgkmcnt(0)" ::: "memory");
    }
    const float inv = 1.f / swap_add(l);
    static_for<4>(LAMBDA(dc_) { out_db(dc_, inv); });
  }
}

DI void st16(__amdgpu_buffer_rsrc_t r, unsigned voff, u32x4 v) { __builtin_amdgcn_raw_buffer_store_b128(v, r, voff, 0, SC1); }
DI void st8(__amdgpu_buffer_rsrc_t r, unsigned voff, u32x2 v) { __builtin_amdgcn_raw_buffer_store_b64(v, r, voff, 0, SC1); }
DI u32x4 ld16c(__amdgpu_buffer_rsrc_t r, unsigned voff) { return __builtin_amdgcn_raw_buffer_load_b128(r, voff, 0, SC1); }
DI void bload16c(bf16x8& dst, unsigned voff, __amdgpu_buffer_rsrc_t srd, unsigned soff) {
  asm volatile("buffer_load_dwordx4 %0, %1, %2, %3 offen sc1" : "=v"(dst) : "v"(voff), "s"(srd), "s"(soff) : "memory");
}
template <int N> DI void wait_bar4b(bf16x8 (&b)[2][2]) {
  asm volatile("s_waitcnt vmcnt(%4)\n\ts_barrier" : "+v"(b[0][0]), "+v"(b[0][1]), "+v"(b[1][0]), "+v"(b[1][1]) : "i"(N) : "memory");
}
template <int NQB, int NT, int KS> struct Ring {
  static constexpr int KSP = 8 / NQB, SK = 2 * KSP, NSTG = (KS + SK - 1) / SK, NP = SK * NT, PA = (NP + 7) / 8, SB = PA * 8 * 1024, FR = 2 * NT;
};
template <int NQB, int NT, int KS, int KC_A, int NSLOT> struct RingA {
  using R = Ring<NQB, NT, KS>;
  static constexpr int NS_ = NSLOT;
  unsigned voff[R::PA]; unsigned ldsw;
  DI void init(unsigned lds0, int wid, int lane) {
    ldsw = lds0 + wid * 1024;
#pragma unroll
    for (int i = 0; i < R::PA; ++i) { const int p = i * 8 + wid, ks = p / NT, nt = p - ks * NT; voff[i] = p < R::NP ? (unsigned)(nt * KC_A * 256 + ks * 1024 + lane * 16) : 0x7ff00000u; }
  }
  template <int S, int I> DI void issue(__amdgpu_buffer_rsrc_t ra) const { dma16(ldsw + ((S % NSLOT) * R::SB + I * 8192), voff[I], ra, S * R::SK * 1024); }
  template <int S0, int S1> DI void pre(__amdgpu_buffer_rsrc_t ra) const { static_for<S1 - S0>(LAMBDA(sc) { static_for<R::PA>(LAMBDA(ic) { this->template issue<S0 + sc.value, ic.value>(ra); }); }); }
};
template <int NQB, int NT, int KS, int KC_A, int KC_B, int NPRE, int NSLOT, int NHOOK, class HOOK>
DI void ring_gemm(f32x4 (&acc)[NT][2], const RingA<NQB, NT, KS, KC_A, NSLOT>& RA, __amdgpu_buffer_rsrc_t ra, __amdgpu_buffer_rsrc_t rb, const char* lds, int wid, int lane, HOOK&& hook) {
  using R = Ring<NQB, NT, KS>;
  constexpr int SK = R::SK, NSTG = R::NSTG, PA = R::PA, FR = R::FR, PD = 5;
  static_assert(NSTG >= 3 && FR > PD + 1 && (NPRE == 0 || NPRE >= 2), "");
  const int kq = wid / NQB;
  bf16x8 B[3][2][2];
  const unsigned voffB = lane * 16 + 2 * kq * 1024;
  auto issueA1 = [&](auto sc, auto ic) __attribute__((always_inline)) { RA.template issue<sc.value, ic.value>(ra); };
  auto issueB1 = [&](auto sc, auto ic) __attribute__((always_inline)) {
    constexpr int s = sc.value, i = ic.value, ks = i >> 1, q = i & 1;
    bload16c(B[s % 3][ks][q], voffB, rb, q * KC_B * 256 + (s * SK + ks) * 1024);
  };
  if constexpr (NPRE == 0) static_for<PA>(LAMBDA(ic) { issueA1(IC<0>{}, ic); });
  static_for<4>(LAMBDA(ic) { issueB1(IC<0>{}, ic); });
  if constexpr (NPRE > 0) { static_for<4>(LAMBDA(ic) { issueB1(IC<1>{}, ic); }); wait_bar4b<4>(B[0]); } else wait_bar4b<0>(B[0]);
  const LAS bf16x8* pl = (const LAS bf16x8*)lds + lane + 2 * kq * NT * 64;
  bf16x8 A[PD + 1];
  auto rd = [&](auto mc) __attribute__((always_inline)) { constexpr int M = mc.value, s = M / FR, m = M % FR; A[M % (PD + 1)] = pl[((s % NSLOT) * PA * 8 + m) * 64]; };
  static_for<PD>(LAMBDA(jc) { rd(jc); });
  SBAR();
  static_for<NSTG>(LAMBDA(sc) {
    constexpr int s = sc.value;
    constexpr int n0 = (NPRE == 0 && s == 0) ? PA + 4 : 0;
    constexpr int sa = s + 2;
    constexpr int nA = (sa < NSTG && sa >= NPRE) ? PA : 0, nB = (s + 2 < NSTG) ? 4 : 0, NI = n0 + nA + nB, NGI = FR - PD - 1;
    constexpr bool full = (s + 1) * SK <= KS;
    static_for<FR>(LAMBDA(mc) {
      constexpr int m = mc.value, M = s * FR + m, ks = m / NT, nt = m % NT;
      if constexpr (m == FR - PD && s + 1 < NSTG) wait_bar4b<nA + nB>(B[(s + 1) % 3]);
      if constexpr (M + PD < NSTG * FR) rd(IC<M + PD>{});
      if constexpr (m < NGI) {
        constexpr int i0 = (m * NI + NGI - 1) / NGI, i1 = ((m + 1) * NI + NGI - 1) / NGI;
        static_for<i1 - i0>(LAMBDA(uc) {
          constexpr int i = i0 + uc.value;
          if constexpr (i < n0) { if constexpr (i < PA) issueA1(IC<1>{}, IC<i>{}); else issueB1(IC<1>{}, IC<i - PA>{}); }
          else if constexpr (i - n0 < nA) issueA1(IC<sa>{}, IC<i - n0>{});
          else issueB1(IC<s + 2>{}, IC<i - n0 - nA>{});
        });
      }
      if constexpr (NHOOK > 0 && M >= (NSTG - 1) * FR - PD) {
        constexpr int j = M - ((NSTG - 1) * FR - PD), HG = FR + PD - 2, i0 = (j * NHOOK + HG - 1) / HG, i1 = ((j + 1) * NHOOK + HG - 1) / HG;
        if constexpr (j < HG) static_for<i1 - i0>(LAMBDA(uc) { hook(IC<i0 + uc.value>{}); });
      }
      if (full || s * SK + 2 * kq + ks < KS) { acc[nt][0] = mfma16(A[M % (PD + 1)], B[s % 3][ks][0], acc[nt][0]); acc[nt][1] = mfma16(A[M % (PD + 1)], B[s % 3][ks][1], acc[nt][1]); }
      SBAR();
    });
  });
}
template <int NT> DI void pair_reduce(f32x4 (&acc)[NT][2], char* lds, int wid, int lane) {
  constexpr int H0 = (NT + 1) / 2;
  f32x4* red = (f32x4*)lds + (size_t)wid * H0 * 2 * 64 + lane;
  const f32x4* in = (const f32x4*)lds + (size_t)(wid ^ 4) * H0 * 2 * 64 + lane;
  lds_barrier();
  if (wid < 4) {
    asm volatile("; lo-send" ::: "memory");
#pragma unroll
    for (int nt = H0; nt < NT; ++nt) { red[((nt - H0) * 2) * 64] = acc[nt][0]; red[((nt - H0) * 2 + 1) * 64] = acc[nt][1]; }
    asm volatile("; lo-sent" ::: "memory");
  } else {
    asm volatile("; hi-send" ::: "memory");
#pragma unroll
    for (int nt = 0; nt < H0; ++nt) { red[(nt * 2) * 64] = acc[nt][0]; red[(nt * 2 + 1) * 64] = acc[nt][1]; }
    asm volatile("; hi-sent" ::: "memory");
  }
  lds_barrier();
  if (wid < 4) {
    asm volatile("; lo-recv" ::: "memory");
#pragma unroll
    for (int nt = 0; nt < H0; ++nt) { acc[nt][0] += in[(nt * 2) * 64]; acc[nt][1] += in[(nt * 2 + 1) * 64]; PIN(acc[nt][0]); PIN(acc[nt][1]); }
    asm volatile("; lo-done" ::: "memory");
  } else {
    asm volatile("; hi-recv" ::: "memory");
#pragma unroll
    for (int nt = H0; nt < NT; ++nt) { acc[nt][0] += in[((nt - H0) * 2) * 64]; acc[nt][1] += in[((nt - H0) * 2 + 1) * 64]; PIN(acc[nt][0]); PIN(acc[nt][1]); }
    asm volatile("; hi-done" ::: "memory");
  }
}

DI void group_arrive(unsigned* cnt) {
  asm volatile("s_waitcnt vmcnt(0)" ::: "memory");
  __syncthreads();
  if (threadIdx.x == 0) __hip_atomic_fetch_add((gu32*)cnt, 1u, __ATOMIC_RELAXED, __HIP_MEMORY_SCOPE_AGENT);
}
DI void group_wait(unsigned* cnt, unsigned want) {
  if (threadIdx.x == 0) {
    gu32* p = (gu32*)cnt;
    unsigned a = __hip_atomic_load(p, RLX_AGENT); __builtin_amdgcn_s_sleep(POLL_SLEEP);
    unsigned b = __hip_atomic_load(p, RLX_AGENT); __builtin_amdgcn_s_sleep(POLL_SLEEP);
    unsigned c = __hip_atomic_load(p, RLX_AGENT); __builtin_amdgcn_s_sleep(POLL_SLEEP);
    unsigned d = __hip_atomic_load(p, RLX_AGENT);
    for (unsigned spins = 0; spins < SPIN_CAP; ++spins) {
      if (a >= want) break;
      a = __hip_atomic_load(p, RLX_AGENT); __builtin_amdgcn_s_sleep(POLL_SLEEP);
      if (b >= want) break;
      b = __hip_atomic_load(p, RLX_AGENT); __builtin_amdgcn_s_sleep(POLL_SLEEP);
      if (c >= want) break;
      c = __hip_atomic_load(p, RLX_AGENT); __builtin_amdgcn_s_sleep(POLL_SLEEP);
      if (d >= want) break;
      d = __hip_atomic_load(p, RLX_AGENT); __builtin_amdgcn_s_sleep(POLL_SLEEP);
    }
  }
  asm volatile("" ::: "memory");
  __syncthreads();
}

__global__ __launch_bounds__(512, 2) void k_tail(const u16* __restrict__ obuf, const u16* __restrict__ wot, const u16* __restrict__ w1t, const u16* __restrict__ w2t,
                                                  const u16* __restrict__ x, const u16* __restrict__ mod, const u16* __restrict__ b1, const u16* __restrict__ b2,
                                                  u16* part, u16* x1, u16* h2, u16* hid, u16* __restrict__ out, unsigned* cnt) {
  extern __shared__ __attribute__((aligned(16))) char lds[];
  const int tid = threadIdx.x, lane = tid & 63, wid = __builtin_amdgcn_readfirstlane(tid >> 6);
  const unsigned lds0 = (unsigned)(uintptr_t)lds;
  const int xcd = blockIdx.x & 7, idx = blockIdx.x >> 3;
  const int rt = (xcd >> 2) * 8 + (idx >> 2), gi = (xcd & 3) * 4 + (idx & 3);
  unsigned* const c1 = cnt + rt * 64, * const c2 = cnt + (16 + rt) * 64, * const c3 = cnt + (32 + rt) * 64;
  const int nft = gi < 15 ? 7 : 3;
  const __amdgpu_buffer_rsrc_t raD = __builtin_amdgcn_make_buffer_rsrc((void*)(w1t + (size_t)(7 * gi) * 80 * 128), 0, nft * 80 * 256, 0x00020000);
  RingA<4, 7, 20, 80, 5> RD; RD.init(lds0, wid, lane);
  {
    const int kq = gi >> 2, j = gi & 3, qb = wid & 3;
    const __amdgpu_buffer_rsrc_t ra = __builtin_amdgcn_make_buffer_rsrc((void*)(wot + ((size_t)(10 * j) * 512 + kq * 128) * 128), 0, 10 * 512 * 256, 0x00020000);
    const __amdgpu_buffer_rsrc_t rb = __builtin_amdgcn_make_buffer_rsrc((void*)(obuf + ((size_t)(8 * rt + 2 * qb) * 512 + kq * 128) * 128), 0, 2 * 512 * 256, 0x00020000);
    const __amdgpu_buffer_rsrc_t rp = __builtin_amdgcn_make_buffer_rsrc((void*)(part + (size_t)kq * SQ * DM), 0, SQ * DM * 2, 0x00020000);
    f32x4 acc[10][2] = {};
    RingA<4, 10, 32, 512, 4> RA; RA.init(lds0, wid, lane);
    ring_gemm<4, 10, 32, 512, 512, 0, 4, 8>(acc, RA, ra, rb, lds, wid, lane, [&](auto ic) __attribute__((always_inline)) { RD.template issue<ic.value / 4, ic.value % 4>(raD); });
    pair_reduce<10>(acc, lds + 81920, wid, lane);
    const int nt0 = wid < 4 ? 0 : 5;
    const unsigned vo = (((8 * rt + 2 * qb + ((lane >> 4) & 1)) * 80 + j * 20 + nt0 * 2 + (lane >> 5)) * 16 + (lane & 15)) * 16;
    auto outp = [&](auto ntc) __attribute__((always_inline)) {
      constexpr int nt = ntc.value, o = nt % 5;
      const u32x2 w0 = {cvtpk(acc[nt][0][0], acc[nt][0][1]), cvtpk(acc[nt][0][2], acc[nt][0][3])}, w1 = {cvtpk(acc[nt][1][0], acc[nt][1][1]), cvtpk(acc[nt][1][2], acc[nt][1][3])};
      st16(rp, vo + o * 2 * 256, pair16(w0, w1));
    };
    if (wid < 4) { asm volatile("; lo-out" ::: "memory"); static_for<5>(LAMBDA(c) { outp(c); }); asm volatile("; lo-end" ::: "memory"); }
    else { asm volatile("; hi-out" ::: "memory"); static_for<5>(LAMBDA(c) { outp(IC<5 + c.value>{}); }); asm volatile("; hi-end" ::: "memory"); }
  }
  group_arrive(c1);
  {
    float* red = (float*)(lds + 98304);
    const int r = tid & 7, row = rt * 128 + gi * 8 + r;
    const bool two = wid < 2;
    const __amdgpu_buffer_rsrc_t rp = __builtin_amdgcn_make_buffer_rsrc((void*)part, 0, 4 * SQ * DM * 2, 0x00020000);
    const __amdgpu_buffer_rsrc_t rx1 = __builtin_amdgcn_make_buffer_rsrc((void*)x1, 0, SQ * DM * 2, 0x00020000);
    const __amdgpu_buffer_rsrc_t rh2 = __builtin_amdgcn_make_buffer_rsrc((void*)h2, 0, SQ * DM * 2, 0x00020000);
    u32x4 p[2][4], xv[2], gv[2], sbv[2], cbv[2]; unsigned blk[2];
#pragma unroll
    for (int u = 0; u < 2; ++u) {
      const int c = (u == 0 || two) ? (tid >> 3) + 64 * u : (tid >> 3);
      blk[u] = (((row >> 4) * 80 + c) * 16 + (row & 15)) * 16;
      xv[u] = *(const u32x4*)(x + (size_t)row * DM + c * 8);
      gv[u] = *(const u32x4*)(mod + 2 * 640 + c * 8); sbv[u] = *(const u32x4*)(mod + 3 * 640 + c * 8); cbv[u] = *(const u32x4*)(mod + 4 * 640 + c * 8);
    }
    RD.pre<2, 3>(raD);
    group_wait(c1, 16);
#pragma unroll
    for (int k = 0; k < 4; ++k) p[0][k] = ld16c(rp, blk[0] + k * SQ * DM * 2);
    if (two) {
#pragma unroll
      for (int k = 0; k < 4; ++k) p[1][k] = ld16c(rp, blk[1] + k * SQ * DM * 2);
    }
    float v[2][8]; float s = 0; u32x4 x1v[2];
    auto resid = [&](auto uc) __attribute__((always_inline)) {
      constexpr int u = uc.value;
#pragma unroll
      for (int e = 0; e < 4; ++e) {
        const float a = (bf_lo(p[u][0][e]) + bf_lo(p[u][1][e])) + (bf_lo(p[u][2][e]) + bf_lo(p[u][3][e])), b = (bf_hi(p[u][0][e]) + bf_hi(p[u][1][e])) + (bf_hi(p[u][2][e]) + bf_hi(p[u][3][e]));
        x1v[u][e] = cvtpk(__builtin_fmaf(bf_lo(gv[u][e]), a, bf_lo(xv[u][e])), __builtin_fmaf(bf_hi(gv[u][e]), b, bf_hi(xv[u][e])));
        v[u][2 * e] = bf_lo(x1v[u][e]); v[u][2 * e + 1] = bf_hi(x1v[u][e]); s += v[u][2 * e] + v[u][2 * e + 1];
      }
    };
    resid(IC<0>{});
    if (two) resid(IC<1>{});
    s = xor_add<32>(xor_add<16>(xor_add<8>(s)));
    if (lane < 8) red[wid * 8 + r] = s;
    lds_barrier();
    float tot = 0;
#pragma unroll
    for (int i = 0; i < 8; ++i) tot += red[i * 8 + r];
    const float mean = tot * (1.f / DM); float q = 0;
#pragma unroll
    for (int e = 0; e < 8; ++e) { v[0][e] -= mean; q += v[0][e] * v[0][e]; }
    if (two) {
#pragma unroll
      for (int e = 0; e < 8; ++e) { v[1][e] -= mean; q += v[1][e] * v[1][e]; }
    }
    q = xor_add<32>(xor_add<16>(xor_add<8>(q)));
    if (lane < 8) red[64 + wid * 8 + r] = q;
    lds_barrier();
    float qt = 0;
#pragma unroll
    for (int i = 0; i < 8; ++i) qt += red[64 + i * 8 + r];
    const float rstd = __builtin_amdgcn_rsqf(qt * (1.f / DM) + 1e-5f);
    auto norm = [&](auto uc) __attribute__((always_inline)) {
      constexpr int u = uc.value;
      u32x4 hv;
#pragma unroll
      for (int e = 0; e < 4; ++e)
        hv[e] = cvtpk(__builtin_fmaf(v[u][2 * e] * rstd, bf_lo(cbv[u][e]), bf_lo(sbv[u][e])), __builtin_fmaf(v[u][2 * e + 1] * rstd, bf_hi(cbv[u][e]), bf_hi(sbv[u][e])));
      st16(rh2, blk[u], hv); st16(rx1, blk[u], x1v[u]);
    };
    norm(IC<0>{});
    if (two) norm(IC<1>{});
  }
  group_arrive(c2);
  const int jE = gi & 7, rtE = rt * 2 + (gi >> 3);
  const __amdgpu_buffer_rsrc_t raE = __builtin_amdgcn_make_buffer_rsrc((void*)(w2t + (size_t)(5 * jE) * 216 * 128), 0, 5 * 216 * 256, 0x00020000);
  RingA<2, 5, 54, 216, 4> RE; RE.init(lds0, wid, lane);
  {
    const int qb = wid & 3, nt0 = wid < 4 ? 0 : 4;
    const __amdgpu_buffer_rsrc_t rb = __builtin_amdgcn_make_buffer_rsrc((void*)(h2 + (size_t)(8 * rt + 2 * qb) * 80 * 128), 0, 2 * 80 * 256, 0x00020000);
    const __amdgpu_buffer_rsrc_t rh = __builtin_amdgcn_make_buffer_rsrc((void*)hid, 0, SQ * FF * 2, 0x00020000);
    u32x2 bv[4];
#pragma unroll
    for (int o = 0; o < 4; ++o) bv[o] = *(const u32x2*)(b1 + (nt0 + o < nft ? (7 * gi + nt0 + o) * 16 + 4 * (lane >> 4) : 0));
    f32x4 acc[7][2] = {};
    group_wait(c2, 16);
    ring_gemm<4, 7, 20, 80, 80, 3, 5, 10>(acc, RD, raD, rb, lds, wid, lane, [&](auto ic) __attribute__((always_inline)) { RE.template issue<ic.value / 5, ic.value % 5>(raE); });
    pair_reduce<7>(acc, lds + 98304, wid, lane);
    const unsigned vo = (((8 * rt + 2 * qb + ((lane >> 4) & 1)) * 216 + (7 * gi + nt0) * 2 + (lane >> 5)) * 16 + (lane & 15)) * 16;
    auto outp = [&](auto ntc, auto oc) __attribute__((always_inline)) {
      constexpr int nt = ntc.value, o = oc.value;
      if (nt < nft) {
        const float b0 = bf_lo(bv[o][0]), b1_ = bf_hi(bv[o][0]), b2_ = bf_lo(bv[o][1]), b3 = bf_hi(bv[o][1]);
        u32x2 w[2];
#pragma unroll
        for (int q = 0; q < 2; ++q) {
          const float v0 = rbf(__builtin_fmaxf(rbf(acc[nt][q][0]) + b0, 0.f)), v1 = rbf(__builtin_fmaxf(rbf(acc[nt][q][1]) + b1_, 0.f));
          const float v2 = rbf(__builtin_fmaxf(rbf(acc[nt][q][2]) + b2_, 0.f)), v3 = rbf(__builtin_fmaxf(rbf(acc[nt][q][3]) + b3, 0.f));
          w[q] = u32x2{cvtpk(v0 * v0, v1 * v1), cvtpk(v2 * v2, v3 * v3)};
        }
        st16(rh, vo + o * 2 * 256, pair16(w[0], w[1]));
      }
    };
    if (wid < 4) { asm volatile("; lo-out" ::: "memory"); static_for<4>(LAMBDA(c) { outp(c, c); }); asm volatile("; lo-end" ::: "memory"); }
    else { asm volatile("; hi-out" ::: "memory"); static_for<3>(LAMBDA(c) { outp(IC<4 + c.value>{}, c); }); asm volatile("; hi-end" ::: "memory"); }
  }
  group_arrive(c3);
  {
    const int qb = wid & 1;
    const __amdgpu_buffer_rsrc_t rb = __builtin_amdgcn_make_buffer_rsrc((void*)(hid + (size_t)(4 * rtE + 2 * qb) * 216 * 128), 0, 2 * 216 * 256, 0x00020000);
    const __amdgpu_buffer_rsrc_t rx1 = __builtin_amdgcn_make_buffer_rsrc((void*)x1, 0, SQ * DM * 2, 0x00020000);
    u32x4 xv[2], gv[2], bv[2];
#pragma unroll
    for (int it = 0; it < 2; ++it) {
      const int item = tid + it * 512, q = item & 63, c = item < 640 ? item >> 6 : 0, row = rtE * 64 + q;
      xv[it] = ld16c(rx1, (((row >> 4) * 80 + jE * 10 + c) * 16 + (row & 15)) * 16);
      gv[it] = *(const u32x4*)(mod + 5 * 640 + jE * 80 + c * 8); bv[it] = *(const u32x4*)(b2 + jE * 80 + c * 8);
    }
    f32x4 acc[5][2] = {};
    RE.pre<2, 3>(raE);
    group_wait(c3, 16);
    ring_gemm<2, 5, 54, 216, 216, 3, 4, 0>(acc, RE, raE, rb, lds, wid, lane, [&](auto) {});
    f32x4* red = (f32x4*)lds;
    constexpr int HI = (3 * 40960 - 4 * 10240) / 16;
    {
      f32x4* mine = red + (wid * 10) * 64 + (wid >= 4 ? HI : 0) + lane;
#pragma unroll
      for (int n = 0; n < 5; ++n) { mine[(n * 2) * 64] = acc[n][0]; mine[(n * 2 + 1) * 64] = acc[n][1]; }
    }
    lds_barrier();
#pragma unroll
    for (int it = 0; it < 2; ++it) {
      const int item = tid + it * 512;
      if (item < 640) {
        const int q = item & 63, c = item >> 6, n = c >> 1, l0 = (q & 15) + 32 * (c & 1), base = (((q >> 5) * 5 + n) * 2 + ((q >> 4) & 1)) * 64 + l0;
        f32x4 lo = {0, 0, 0, 0}, hi = {0, 0, 0, 0};
#pragma unroll
        for (int w = 0; w < 4; ++w) { const int o_ = base + w * 2 * 5 * 2 * 64 + (w >= 2 ? HI : 0); lo += red[o_]; hi += red[o_ + 16]; }
        const float v[8] = {lo[0], lo[1], lo[2], lo[3], hi[0], hi[1], hi[2], hi[3]};
        u32x4 o;
#pragma unroll
        for (int e = 0; e < 4; ++e) {
          const float a = bf_lo(xv[it][e]) + rbf(bf_lo(gv[it][e]) * rbf(rbf(v[2 * e]) + bf_lo(bv[it][e]))), b = bf_hi(xv[it][e]) + rbf(bf_hi(gv[it][e]) * rbf(rbf(v[2 * e + 1]) + bf_hi(bv[it][e])));
          o[e] = cvtpk(a, b);
        }
        *(u32x4*)(out + (size_t)(rtE * 64 + q) * DM + jE * 80 + c * 8) = o;
      }
    }
  }
}

extern "C" void kernel_launch(void* const* d_in, const int* in_sizes, int n_in, void* d_out, int out_size, void* d_ws, size_t ws_size, hipStream_t stream) {
  const u16 *x = (const u16*)d_in[0], *kc = (const u16*)d_in[1], *vc = (const u16*)d_in[2], *wq = (const u16*)d_in[3], *wo = (const u16*)d_in[4];
  const u16 *w1 = (const u16*)d_in[6], *b1 = (const u16*)d_in[7], *w2 = (const u16*)d_in[8], *b2 = (const u16*)d_in[9], *c = (const u16*)d_in[10], *wmod = (const u16*)d_in[11];
  char* ws = (char*)d_ws;
  u16 *mod = (u16*)(ws + WS_MOD), *h = (u16*)(ws + WS_H), *o = (u16*)(ws + WS_O), *wot = (u16*)(ws + WS_WOT), *w1t = (u16*)(ws + WS_W1T), *w2t = (u16*)(ws + WS_W2T);
  u16 *x1 = (u16*)(ws + WS_X1), *hid = (u16*)(ws + WS_HID);
  u16 *h2 = (u16*)(ws + WS_H2), *part = (u16*)(ws + WS_P);
  (void)hipFuncSetAttribute((const void*)k_attn, hipFuncAttributeMaxDynamicSharedMemorySize, (NS + 2) * SLOT);
  unsigned* cnt = (unsigned*)(ws + WS_CNT);
  (void)hipFuncSetAttribute((const void*)k_tail, hipFuncAttributeMaxDynamicSharedMemorySize, 163840);
  k_front<<<FR_N, 512, 0, stream>>>(c, wmod, x, mod, h, (unsigned*)(ws + WS_SYNC), cnt);
  k_attn<<<256, 512, (NS + 2) * SLOT, stream>>>(h, wq, kc, vc, o, wo, w1, w2, wot);
  k_tail<<<256, 512, 163840, stream>>>(o, wot, w1t, w2t, x, mod, b1, b2, part, x1, h2, hid, (u16*)d_out, cnt);
}
```

```cpp
#include <hip/hip_runtime.h>
#include <stdint.h>
#include <utility>

typedef unsigned short u16;
typedef short bf16x8 __attribute__((ext_vector_type(8)));
typedef short s16x4 __attribute__((ext_vector_type(4)));
typedef float f32x16 __attribute__((ext_vector_type(16)));
typedef float f32x4 __attribute__((ext_vector_type(4)));
typedef float f32x2 __attribute__((ext_vector_type(2)));
typedef __bf16 bf16x2 __attribute__((ext_vector_type(2)));
typedef unsigned u32x4 __attribute__((ext_vector_type(4)));
typedef unsigned u32x2 __attribute__((ext_vector_type(2)));
#define DI __device__ __forceinline__
#define GAS __attribute__((address_space(1)))
#define LAS __attribute__((address_space(3)))
#define SBAR() __builtin_amdgcn_sched_barrier(0)
#define PIN(x) asm volatile("" : "+v"(x))

constexpr int SQ = 2048, DM = 640, NH = 32, DH = 128, SK = 2048, WIN = 512, FF = 1728;
constexpr size_t WS_MOD = 0;
constexpr size_t WS_CNT = 8192;
constexpr size_t WS_SYNC = 32768;
constexpr size_t WS_H = 131072;
constexpr size_t WS_O = WS_H + (size_t)SQ * DM * 2;
constexpr size_t WS_WOT = WS_O + (size_t)SQ * 4096 * 2;
constexpr size_t WS_W1T = WS_WOT + (size_t)4096 * DM * 2;
constexpr size_t WS_W2T = WS_W1T + (size_t)DM * FF * 2;
constexpr size_t WS_X1 = WS_W2T + (size_t)DM * FF * 2 + 65536;
constexpr size_t WS_HID = WS_X1 + (size_t)SQ * DM * 2;
constexpr size_t WS_H2 = WS_HID + (size_t)SQ * FF * 2 + 65536;
constexpr size_t WS_P = WS_H2 + (size_t)SQ * DM * 2;
constexpr size_t WS_END = WS_P + (size_t)4 * SQ * DM * 2;

template <int... I, class F> DI void static_for_impl(std::integer_sequence<int, I...>, F&& f) { (f(std::integral_constant<int, I>{}), ...); }
template <int N, class F> DI void static_for(F&& f) { static_for_impl(std::make_integer_sequence<int, N>{}, f); }
#define LAMBDA(ic) [&](auto ic) __attribute__((always_inline))

DI float bf_lo(unsigned w) { return __uint_as_float(w << 16); }
DI float bf_hi(unsigned w) { return __uint_as_float(w & 0xffff0000u); }
DI float bf2f(u16 v) { return __uint_as_float((unsigned)v << 16); }
DI unsigned cvtpk(float lo, float hi) { f32x2 v = {lo, hi}; bf16x2 b = __builtin_convertvector(v, bf16x2); return __builtin_bit_cast(unsigned, b); }
DI u16 f2bf(float x) { return (u16)(cvtpk(x, 0.f) & 0xffffu); }
DI float rbf(float x) { return bf_lo(cvtpk(x, 0.f)); }
DI f32x16 mfma32(bf16x8 a, bf16x8 b, f32x16 c) { return __builtin_amdgcn_mfma_f32_32x32x16_bf16(a, b, c, 0, 0, 0); }
DI f32x4 mfma16(bf16x8 a, bf16x8 b, f32x4 c) { return __builtin_amdgcn_mfma_f32_16x16x32_bf16(a, b, c, 0, 0, 0); }
DI float swap_max(float m) { auto r = __builtin_amdgcn_permlane32_swap(__float_as_uint(m), __float_as_uint(m), false, false); return __builtin_fmaxf(__uint_as_float(r[0]), __uint_as_float(r[1])); }
DI float swap_add(float m) { auto r = __builtin_amdgcn_permlane32_swap(__float_as_uint(m), __float_as_uint(m), false, false); return __uint_as_float(r[0]) + __uint_as_float(r[1]); }

DI void lds_barrier() { asm volatile("s_waitcnt lgkmcnt(0)\n\ts_barrier" ::: "memory"); }
template <int CTRL> DI float dpp_mov(float v) { return __builtin_bit_cast(float, __builtin_amdgcn_update_dpp(0, __builtin_bit_cast(int, v), CTRL, 0xf, 0xf, true)); }
template <int M> DI float xor_add(float v) {
  if constexpr (M == 1) return v + dpp_mov<0xB1>(v);
  else if constexpr (M == 2) return v + dpp_mov<0x4E>(v);
  else if constexpr (M == 4) return v + dpp_mov<0x141>(dpp_mov<0x1B>(v));
  else if constexpr (M == 8) return v + dpp_mov<0x128>(v);
  else if constexpr (M == 16) { const auto r = __builtin_amdgcn_permlane16_swap(__float_as_uint(v), __float_as_uint(v), false, false); return __uint_as_float(r[0]) + __uint_as_float(r[1]); }
  else { const auto r = __builtin_amdgcn_permlane32_swap(__float_as_uint(v), __float_as_uint(v), false, false); return __uint_as_float(r[0]) + __uint_as_float(r[1]); }
}
typedef GAS unsigned gu32;
typedef GAS unsigned long long gu64;
#define RLX_AGENT __ATOMIC_RELAXED, __HIP_MEMORY_SCOPE_AGENT
constexpr int FR_NP = 120, FR_NL = 128, FR_N = FR_NP + FR_NL;
constexpr unsigned SPIN_CAP = 1u << 18;
#define H_AUX 0
#define POLL_SLEEP 4
#define FR_SLEEP 4
constexpr int SC1 = 16;
__global__ __launch_bounds__(512) void k_front(const u16* __restrict__ c, const u16* __restrict__ wmod, const u16* __restrict__ x, u16* __restrict__ mod, u16* __restrict__ h,
                                               unsigned* sync  , unsigned* __restrict__ cnt) {
  __shared__ float sm[1280];
  const int t = threadIdx.x, b = blockIdx.x;
  if (b < FR_NP) {
    const int cc = t & 3, r0 = t >> 2, col0 = b * 32 + cc * 8;
    u32x4 w[5];
#pragma unroll
    for (int i = 0; i < 5; ++i) w[i] = *(const u32x4*)(wmod + (size_t)(r0 + 128 * i) * 3840 + col0);
    float* sil = sm + 512;
    const unsigned cv = *(const unsigned*)(c + 2 * (t < 320 ? t : 0));
    unsigned tag = __hip_atomic_load((gu32*)sync, RLX_AGENT) + 1u;
    if (t < 320) {
      const float v0 = bf_lo(cv), v1 = bf_hi(cv);
      const unsigned pk = cvtpk(v0 * __builtin_amdgcn_rcpf(1.f + __expf(-v0)), v1 * __builtin_amdgcn_rcpf(1.f + __expf(-v1)));
      *(f32x2*)(sil + 2 * t) = f32x2{bf_lo(pk), bf_hi(pk)};
    }
    if (b == FR_NP - 1 && t < 48) cnt[t * 64] = 0u;
    PIN(tag);
    lds_barrier();
    if (t == 0) __hip_atomic_store((gu32*)sync + 64 + b, tag, RLX_AGENT);
    PIN(w[4]);
    float a[8] = {};
#pragma unroll
    for (int i = 0; i < 5; ++i) {
      const float sv = sil[r0 + 128 * i];
#pragma unroll
      for (int j = 0; j < 4; ++j) { a[2 * j] += sv * bf_lo(w[i][j]); a[2 * j + 1] += sv * bf_hi(w[i][j]); }
    }
#pragma unroll
    for (int j = 0; j < 8; ++j) a[j] = xor_add<32>(xor_add<16>(xor_add<8>(xor_add<4>(a[j]))));
    if ((t & 63) < 4) {
#pragma unroll
      for (int j = 0; j < 8; ++j) sm[(t >> 6) * 32 + cc * 8 + j] = a[j];
    }
    lds_barrier();
    if (t < 128) {
      const int u = t & 15;
      float lo = 0.f, hi = 0.f;
#pragma unroll
      for (int w8 = 0; w8 < 8; ++w8) { lo += sm[w8 * 32 + 2 * u]; hi += sm[w8 * 32 + 2 * u + 1]; }
      const bool onep = (b >= 20 && b < 40) || (b >= 80 && b < 100);
      unsigned pk = cvtpk(lo, hi);
      if (onep) pk = cvtpk(1.f + bf_lo(pk), 1.f + bf_hi(pk));
      if (b < 40) __hip_atomic_store((gu64*)(sync + 2048 + (t >> 4) * 2048) + b * 16 + u, ((unsigned long long)tag << 32) | pk, RLX_AGENT);
      if (t < 16) *(unsigned*)(mod + b * 32 + 2 * u) = pk;
    }
    if (b == FR_NP - 1 && t < 64) {
      bool ok = false;
      for (unsigned spins = 0; spins < SPIN_CAP && !ok; ++spins) {
        bool all = true;
#pragma unroll
        for (int k = 0; k < 4; ++k) { const int w = t + 64 * k; if (w < FR_N) all &= __hip_atomic_load((gu32*)sync + 64 + w, RLX_AGENT) == tag; }
        ok = __all(all);
        if (!ok) __builtin_amdgcn_s_sleep(2);
      }
      if (t == 0) __hip_atomic_store((gu32*)sync, tag, RLX_AGENT);
    }
  } else {
    if (t >= 256) return;
    const int i = t & 15, row = (b - FR_NP) * 16 + (t >> 4);
    u32x4 v[5];
#pragma unroll
    for (int j = 0; j < 5; ++j) v[j] = *(const u32x4*)(x + (size_t)row * DM + (i + 16 * j) * 8);
    const unsigned tag = __hip_atomic_load((gu32*)sync, RLX_AGENT) + 1u;
    if (t == 0) __hip_atomic_store((gu32*)sync + 64 + b, tag, RLX_AGENT);
    float f[40]; float s = 0;
#pragma unroll
    for (int j = 0; j < 5; ++j)
#pragma unroll
      for (int e = 0; e < 4; ++e) { f[j * 8 + 2 * e] = bf_lo(v[j][e]); f[j * 8 + 2 * e + 1] = bf_hi(v[j][e]); s += f[j * 8 + 2 * e] + f[j * 8 + 2 * e + 1]; }
    s = xor_add<8>(xor_add<4>(xor_add<2>(xor_add<1>(s))));
    const float mean = s * (1.f / DM); float q = 0;
#pragma unroll
    for (int e = 0; e < 40; ++e) { const float d = f[e] - mean; q += d * d; }
    q = xor_add<8>(xor_add<4>(xor_add<2>(xor_add<1>(q))));
    const float mb = rbf(mean), rsd = 1.f / rbf(sqrtf(rbf(rbf(q * (1.f / DM)) + 1e-5f)));
#pragma unroll
    for (int e = 0; e < 40; e += 2) {
      const unsigned d = cvtpk(f[e] - mb, f[e + 1] - mb), n = cvtpk(bf_lo(d) * rsd, bf_hi(d) * rsd);
      f[e] = bf_lo(n); f[e + 1] = bf_hi(n);
    }
    if (t < 64) {
      const __amdgpu_buffer_rsrc_t rg = __builtin_amdgcn_make_buffer_rsrc((void*)(sync + 2048 + (b & 7) * 2048), 0, 5120, 0x00020000);
      u32x4 gv[5];
      for (unsigned spins = 0; spins < SPIN_CAP; ++spins) {
        bool ok = true;
#pragma unroll
        for (int k = 0; k < 5; ++k) { gv[k] = __builtin_amdgcn_raw_buffer_load_b128(rg, (t + 64 * k) * 16, 0, SC1); ok &= gv[k][1] == tag && gv[k][3] == tag; }
        if (__all(ok)) break;
        __builtin_amdgcn_s_sleep(FR_SLEEP);
      }
#pragma unroll
      for (int k = 0; k < 5; ++k) {
        const int g = (t + 64 * k) * 2;
        *(f32x4*)(sm + 2 * g) = f32x4{bf_lo(gv[k][0]), bf_hi(gv[k][0]), bf_lo(gv[k][2]), bf_hi(gv[k][2])};
      }
    }
    __syncthreads();
#pragma unroll
    for (int j = 0; j < 5; ++j) {
      const int k0 = (i + 16 * j) * 8;
      const f32x4 s0 = *(const f32x4*)(sm + k0), s1 = *(const f32x4*)(sm + k0 + 4), c0 = *(const f32x4*)(sm + 640 + k0), c1 = *(const f32x4*)(sm + 640 + k0 + 4);
      const float sa[8] = {s0[0], s0[1], s0[2], s0[3], s1[0], s1[1], s1[2], s1[3]}, oc[8] = {c0[0], c0[1], c0[2], c0[3], c1[0], c1[1], c1[2], c1[3]};
      u32x4 o;
#pragma unroll
      for (int e = 0; e < 4; ++e) {
        const unsigned m = cvtpk(f[j * 8 + 2 * e] * oc[2 * e], f[j * 8 + 2 * e + 1] * oc[2 * e + 1]);
        o[e] = cvtpk(bf_lo(m) + sa[2 * e], bf_hi(m) + sa[2 * e + 1]);
      }
      __builtin_amdgcn_raw_buffer_store_b128(o, __builtin_amdgcn_make_buffer_rsrc((void*)h, 0, SQ * DM * 2, 0x00020000), (((row >> 4) * 80 + (i + 16 * j)) * 16 + (row & 15)) * 16, 0, H_AUX);
    }
  }
}

DI u32x4 pair16(u32x2 x, u32x2 y) {
  const auto a = __builtin_amdgcn_permlane16_swap(x[0], y[0], false, false), b = __builtin_amdgcn_permlane16_swap(x[1], y[1], false, false);
  return u32x4{a[0], b[0], a[1], b[1]};
}
#define P1_32 1
#define A_NS 8
#define A_PD 5
#define O_AUX 16
constexpr int NS = A_NS, SLOT = 16384;
constexpr float QSCALE = 0.088388347648318440f * 1.4426950408889634f;
constexpr float PBIAS = 96.f;
#define PLIM_V 1073741824.f
constexpr float PLIM = PLIM_V;
constexpr int tile_K(int t) { return t == 0 ? 10 : 9 + 2 * t; }
constexpr int tile_V(int t) { return t == 7 ? 25 : 12 + 2 * t; }
constexpr int slot_of(int idx) { return (idx % NS) * SLOT; }
constexpr int lastuse(int idx) { return idx < 10 ? idx : (idx == 10 ? 10 : (idx == 25 ? 18 : ((idx & 1) ? 10 + (idx - 9) / 2 : 11 + (idx - 12) / 2))); }
constexpr int need_bar(int idx) {
  if (idx < 10) return idx == 0 ? 0 : idx - 1;
  if (idx == 10) return 9;
  if (idx == 25) return 17;
  return (idx & 1) ? 10 + (idx - 9) / 2 - 1 : 11 + (idx - 12) / 2;
}
constexpr int rt_step(int u) { return u == 0 ? 9 : (u == 1 ? 10 : 12); }
constexpr int rt_proc(int u) { return u == 0 ? 11 : (u == 1 ? 12 : 14); }
constexpr int rt_slot(int u) { return (u == 1 ? NS + 1 : NS) * SLOT; }
struct Sched { int npro; int pro[16]; int nops[19]; int op[19][16]; int wait[19]; };
constexpr Sched make_sched() {
  Sched p{}; int pos = 0; int tile_end[26] = {}; int h_end[10] = {}; int u_end[3] = {}; int next_tile = 3;
  auto pro_tile = [&](int i) { p.pro[p.npro++] = 2 * i; p.pro[p.npro++] = 2 * i + 1; pos += 2; tile_end[i] = pos; };
  auto pro_h = [&](int i) { for (int k = 0; k < 4; ++k) p.pro[p.npro++] = 100 + 4 * i + k; pos += 4; h_end[i] = pos; };
  pro_tile(0); pro_h(0); pro_tile(1); pro_h(1); pro_tile(2);
  for (int g = 0; g < 19; ++g) {
    int need = 0;
    for (int idx = 0; idx < next_tile; ++idx) if (need_bar(idx) <= g && tile_end[idx] > need) need = tile_end[idx];
    if (g < 10 && h_end[g] > need) need = h_end[g];
    for (int u = 0; u < 3; ++u) if (rt_proc(u) <= g && u_end[u] > need) need = u_end[u];
    p.wait[g] = pos - need;
    int n = 0;
    for (int u = 0; u < 3; ++u) if (g == rt_proc(u)) { p.op[g][n++] = 300 + 2 * u; p.op[g][n++] = 301 + 2 * u; }
    if (g == 0) { for (int k = 0; k < 4; ++k) p.op[g][n++] = 100 + 4 * 2 + k; pos += 4; h_end[2] = pos; }
    const int maxt = g < 7 ? 1 : 2;
    for (int c = 0; c < maxt && next_tile < 26; ++c) {
      if (next_tile >= NS && lastuse(next_tile - NS) >= g) break;
      p.op[g][n++] = 2 * next_tile; p.op[g][n++] = 2 * next_tile + 1; pos += 2; tile_end[next_tile] = pos; ++next_tile;
    }
    if (g + 3 < 10) { for (int k = 0; k < 4; ++k) p.op[g][n++] = 100 + 4 * (g + 3) + k; pos += 4; h_end[g + 3] = pos; }
    for (int u = 0; u < 3; ++u) if (g == rt_step(u)) { p.op[g][n++] = 200 + 2 * u; p.op[g][n++] = 201 + 2 * u; pos += 2; u_end[u] = pos; }
    for (int u = 0; u < 3; ++u) if (g == rt_proc(u)) { p.op[g][n++] = 310 + 2 * u; p.op[g][n++] = 311 + 2 * u; pos += 2; }
    p.nops[g] = n;
  }
  for (int idx = 0; idx < 26; ++idx) if (tile_end[idx] == 0) p.wait[0] = -1;
  return p;
}
constexpr Sched SCH = make_sched();
static_assert(SCH.wait[0] >= 0, "schedule incomplete");
constexpr bool sched_ok() {
  int step_of[26] = {}; for (int i = 0; i < 26; ++i) step_of[i] = -1;
  for (int g = 0; g < 19; ++g) for (int j = 0; j < SCH.nops[g]; ++j) if (SCH.op[g][j] < 100) step_of[SCH.op[g][j] >> 1] = g;
  for (int i = 3; i < 26; ++i) if (step_of[i] >= need_bar(i)) return false;
  return true;
}
static_assert(sched_ok(), "a tile is issued too late");

DI void dma16(unsigned ldsaddr, unsigned voff, __amdgpu_buffer_rsrc_t srd, unsigned soff) {
  asm volatile("s_mov_b32 m0, %0\n\ts_nop 0\n\tbuffer_load_dwordx4 %1, %2, %3 offen lds" ::"s"(ldsaddr), "v"(voff), "s"(srd), "s"(soff) : "memory");
}
template <int OFF> DI void bload16(bf16x8& dst, unsigned voff, __amdgpu_buffer_rsrc_t srd, unsigned soff) {
  asm volatile("buffer_load_dwordx4 %0, %1, %2, %3 offen offset:%4" : "=v"(dst) : "v"(voff), "s"(srd), "s"(soff), "i"(OFF) : "memory");
}
typedef LAS const char* lds_cptr;
typedef short v4i16_t __attribute__((ext_vector_type(4)));
DI s16x4 vtr(lds_cptr p) { return __builtin_bit_cast(s16x4, __builtin_amdgcn_ds_read_tr16_b64_v4i16((LAS v4i16_t*)p)); }
DI bf16x8 trfrag(lds_cptr p0, lds_cptr p1) { const s16x4 lo = vtr(p0), hi = vtr(p1); return __builtin_shufflevector(lo, hi, 0, 1, 2, 3, 4, 5, 6, 7); }
DI bf16x8 rowfrag(lds_cptr p) { return *(LAS const bf16x8*)p; }
DI float max3(float a, float b, float c) { return __builtin_fmaxf(__builtin_fmaxf(a, b), c); }

template <int N> DI void wait_bar4(bf16x8 (&b)[4]) {
  asm volatile("s_waitcnt vmcnt(%4)\n\ts_barrier" : "+v"(b[0]), "+v"(b[1]), "+v"(b[2]), "+v"(b[3]) : "i"(N) : "memory");
}
template <int N> DI void wait_bar() { asm volatile("s_waitcnt vmcnt(%0)\n\ts_barrier" ::"i"(N) : "memory"); }
template <int V> using IC = std::integral_constant<int, V>;

__global__ __launch_bounds__(512, 2) void k_attn(const u16* __restrict__ hbuf, const u16* __restrict__ wq, const u16* __restrict__ kc, const u16* __restrict__ vc, u16* __restrict__ obuf,
    const u16* __restrict__ wo, const u16* __restrict__ w1, const u16* __restrict__ w2, u16* __restrict__ wot) {
  extern __shared__ __attribute__((aligned(16))) char lds[];
  const int tid = threadIdx.x, lane = tid & 63, r32 = lane & 31, hi = lane >> 5;
  const int wid = __builtin_amdgcn_readfirstlane(tid >> 6);
  const int xcd = blockIdx.x & 7, idx = blockIdx.x >> 3;
  const int head = (xcd & 3) * 8 + (idx & 7), qt = (xcd >> 2) * 4 + (idx >> 3);
  const unsigned lds0 = (unsigned)(uintptr_t)lds;
  const __amdgpu_buffer_rsrc_t srdW = __builtin_amdgcn_make_buffer_rsrc((void*)(wq + head * DH), 0, (DM - 1) * 8192 + 256, 0x00020000);
  const __amdgpu_buffer_rsrc_t srdK = __builtin_amdgcn_make_buffer_rsrc((void*)(kc + ((size_t)head * SK + (SK - WIN)) * DH), 0, WIN * 256, 0x00020000);
  const __amdgpu_buffer_rsrc_t srdV = __builtin_amdgcn_make_buffer_rsrc((void*)(vc + ((size_t)head * SK + (SK - WIN)) * DH), 0, WIN * 256, 0x00020000);
  const __amdgpu_buffer_rsrc_t srdH = __builtin_amdgcn_make_buffer_rsrc((void*)(hbuf + (size_t)(qt * 16 + 2 * wid) * 80 * 128), 0, 2 * 80 * 256, 0x00020000);
  const int dr = (lane >> 2) & 7, dx = lane & 3, dc = lane >> 5, dz = (2 * wid + (dr >> 2)) & 3;
  const unsigned voffW = (unsigned)((8 * wid + dr) * 8192 + 64 * dc + 16 * (dx ^ dz));
  const unsigned voffKV = (unsigned)((8 * wid + dr) * 256 + 64 * dc + 16 * (dx ^ dz));
  const unsigned ldsw = lds0 + 2048 * wid;
  const unsigned voffH = P1_32 ? (unsigned)(((lane >> 4) & 1) * 20480 + (lane >> 5) * 256 + (lane & 15) * 16) : (unsigned)(lane * 16);
  bf16x8 hf[10][4], rtf[2];
  __amdgpu_buffer_rsrc_t rsrc_u[3]; unsigned pitch_u[3], dst_u[3];
  const __amdgpu_buffer_rsrc_t srdT = __builtin_amdgcn_make_buffer_rsrc((void*)wot, 0, (int)(WS_W2T - WS_WOT) + DM * FF * 2, 0x00020000);
  auto vmem_op = [&](auto oc) __attribute__((always_inline)) {
    constexpr int op = oc.value;
    if constexpr (op >= 310) {
      constexpr int u = (op - 310) >> 1, ks = op & 1;
      __builtin_amdgcn_raw_buffer_store_b128(__builtin_bit_cast(u32x4, rtf[ks]), srdT, lane * 16 + ks * 1024, dst_u[u], O_AUX);
    } else if constexpr (op >= 300) {
      constexpr int u = (op - 300) >> 1, ks = op & 1;
      const int g4 = lane >> 4, chq = 2 * wid + ((lane & 3) >> 1), q4_ = (lane & 15) >> 2;
      const lds_cptr p0 = (lds_cptr)lds + rt_slot(u) + 2048 * (4 * ks + g4) + 512 * (chq >> 2) + 64 * q4_ + 16 * ((chq & 3) ^ ((2 * g4) & 3)) + 8 * (lane & 1);
      const lds_cptr p1 = (lds_cptr)lds + rt_slot(u) + 2048 * (4 * ks + g4) + 512 * (chq >> 2) + 64 * (4 + q4_) + 16 * ((chq & 3) ^ ((2 * g4 + 1) & 3)) + 8 * (lane & 1);
      rtf[ks] = trfrag(p0, p1);
    } else if constexpr (op >= 200) {
      constexpr int u = (op - 200) >> 1, half = op & 1;
      dma16(ldsw + rt_slot(u) + 1024 * half, (unsigned)(8 * wid + dr) * pitch_u[u] + (unsigned)(64 * dc + 16 * (dx ^ dz)), rsrc_u[u], 128 * half);
    } else if constexpr (op >= 100) { constexpr int i = (op - 100) >> 2, k = (op - 100) & 3; if constexpr (P1_32) bload16<512 * k>(hf[i][k], voffH, srdH, i * 2048); else bload16<1024 * (k >> 1)>(hf[i][k], voffH, srdH, i * 2048 + (k & 1) * 20480); }
    else {
      constexpr int i = op >> 1, half = op & 1; constexpr unsigned so = slot_of(i) + 1024 * half;
      if constexpr (i < 10) dma16(ldsw + so, voffW, srdW, i * 64 * 8192 + 128 * half);
      else {
        constexpr bool isK = (i == 10) || (i != 25 && (i & 1)); constexpr int t = i == 10 ? 0 : (i == 25 ? 7 : (isK ? (i - 9) / 2 : (i - 12) / 2));
        dma16(ldsw + so, voffKV, isK ? srdK : srdV, t * 16384 + 128 * half);
      }
    }
  };
  static_for<SCH.npro>(LAMBDA(jc) { vmem_op(IC<SCH.pro[jc.value]>{}); });
  SBAR();
#pragma unroll
  for (int u = 0; u < 3; ++u) {
    const int id = blockIdx.x + 256 * u;
    const u16* src; int pitch, rows, kt, nt, kc, nsub; unsigned dbase;
    if (id < 320) { src = wo; pitch = 1280; rows = 4096; kt = id / 5; nt = id - kt * 5; kc = 512; nsub = 40; dbase = 0; }
    else if (id < 455) { const int v = id - 320; src = w2; pitch = 1280; rows = 1728; kt = v / 5; nt = v - kt * 5; kc = 216; nsub = 40; dbase = (unsigned)(WS_W2T - WS_WOT); }
    else { const int v = id < 595 ? id - 455 : 0; src = w1; pitch = 3456; rows = id < 595 ? 640 : 0; kt = v / 14; nt = v - kt * 14; kc = 80; nsub = id < 595 ? 108 : 0; dbase = (unsigned)(WS_W1T - WS_WOT); }
    const int first = kt * 64 * pitch + nt * 256;
    rsrc_u[u] = __builtin_amdgcn_make_buffer_rsrc((void*)((const char*)src + first), 0, rows ? rows * pitch - first : 0, 0x00020000);
    pitch_u[u] = pitch;
    dst_u[u] = nt * 8 + wid < nsub ? dbase + ((nt * 8 + wid) * kc + kt * 8) * 256 : 0x7ff00000u;
  }
  auto vmem_gap = [&](auto gc, auto jc, auto nc) __attribute__((always_inline)) {
    constexpr int g = gc.value, j = jc.value, n = nc.value, no = SCH.nops[g];
    constexpr int i0 = (j * no + n - 1) / n, i1 = ((j + 1) * no + n - 1) / n;
    static_for<i1 - i0>(LAMBDA(uc) { vmem_op(IC<SCH.op[g][i0 + uc.value]>{}); });
  };

  const int blk = (lane >> 4) & 1, q4 = (lane & 15) >> 2, p4 = lane & 3;
  constexpr int NR = (NS * SLOT + 65535) / 65536;
  const lds_cptr L = (lds_cptr)lds;
  lds_cptr wb[NR][4], vb[NR][2], kb_[NR][2];
#pragma unroll
  for (int r = 0; r < NR; ++r)
#pragma unroll
    for (int t = 0; t < 2; ++t) {
#pragma unroll
      for (int par = 0; par < 2; ++par) {
        const int g4 = lane >> 4;
        wb[r][2 * par + t] = P1_32 ? L + 65536 * r + 2048 * hi + 64 * (4 * t + q4) + 16 * ((2 * blk + (p4 & 1)) ^ (2 * hi + t)) + 8 * (p4 >> 1)
                                   : L + 65536 * r + 2048 * g4 + 64 * (4 * t + q4) + 16 * ((2 * par + (p4 >> 1)) ^ ((2 * g4 + t) & 3)) + 8 * (p4 & 1);
        PIN(wb[r][2 * par + t]);
      }
      vb[r][t] = L + 65536 * r + 64 * (4 * hi + q4) + 16 * ((2 * blk + (p4 & 1)) ^ (2 * t + hi)) + 8 * (p4 >> 1) + 2048 * t;
      kb_[r][t] = L + 65536 * r + 2048 * (r32 >> 3) + 64 * (r32 & 7) + 16 * ((2 * t + hi) ^ ((r32 >> 2) & 3));
    }
  constexpr int PD = A_PD, NG = 416;
  bf16x8 fr[PD + 1];
  auto ldfrag_g = [&](auto Gc) __attribute__((always_inline)) {
    constexpr int G = Gc.value;
    if constexpr (G < NG) {
      if constexpr (G < 160) {
        if constexpr (P1_32) {
          constexpr int i = G >> 4, ks = (G >> 2) & 3, db = G & 3, o = slot_of(i) + 4096 * ks + 512 * db;
          fr[G % (PD + 1)] = trfrag(wb[o >> 16][0] + (o & 65535), wb[o >> 16][1] + (o & 65535));
        } else {
          constexpr int i = G >> 4, ks = (G >> 3) & 1, dt = G & 7, o = slot_of(i) + 8192 * ks + 512 * (dt >> 1);
          fr[G % (PD + 1)] = trfrag(wb[o >> 16][2 * (dt & 1)] + (o & 65535), wb[o >> 16][2 * (dt & 1) + 1] + (o & 65535));
        }
      } else {
        constexpr int u = G - 176, t = G < 176 ? 0 : (G >= 400 ? 8 : 1 + u / 32), g = G < 176 ? G - 160 : (G >= 400 ? G - 400 + 16 : u % 32);
        if constexpr (g < 16) {
          constexpr int kb = g >> 3, s = g & 7, o = slot_of(tile_K(t)) + 8192 * kb + 512 * (s >> 1);
          fr[G % (PD + 1)] = rowfrag(kb_[o >> 16][s & 1] + (o & 65535));
        } else {
          constexpr int gg = g - 16, ks = t == 8 ? (gg & 3) : (gg >> 2), db = t == 8 ? (gg >> 2) : (gg & 3), o = slot_of(tile_V(t - 1)) + 4096 * ks + 512 * db;
          fr[G % (PD + 1)] = trfrag(vb[o >> 16][0] + (o & 65535), vb[o >> 16][1] + (o & 65535));
        }
      }
    }
  };
  auto barrier = [&](auto gc) __attribute__((always_inline)) {
    constexpr int g = gc.value;
    if constexpr (g < 10) wait_bar4<SCH.wait[g]>(hf[g]); else wait_bar<SCH.wait[g]>();
    SBAR();
  };

  f32x16 acc[4] = {};
  static_for<10>(LAMBDA(ic) {
    constexpr int i = ic.value;
    barrier(ic);
    if constexpr (i == 0) { static_for<PD>(LAMBDA(jc) { ldfrag_g(jc); }); SBAR(); }
    static_for<16>(LAMBDA(mc) {
      constexpr int m = mc.value, G = 16 * i + m;
      ldfrag_g(IC<G + PD>{});
      vmem_gap(ic, mc, IC<16>{});
      acc[m & 3] = mfma32(fr[G % (PD + 1)], hf[i][m >> 2], acc[m & 3]);
      SBAR();
    });
  });
  bf16x8 qf[8];
#pragma unroll
  for (int s = 0; s < 8; ++s) {
    u32x4 w;
#pragma unroll
    for (int e = 0; e < 4; ++e) w[e] = cvtpk(acc[s >> 1][8 * (s & 1) + 2 * e] * QSCALE, acc[s >> 1][8 * (s & 1) + 2 * e + 1] * QSCALE);
    qf[s] = __builtin_bit_cast(bf16x8, w);
  }
#pragma unroll
  for (int r = 0; r < NR; ++r)
#pragma unroll
    for (int t = 0; t < 2; ++t) { PIN(vb[r][t]); PIN(kb_[r][t]); }

  const int qrow = qt * 256 + wid * 32 + r32;
  const __amdgpu_buffer_rsrc_t srdO = __builtin_amdgcn_make_buffer_rsrc((void*)obuf, 0, SQ * 4096 * 2, 0x00020000);
  const unsigned voffO = (((qrow >> 4) * 512 + head * 16 + hi) * 16 + (qrow & 15)) * 16;
  f32x16 o[4] = {}; f32x16 sA[2], sB[2], negm; u32x4 pw[4];
  float mref, lsum = 0.f; bool bad = false;
  barrier(IC<10>{});
  {
    const f32x16 zero = {};
    static_for<16>(LAMBDA(gc) {
      constexpr int g = gc.value, G = 160 + g;
      ldfrag_g(IC<G + PD>{});
      vmem_gap(IC<10>{}, gc, IC<16>{});
      sA[g >> 3] = mfma32(fr[G % (PD + 1)], qf[g & 7], (g & 7) == 0 ? zero : sA[g >> 3]);
      SBAR();
    });
    float a = max3(sA[0][0], sA[0][1], sA[1][0]), b = max3(sA[0][2], sA[0][3], sA[1][1]); a = max3(a, sA[1][2], sA[1][3]);
#pragma unroll
    for (int r = 4; r < 16; r += 4) { a = max3(a, sA[0][r], sA[0][r + 1]); b = max3(b, sA[0][r + 2], sA[0][r + 3]); a = max3(a, sA[1][r], sA[1][r + 1]); b = max3(b, sA[1][r + 2], sA[1][r + 3]); }
    mref = swap_max(__builtin_fmaxf(a, b)) + PBIAS;
#pragma unroll
    for (int r = 0; r < 16; ++r) negm[r] = -mref;
    PIN(negm);
    sA[0][0] = __builtin_amdgcn_exp2f(sA[0][0] - mref); sA[0][1] = __builtin_amdgcn_exp2f(sA[0][1] - mref);
    SBAR();
  }
  auto step = [&](auto tc, f32x16(&C)[2], f32x16(&P)[2]) __attribute__((always_inline)) {
    constexpr int t = tc.value;
    constexpr int G0 = 176 + 32 * (t - 1), GB = t < 8 ? G0 + 16 : 400;
    barrier(IC<10 + t>{});
    float sacc = 0.f;
    static_for<16>(LAMBDA(gc) {
      constexpr int g = gc.value;
      if constexpr (t < 8) {
        ldfrag_g(IC<G0 + g + PD>{});
        vmem_gap(IC<10 + t>{}, gc, IC<32>{});
        C[g >> 3] = mfma32(fr[(G0 + g) % (PD + 1)], qf[g & 7], (g & 7) == 0 ? negm : C[g >> 3]);
      }
      if constexpr (t == 1 && g < 15) {
        constexpr int e1 = 2 * (g + 1);
        P[e1 >> 4][e1 & 15] = __builtin_amdgcn_exp2f(P[e1 >> 4][e1 & 15] - mref); P[e1 >> 4][(e1 & 15) + 1] = __builtin_amdgcn_exp2f(P[e1 >> 4][(e1 & 15) + 1] - mref);
        PIN(P[e1 >> 4]);
      }
      constexpr int ks = g >> 2, w = g & 3, e = 8 * (ks & 1) + 2 * w;
      pw[ks][w] = cvtpk(P[ks >> 1][e], P[ks >> 1][e + 1]);
      sacc += P[ks >> 1][e]; sacc += P[ks >> 1][e + 1];
      PIN(pw[ks]); PIN(sacc);
      SBAR();
    });
    bad |= !(sacc <= PLIM);
    lsum += sacc;
    if constexpr (t < 8) {
      static_for<16>(LAMBDA(gc) {
        constexpr int g = gc.value;
        ldfrag_g(IC<GB + g + PD>{});
        vmem_gap(IC<10 + t>{}, IC<16 + g>{}, IC<32>{});
        o[g & 3] = mfma32(fr[(GB + g) % (PD + 1)], __builtin_bit_cast(bf16x8, pw[g >> 2]), o[g & 3]);
        C[g >> 3][(2 * g) & 15] = __builtin_amdgcn_exp2f(C[g >> 3][(2 * g) & 15]); C[g >> 3][(2 * g + 1) & 15] = __builtin_amdgcn_exp2f(C[g >> 3][(2 * g + 1) & 15]);
        PIN(C[g >> 3]);
        SBAR();
      });
    }
  };
  auto out_db = [&](auto dc, float inv) __attribute__((always_inline)) {
    constexpr int db = dc.value;
#pragma unroll
    for (int s = 0; s < 2; ++s) {
      u32x4 w;
#pragma unroll
      for (int e = 0; e < 4; ++e) w[e] = cvtpk(o[db][8 * s + 2 * e] * inv, o[db][8 * s + 2 * e + 1] * inv);
      __builtin_amdgcn_raw_buffer_store_b128(w, srdO, voffO + (4 * db + 2 * s) * 256, 0, O_AUX);
    }
  };
  step(IC<1>{}, sB, sA);
  step(IC<2>{}, sA, sB);
  step(IC<3>{}, sB, sA);
  step(IC<4>{}, sA, sB);
  step(IC<5>{}, sB, sA);
  step(IC<6>{}, sA, sB);
  step(IC<7>{}, sB, sA);
  step(IC<8>{}, sA, sB);
  if (__builtin_expect(!__any(bad), 1)) {
    const float inv = 1.f / swap_add(lsum);
    static_for<16>(LAMBDA(gc) {
      constexpr int g = gc.value;
      ldfrag_g(IC<400 + g + PD>{});
      o[g >> 2] = mfma32(fr[(400 + g) % (PD + 1)], __builtin_bit_cast(bf16x8, pw[g & 3]), o[g >> 2]);
      if constexpr (g >= 5 && (g & 3) == 1) out_db(IC<(g >> 2) - 1>{}, inv);
      SBAR();
    });
    out_db(IC<3>{}, inv);
  } else {
    const unsigned reg = (unsigned)((wid == 0 ? 0 : wid + 1) * SLOT);
    static_assert(slot_of(tile_V(7)) == SLOT, "");
    const lds_cptr kbs[2] = {kb_[0][0] + reg, kb_[0][1] + reg}, vbs[2] = {vb[0][0] + reg + 8192, vb[0][1] + reg + 8192};
    float m = -__builtin_inff(), l = 0.f;
#pragma unroll
    for (int d = 0; d < 4; ++d) o[d] = f32x16{};
#pragma unroll 1
    for (int kb = 0; kb < WIN / 32; ++kb) {
#pragma unroll
      for (int R = 0; R < 4; ++R) {
        const unsigned vo = (unsigned)((8 * R + dr) * 256 + 64 * dc + 16 * (dx ^ ((2 * R + (dr >> 2)) & 3)));
        const unsigned so = __builtin_amdgcn_readfirstlane(kb * 8192), ld = __builtin_amdgcn_readfirstlane(lds0 + reg + 2048 * R);
        asm volatile("s_nop 4" ::: "memory");
        dma16(ld, vo, srdK, so); dma16(ld + 1024, vo + 128, srdK, so); dma16(ld + 8192, vo, srdV, so); dma16(ld + 9216, vo + 128, srdV, so);
      }
      asm volatile("s_waitcnt vmcnt(0)" ::: "memory");
      f32x16 sc = {};
#pragma unroll
      for (int s = 0; s < 8; ++s) { sc = mfma32(rowfrag(kbs[s & 1] + 512 * (s >> 1)), qf[s], sc); SBAR(); }
      float mx = sc[0];
#pragma unroll
      for (int r = 1; r < 16; ++r) mx = __builtin_fmaxf(mx, sc[r]);
      const float mn = __builtin_fmaxf(m, swap_max(mx)), al = __builtin_amdgcn_exp2f(m - mn);
      m = mn; l *= al;
#pragma unroll
      for (int r = 0; r < 16; ++r) { sc[r] = __builtin_amdgcn_exp2f(sc[r] - mn); l += sc[r]; }
#pragma unroll
      for (int d = 0; d < 4; ++d)
#pragma unroll
        for (int r = 0; r < 16; ++r) o[d][r] *= al;
#pragma unroll
      for (int ks = 0; ks < 2; ++ks) {
        const u32x4 pk = {cvtpk(sc[8 * ks], sc[8 * ks + 1]), cvtpk(sc[8 * ks + 2], sc[8 * ks + 3]), cvtpk(sc[8 * ks + 4], sc[8 * ks + 5]), cvtpk(sc[8 * ks + 6], sc[8 * ks + 7])};
#pragma unroll
        for (int d = 0; d < 4; ++d) { o[d] = mfma32(trfrag(vbs[0] + 4096 * ks + 512 * d, vbs[1] + 4096 * ks + 512 * d), __builtin_bit_cast(bf16x8, pk), o[d]); SBAR(); }
      }
      asm volatile("s_waitcnt lgkmcnt(0)" ::: "memory");
    }
    const float inv = 1.f / swap_add(l);
    static_for<4>(LAMBDA(dc_) { out_db(dc_, inv); });
  }
}

DI void st16(__amdgpu_buffer_rsrc_t r, unsigned voff, u32x4 v) { __builtin_amdgcn_raw_buffer_store_b128(v, r, voff, 0, SC1); }
DI void st8(__amdgpu_buffer_rsrc_t r, unsigned voff, u32x2 v) { __builtin_amdgcn_raw_buffer_store_b64(v, r, voff, 0, SC1); }
DI u32x4 ld16c(__amdgpu_buffer_rsrc_t r, unsigned voff) { return __builtin_amdgcn_raw_buffer_load_b128(r, voff, 0, SC1); }
DI void bload16c(bf16x8& dst, unsigned voff, __amdgpu_buffer_rsrc_t srd, unsigned soff) {
  asm volatile("buffer_load_dwordx4 %0, %1, %2, %3 offen sc1" : "=v"(dst) : "v"(voff), "s"(srd), "s"(soff) : "memory");
}
template <int N> DI void wait_bar4b(bf16x8 (&b)[2][2]) {
  asm volatile("s_waitcnt vmcnt(%4)\n\ts_barrier" : "+v"(b[0][0]), "+v"(b[0][1]), "+v"(b[1][0]), "+v"(b[1][1]) : "i"(N) : "memory");
}
template <int NQB, int NT, int KS> struct Ring {
  static constexpr int KSP = 8 / NQB, SK = 2 * KSP, NSTG = (KS + SK - 1) / SK, NP = SK * NT, PA = (NP + 7) / 8, SB = PA * 8 * 1024, FR = 2 * NT;
};
template <int NQB, int NT, int KS, int KC_A, int NSLOT> struct RingA {
  using R = Ring<NQB, NT, KS>;
  static constexpr int NS_ = NSLOT;
  unsigned voff[R::PA]; unsigned ldsw;
  DI void init(unsigned lds0, int wid, int lane) {
    ldsw = lds0 + wid * 1024;
#pragma unroll
    for (int i = 0; i < R::PA; ++i) { const int p = i * 8 + wid, ks = p / NT, nt = p - ks * NT; voff[i] = p < R::NP ? (unsigned)(nt * KC_A * 256 + ks * 1024 + lane * 16) : 0x7ff00000u; }
  }
  template <int S, int I> DI void issue(__amdgpu_buffer_rsrc_t ra) const { dma16(ldsw + ((S % NSLOT) * R::SB + I * 8192), voff[I], ra, S * R::SK * 1024); }
  template <int S0, int S1> DI void pre(__amdgpu_buffer_rsrc_t ra) const { static_for<S1 - S0>(LAMBDA(sc) { static_for<R::PA>(LAMBDA(ic) { this->template issue<S0 + sc.value, ic.value>(ra); }); }); }
};
template <int NQB, int NT, int KS, int KC_A, int KC_B, int NPRE, int NSLOT, int NHOOK, class HOOK>
DI void ring_gemm(f32x4 (&acc)[NT][2], const RingA<NQB, NT, KS, KC_A, NSLOT>& RA, __amdgpu_buffer_rsrc_t ra, __amdgpu_buffer_rsrc_t rb, const char* lds, int wid, int lane, HOOK&& hook) {
  using R = Ring<NQB, NT, KS>;
  constexpr int SK = R::SK, NSTG = R::NSTG, PA = R::PA, FR = R::FR, PD = 5;
  static_assert(NSTG >= 3 && FR > PD + 1 && (NPRE == 0 || NPRE >= 2), "");
  const int kq = wid / NQB;
  bf16x8 B[3][2][2];
  const unsigned voffB = lane * 16 + 2 * kq * 1024;
  auto issueA1 = [&](auto sc, auto ic) __attribute__((always_inline)) { RA.template issue<sc.value, ic.value>(ra); };
  auto issueB1 = [&](auto sc, auto ic) __attribute__((always_inline)) {
    constexpr int s = sc.value, i = ic.value, ks = i >> 1, q = i & 1;
    bload16c(B[s % 3][ks][q], voffB, rb, q * KC_B * 256 + (s * SK + ks) * 1024);
  };
  if constexpr (NPRE == 0) static_for<PA>(LAMBDA(ic) { issueA1(IC<0>{}, ic); });
  static_for<4>(LAMBDA(ic) { issueB1(IC<0>{}, ic); });
  if constexpr (NPRE > 0) { static_for<4>(LAMBDA(ic) { issueB1(IC<1>{}, ic); }); wait_bar4b<4>(B[0]); } else wait_bar4b<0>(B[0]);
  const LAS bf16x8* pl = (const LAS bf16x8*)lds + lane + 2 * kq * NT * 64;
  bf16x8 A[PD + 1];
  auto rd = [&](auto mc) __attribute__((always_inline)) { constexpr int M = mc.value, s = M / FR, m = M % FR; A[M % (PD + 1)] = pl[((s % NSLOT) * PA * 8 + m) * 64]; };
  static_for<PD>(LAMBDA(jc) { rd(jc); });
  SBAR();
  static_for<NSTG>(LAMBDA(sc) {
    constexpr int s = sc.value;
    constexpr int n0 = (NPRE == 0 && s == 0) ? PA + 4 : 0;
    constexpr int sa = s + 2;
    constexpr int nA = (sa < NSTG && sa >= NPRE) ? PA : 0, nB = (s + 2 < NSTG) ? 4 : 0, NI = n0 + nA + nB, NGI = FR - PD - 1;
    constexpr bool full = (s + 1) * SK <= KS;
    static_for<FR>(LAMBDA(mc) {
      constexpr int m = mc.value, M = s * FR + m, ks = m / NT, nt = m % NT;
      if constexpr (m == FR - PD && s + 1 < NSTG) wait_bar4b<nA + nB>(B[(s + 1) % 3]);
      if constexpr (M + PD < NSTG * FR) rd(IC<M + PD>{});
      if constexpr (m < NGI) {
        constexpr int i0 = (m * NI + NGI - 1) / NGI, i1 = ((m + 1) * NI + NGI - 1) / NGI;
        static_for<i1 - i0>(LAMBDA(uc) {
          constexpr int i = i0 + uc.value;
          if constexpr (i < n0) { if constexpr (i < PA) issueA1(IC<1>{}, IC<i>{}); else issueB1(IC<1>{}, IC<i - PA>{}); }
          else if constexpr (i - n0 < nA) issueA1(IC<sa>{}, IC<i - n0>{});
          else issueB1(IC<s + 2>{}, IC<i - n0 - nA>{});
        });
      }
      if constexpr (NHOOK > 0 && M >= (NSTG - 1) * FR - PD) {
        constexpr int j = M - ((NSTG - 1) * FR - PD), HG = FR + PD - 2, i0 = (j * NHOOK + HG - 1) / HG, i1 = ((j + 1) * NHOOK + HG - 1) / HG;
        if constexpr (j < HG) static_for<i1 - i0>(LAMBDA(uc) { hook(IC<i0 + uc.value>{}); });
      }
      if (full || s * SK + 2 * kq + ks < KS) { acc[nt][0] = mfma16(A[M % (PD + 1)], B[s % 3][ks][0], acc[nt][0]); acc[nt][1] = mfma16(A[M % (PD + 1)], B[s % 3][ks][1], acc[nt][1]); }
      SBAR();
    });
  });
}
template <int NT> DI void pair_reduce(f32x4 (&acc)[NT][2], char* lds, int wid, int lane) {
  constexpr int H0 = (NT + 1) / 2;
  f32x4* red = (f32x4*)lds + (size_t)wid * H0 * 2 * 64 + lane;
  const f32x4* in = (const f32x4*)lds + (size_t)(wid ^ 4) * H0 * 2 * 64 + lane;
  lds_barrier();
  if (wid < 4) {
    asm volatile("; lo-send" ::: "memory");
#pragma unroll
    for (int nt = H0; nt < NT; ++nt) { red[((nt - H0) * 2) * 64] = acc[nt][0]; red[((nt - H0) * 2 + 1) * 64] = acc[nt][1]; }
    asm volatile("; lo-sent" ::: "memory");
  } else {
    asm volatile("; hi-send" ::: "memory");
#pragma unroll
    for (int nt = 0; nt < H0; ++nt) { red[(nt * 2) * 64] = acc[nt][0]; red[(nt * 2 + 1) * 64] = acc[nt][1]; }
    asm volatile("; hi-sent" ::: "memory");
  }
  lds_barrier();
  if (wid < 4) {
    asm volatile("; lo-recv" ::: "memory");
#pragma unroll
    for (int nt = 0; nt < H0; ++nt) { acc[nt][0] += in[(nt * 2) * 64]; acc[nt][1] += in[(nt * 2 + 1) * 64]; PIN(acc[nt][0]); PIN(acc[nt][1]); }
    asm volatile("; lo-done" ::: "memory");
  } else {
    asm volatile("; hi-recv" ::: "memory");
#pragma unroll
    for (int nt = H0; nt < NT; ++nt) { acc[nt][0] += in[((nt - H0) * 2) * 64]; acc[nt][1] += in[((nt - H0) * 2 + 1) * 64]; PIN(acc[nt][0]); PIN(acc[nt][1]); }
    asm volatile("; hi-done" ::: "memory");
  }
}

DI void group_arrive(unsigned* cnt) {
  asm volatile("s_waitcnt vmcnt(0)" ::: "memory");
  __syncthreads();
  if (threadIdx.x == 0) __hip_atomic_fetch_add((gu32*)cnt, 1u, __ATOMIC_RELAXED, __HIP_MEMORY_SCOPE_AGENT);
}
DI void group_wait(unsigned* cnt, unsigned want) {
  if (threadIdx.x == 0) {
    gu32* p = (gu32*)cnt;
    unsigned a = __hip_atomic_load(p, RLX_AGENT); __builtin_amdgcn_s_sleep(POLL_SLEEP);
    unsigned b = __hip_atomic_load(p, RLX_AGENT); __builtin_amdgcn_s_sleep(POLL_SLEEP);
    unsigned c = __hip_atomic_load(p, RLX_AGENT); __builtin_amdgcn_s_sleep(POLL_SLEEP);
    unsigned d = __hip_atomic_load(p, RLX_AGENT);
    for (unsigned spins = 0; spins < SPIN_CAP; ++spins) {
      if (a >= want) break;
      a = __hip_atomic_load(p, RLX_AGENT); __builtin_amdgcn_s_sleep(POLL_SLEEP);
      if (b >= want) break;
      b = __hip_atomic_load(p, RLX_AGENT); __builtin_amdgcn_s_sleep(POLL_SLEEP);
      if (c >= want) break;
      c = __hip_atomic_load(p, RLX_AGENT); __builtin_amdgcn_s_sleep(POLL_SLEEP);
      if (d >= want) break;
      d = __hip_atomic_load(p, RLX_AGENT); __builtin_amdgcn_s_sleep(POLL_SLEEP);
    }
  }
  asm volatile("" ::: "memory");
  __syncthreads();
}

__global__ __launch_bounds__(512, 2) void k_tail(const u16* __restrict__ obuf, const u16* __restrict__ wot, const u16* __restrict__ w1t, const u16* __restrict__ w2t,
                                                  const u16* __restrict__ x, const u16* __restrict__ mod, const u16* __restrict__ b1, const u16* __restrict__ b2,
                                                  u16* part, u16* x1, u16* h2, u16* hid, u16* __restrict__ out, unsigned* cnt) {
  extern __shared__ __attribute__((aligned(16))) char lds[];
  const int tid = threadIdx.x, lane = tid & 63, wid = __builtin_amdgcn_readfirstlane(tid >> 6);
  const unsigned lds0 = (unsigned)(uintptr_t)lds;
  const int xcd = blockIdx.x & 7, idx = blockIdx.x >> 3;
  const int rt = (xcd >> 2) * 8 + (idx >> 2), gi = (xcd & 3) * 4 + (idx & 3);
  unsigned* const c1 = cnt + rt * 64, * const c2 = cnt + (16 + rt) * 64, * const c3 = cnt + (32 + rt) * 64;
  const int nft = gi < 15 ? 7 : 3;
  const __amdgpu_buffer_rsrc_t raD = __builtin_amdgcn_make_buffer_rsrc((void*)(w1t + (size_t)(7 * gi) * 80 * 128), 0, nft * 80 * 256, 0x00020000);
  RingA<4, 7, 20, 80, 5> RD; RD.init(lds0, wid, lane);
  {
    const int kq = gi >> 2, j = gi & 3, qb = wid & 3;
    const __amdgpu_buffer_rsrc_t ra = __builtin_amdgcn_make_buffer_rsrc((void*)(wot + ((size_t)(10 * j) * 512 + kq * 128) * 128), 0, 10 * 512 * 256, 0x00020000);
    const __amdgpu_buffer_rsrc_t rb = __builtin_amdgcn_make_buffer_rsrc((void*)(obuf + ((size_t)(8 * rt + 2 * qb) * 512 + kq * 128) * 128), 0, 2 * 512 * 256, 0x00020000);
    const __amdgpu_buffer_rsrc_t rp = __builtin_amdgcn_make_buffer_rsrc((void*)part, 0, 4 * SQ * DM * 2, 0x00020000);
    f32x4 acc[10][2] = {};
    RingA<4, 10, 32, 512, 4> RA; RA.init(lds0, wid, lane);
    ring_gemm<4, 10, 32, 512, 512, 0, 4, 8>(acc, RA, ra, rb, lds, wid, lane, [&](auto ic) __attribute__((always_inline)) { RD.template issue<ic.value / 4, ic.value % 4>(raD); });
    pair_reduce<10>(acc, lds + 81920, wid, lane);
    const int nt0 = wid < 4 ? 0 : 5;
    const unsigned vo = ((((8 * rt + 2 * qb + ((lane >> 4) & 1)) * 80 + j * 20 + nt0 * 2 + (lane >> 5)) * 4 + kq) * 16 + (lane & 15)) * 16;
    auto outp = [&](auto ntc) __attribute__((always_inline)) {
      constexpr int nt = ntc.value, o = nt % 5;
      const u32x2 w0 = {cvtpk(acc[nt][0][0], acc[nt][0][1]), cvtpk(acc[nt][0][2], acc[nt][0][3])}, w1 = {cvtpk(acc[nt][1][0], acc[nt][1][1]), cvtpk(acc[nt][1][2], acc[nt][1][3])};
      st16(rp, vo + o * 2 * 1024, pair16(w0, w1));
    };
    if (wid < 4) { asm volatile("; lo-out" ::: "memory"); static_for<5>(LAMBDA(c) { outp(c); }); asm volatile("; lo-end" ::: "memory"); }
    else { asm volatile("; hi-out" ::: "memory"); static_for<5>(LAMBDA(c) { outp(IC<5 + c.value>{}); }); asm volatile("; hi-end" ::: "memory"); }
  }
  group_arrive(c1);
  {
    float* red = (float*)(lds + 98304);
    const int r = tid & 7, row = rt * 128 + gi * 8 + r;
    const bool two = wid < 2;
    const __amdgpu_buffer_rsrc_t rp = __builtin_amdgcn_make_buffer_rsrc((void*)part, 0, 4 * SQ * DM * 2, 0x00020000);
    const __amdgpu_buffer_rsrc_t rx1 = __builtin_amdgcn_make_buffer_rsrc((void*)x1, 0, SQ * DM * 2, 0x00020000);
    const __amdgpu_buffer_rsrc_t rh2 = __builtin_amdgcn_make_buffer_rsrc((void*)h2, 0, SQ * DM * 2, 0x00020000);
    u32x4 p[2][4], xv[2], gv[2], sbv[2], cbv[2]; unsigned blk[2], pblk[2];
#pragma unroll
    for (int u = 0; u < 2; ++u) {
      const int c = (u == 0 || two) ? (tid >> 3) + 64 * u : (tid >> 3);
      blk[u] = (((row >> 4) * 80 + c) * 16 + (row & 15)) * 16; pblk[u] = (((row >> 4) * 80 + c) * 64 + (row & 15)) * 16;
      xv[u] = *(const u32x4*)(x + (size_t)row * DM + c * 8);
      gv[u] = *(const u32x4*)(mod + 2 * 640 + c * 8); sbv[u] = *(const u32x4*)(mod + 3 * 640 + c * 8); cbv[u] = *(const u32x4*)(mod + 4 * 640 + c * 8);
    }
    RD.pre<2, 3>(raD);
    group_wait(c1, 16);
#pragma unroll
    for (int k = 0; k < 4; ++k) p[0][k] = ld16c(rp, pblk[0] + k * 256);
    if (two) {
#pragma unroll
      for (int k = 0; k < 4; ++k) p[1][k] = ld16c(rp, pblk[1] + k * 256);
    }
    float v[2][8]; float s = 0; u32x4 x1v[2];
    auto resid = [&](auto uc) __attribute__((always_inline)) {
      constexpr int u = uc.value;
#pragma unroll
      for (int e = 0; e < 4; ++e) {
        const float a = (bf_lo(p[u][0][e]) + bf_lo(p[u][1][e])) + (bf_lo(p[u][2][e]) + bf_lo(p[u][3][e])), b = (bf_hi(p[u][0][e]) + bf_hi(p[u][1][e])) + (bf_hi(p[u][2][e]) + bf_hi(p[u][3][e]));
        x1v[u][e] = cvtpk(__builtin_fmaf(bf_lo(gv[u][e]), a, bf_lo(xv[u][e])), __builtin_fmaf(bf_hi(gv[u][e]), b, bf_hi(xv[u][e])));
        v[u][2 * e] = bf_lo(x1v[u][e]); v[u][2 * e + 1] = bf_hi(x1v[u][e]); s += v[u][2 * e] + v[u][2 * e + 1];
      }
    };
    resid(IC<0>{});
    if (two) resid(IC<1>{});
    s = xor_add<32>(xor_add<16>(xor_add<8>(s)));
    if (lane < 8) red[wid * 8 + r] = s;
    lds_barrier();
    float tot = 0;
#pragma unroll
    for (int i = 0; i < 8; ++i) tot += red[i * 8 + r];
    const float mean = tot * (1.f / DM); float q = 0;
#pragma unroll
    for (int e = 0; e < 8; ++e) { v[0][e] -= mean; q += v[0][e] * v[0][e]; }
    if (two) {
#pragma unroll
      for (int e = 0; e < 8; ++e) { v[1][e] -= mean; q += v[1][e] * v[1][e]; }
    }
    q = xor_add<32>(xor_add<16>(xor_add<8>(q)));
    if (lane < 8) red[64 + wid * 8 + r] = q;
    lds_barrier();
    float qt = 0;
#pragma unroll
    for (int i = 0; i < 8; ++i) qt += red[64 + i * 8 + r];
    const float rstd = __builtin_amdgcn_rsqf(qt * (1.f / DM) + 1e-5f);
    auto norm = [&](auto uc) __attribute__((always_inline)) {
      constexpr int u = uc.value;
      u32x4 hv;
#pragma unroll
      for (int e = 0; e < 4; ++e)
        hv[e] = cvtpk(__builtin_fmaf(v[u][2 * e] * rstd, bf_lo(cbv[u][e]), bf_lo(sbv[u][e])), __builtin_fmaf(v[u][2 * e + 1] * rstd, bf_hi(cbv[u][e]), bf_hi(sbv[u][e])));
      st16(rh2, blk[u], hv); st16(rx1, blk[u], x1v[u]);
    };
    norm(IC<0>{});
    if (two) norm(IC<1>{});
  }
  group_arrive(c2);
  const int jE = gi & 7, rtE = rt * 2 + (gi >> 3);
  const __amdgpu_buffer_rsrc_t raE = __builtin_amdgcn_make_buffer_rsrc((void*)(w2t + (size_t)(5 * jE) * 216 * 128), 0, 5 * 216 * 256, 0x00020000);
  RingA<2, 5, 54, 216, 4> RE; RE.init(lds0, wid, lane);
  {
    const int qb = wid & 3, nt0 = wid < 4 ? 0 : 4;
    const __amdgpu_buffer_rsrc_t rb = __builtin_amdgcn_make_buffer_rsrc((void*)(h2 + (size_t)(8 * rt + 2 * qb) * 80 * 128), 0, 2 * 80 * 256, 0x00020000);
    const __amdgpu_buffer_rsrc_t rh = __builtin_amdgcn_make_buffer_rsrc((void*)hid, 0, SQ * FF * 2, 0x00020000);
    u32x2 bv[4];
#pragma unroll
    for (int o = 0; o < 4; ++o) bv[o] = *(const u32x2*)(b1 + (nt0 + o < nft ? (7 * gi + nt0 + o) * 16 + 4 * (lane >> 4) : 0));
    f32x4 acc[7][2] = {};
    group_wait(c2, 16);
    ring_gemm<4, 7, 20, 80, 80, 3, 5, 10>(acc, RD, raD, rb, lds, wid, lane, [&](auto ic) __attribute__((always_inline)) { RE.template issue<ic.value / 5, ic.value % 5>(raE); });
    pair_reduce<7>(acc, lds + 98304, wid, lane);
    const unsigned vo = (((8 * rt + 2 * qb + ((lane >> 4) & 1)) * 216 + (7 * gi + nt0) * 2 + (lane >> 5)) * 16 + (lane & 15)) * 16;
    auto outp = [&](auto ntc, auto oc) __attribute__((always_inline)) {
      constexpr int nt = ntc.value, o = oc.value;
      if (nt < nft) {
        const float b0 = bf_lo(bv[o][0]), b1_ = bf_hi(bv[o][0]), b2_ = bf_lo(bv[o][1]), b3 = bf_hi(bv[o][1]);
        u32x2 w[2];
#pragma unroll
        for (int q = 0; q < 2; ++q) {
          const float v0 = rbf(__builtin_fmaxf(rbf(acc[nt][q][0]) + b0, 0.f)), v1 = rbf(__builtin_fmaxf(rbf(acc[nt][q][1]) + b1_, 0.f));
          const float v2 = rbf(__builtin_fmaxf(rbf(acc[nt][q][2]) + b2_, 0.f)), v3 = rbf(__builtin_fmaxf(rbf(acc[nt][q][3]) + b3, 0.f));
          w[q] = u32x2{cvtpk(v0 * v0, v1 * v1), cvtpk(v2 * v2, v3 * v3)};
        }
        st16(rh, vo + o * 2 * 256, pair16(w[0], w[1]));
      }
    };
    if (wid < 4) { asm volatile("; lo-out" ::: "memory"); static_for<4>(LAMBDA(c) { outp(c, c); }); asm volatile("; lo-end" ::: "memory"); }
    else { asm volatile("; hi-out" ::: "memory"); static_for<3>(LAMBDA(c) { outp(IC<4 + c.value>{}, c); }); asm volatile("; hi-end" ::: "memory"); }
  }
  group_arrive(c3);
  {
    const int qb = wid & 1;
    const __amdgpu_buffer_rsrc_t rb = __builtin_amdgcn_make_buffer_rsrc((void*)(hid + (size_t)(4 * rtE + 2 * qb) * 216 * 128), 0, 2 * 216 * 256, 0x00020000);
    const __amdgpu_buffer_rsrc_t rx1 = __builtin_amdgcn_make_buffer_rsrc((void*)x1, 0, SQ * DM * 2, 0x00020000);
    u32x4 xv[2], gv[2], bv[2];
#pragma unroll
    for (int it = 0; it < 2; ++it) {
      const int item = tid + it * 512, q = item & 63, c = item < 640 ? item >> 6 : 0, row = rtE * 64 + q;
      xv[it] = ld16c(rx1, (((row >> 4) * 80 + jE * 10 + c) * 16 + (row & 15)) * 16);
      gv[it] = *(const u32x4*)(mod + 5 * 640 + jE * 80 + c * 8); bv[it] = *(const u32x4*)(b2 + jE * 80 + c * 8);
    }
    f32x4 acc[5][2] = {};
    RE.pre<2, 3>(raE);
    group_wait(c3, 16);
    ring_gemm<2, 5, 54, 216, 216, 3, 4, 0>(acc, RE, raE, rb, lds, wid, lane, [&](auto) {});
    f32x4* red = (f32x4*)lds;
    constexpr int HI = (3 * 40960 - 4 * 10240) / 16;
    {
      f32x4* mine = red + (wid * 10) * 64 + (wid >= 4 ? HI : 0) + lane;
#pragma unroll
      for (int n = 0; n < 5; ++n) { mine[(n * 2) * 64] = acc[n][0]; mine[(n * 2 + 1) * 64] = acc[n][1]; }
    }
    lds_barrier();
#pragma unroll
    for (int it = 0; it < 2; ++it) {
      const int item = tid + it * 512;
      if (item < 640) {
        const int q = item & 63, c = item >> 6, n = c >> 1, l0 = (q & 15) + 32 * (c & 1), base = (((q >> 5) * 5 + n) * 2 + ((q >> 4) & 1)) * 64 + l0;
        f32x4 lo = {0, 0, 0, 0}, hi = {0, 0, 0, 0};
#pragma unroll
        for (int w = 0; w < 4; ++w) { const int o_ = base + w * 2 * 5 * 2 * 64 + (w >= 2 ? HI : 0); lo += red[o_]; hi += red[o_ + 16]; }
        const float v[8] = {lo[0], lo[1], lo[2], lo[3], hi[0], hi[1], hi[2], hi[3]};
        u32x4 o;
#pragma unroll
        for (int e = 0; e < 4; ++e) {
          const float a = bf_lo(xv[it][e]) + rbf(bf_lo(gv[it][e]) * rbf(rbf(v[2 * e]) + bf_lo(bv[it][e]))), b = bf_hi(xv[it][e]) + rbf(bf_hi(gv[it][e]) * rbf(rbf(v[2 * e + 1]) + bf_hi(bv[it][e])));
          o[e] = cvtpk(a, b);
        }
        *(u32x4*)(out + (size_t)(rtE * 64 + q) * DM + jE * 80 + c * 8) = o;
      }
    }
  }
}

extern "C" void kernel_launch(void* const* d_in, const int* in_sizes, int n_in, void* d_out, int out_size, void* d_ws, size_t ws_size, hipStream_t stream) {
  const u16 *x = (const u16*)d_in[0], *kc = (const u16*)d_in[1], *vc = (const u16*)d_in[2], *wq = (const u16*)d_in[3], *wo = (const u16*)d_in[4];
  const u16 *w1 = (const u16*)d_in[6], *b1 = (const u16*)d_in[7], *w2 = (const u16*)d_in[8], *b2 = (const u16*)d_in[9], *c = (const u16*)d_in[10], *wmod = (const u16*)d_in[11];
  char* ws = (char*)d_ws;
  u16 *mod = (u16*)(ws + WS_MOD), *h = (u16*)(ws + WS_H), *o = (u16*)(ws + WS_O), *wot = (u16*)(ws + WS_WOT), *w1t = (u16*)(ws + WS_W1T), *w2t = (u16*)(ws + WS_W2T);
  u16 *x1 = (u16*)(ws + WS_X1), *hid = (u16*)(ws + WS_HID);
  u16 *h2 = (u16*)(ws + WS_H2), *part = (u16*)(ws + WS_P);
  (void)hipFuncSetAttribute((const void*)k_attn, hipFuncAttributeMaxDynamicSharedMemorySize, (NS + 2) * SLOT);
  unsigned* cnt = (unsigned*)(ws + WS_CNT);
  (void)hipFuncSetAttribute((const void*)k_tail, hipFuncAttributeMaxDynamicSharedMemorySize, 163840);
  k_front<<<FR_N, 512, 0, stream>>>(c, wmod, x, mod, h, (unsigned*)(ws + WS_SYNC), cnt);
  k_attn<<<256, 512, (NS + 2) * SLOT, stream>>>(h, wq, kc, vc, o, wo, w1, w2, wot);
  k_tail<<<256, 512, 163840, stream>>>(o, wot, w1t, w2t, x, mod, b1, b2, part, x1, h2, hid, (u16*)d_out, cnt);
}
```

```cpp
#include <hip/hip_runtime.h>
#include <stdint.h>
#include <utility>

typedef unsigned short u16;
typedef short bf16x8 __attribute__((ext_vector_type(8)));
typedef short s16x4 __attribute__((ext_vector_type(4)));
typedef float f32x16 __attribute__((ext_vector_type(16)));
typedef float f32x4 __attribute__((ext_vector_type(4)));
typedef float f32x2 __attribute__((ext_vector_type(2)));
typedef __bf16 bf16x2 __attribute__((ext_vector_type(2)));
typedef unsigned u32x4 __attribute__((ext_vector_type(4)));
typedef unsigned u32x2 __attribute__((ext_vector_type(2)));
#define DI __device__ __forceinline__
#define GAS __attribute__((address_space(1)))
#define LAS __attribute__((address_space(3)))
#define SBAR() __builtin_amdgcn_sched_barrier(0)
#define PIN(x) asm volatile("" : "+v"(x))

constexpr int SQ = 2048, DM = 640, NH = 32, DH = 128, SK = 2048, WIN = 512, FF = 1728;
constexpr size_t WS_MOD = 0;
constexpr size_t WS_SYNC = 32768;
constexpr size_t WS_H = 131072;
constexpr size_t WS_O = WS_H + (size_t)SQ * DM * 2;
constexpr size_t WS_WOT = WS_O + (size_t)SQ * 4096 * 2;
constexpr size_t WS_W1T = WS_WOT + (size_t)4096 * DM * 2;
constexpr size_t WS_W2T = WS_W1T + (size_t)DM * FF * 2;
constexpr size_t WS_X1 = WS_W2T + (size_t)DM * FF * 2 + 65536;
constexpr size_t WS_HID = WS_X1 + (size_t)SQ * DM * 2;
constexpr size_t WS_H2 = WS_HID + (size_t)SQ * FF * 2 + 65536;
constexpr size_t WS_P = WS_H2 + (size_t)SQ * DM * 2;
constexpr size_t WS_END = WS_P + (size_t)4 * SQ * DM * 2;

template <int... I, class F> DI void static_for_impl(std::integer_sequence<int, I...>, F&& f) { (f(std::integral_constant<int, I>{}), ...); }
template <int N, class F> DI void static_for(F&& f) { static_for_impl(std::make_integer_sequence<int, N>{}, f); }
#define LAMBDA(ic) [&](auto ic) __attribute__((always_inline))

DI float bf_lo(unsigned w) { return __uint_as_float(w << 16); }
DI float bf_hi(unsigned w) { return __uint_as_float(w & 0xffff0000u); }
DI float bf2f(u16 v) { return __uint_as_float((unsigned)v << 16); }
DI unsigned cvtpk(float lo, float hi) { f32x2 v = {lo, hi}; bf16x2 b = __builtin_convertvector(v, bf16x2); return __builtin_bit_cast(unsigned, b); }
DI u16 f2bf(float x) { return (u16)(cvtpk(x, 0.f) & 0xffffu); }
DI float rbf(float x) { return bf_lo(cvtpk(x, 0.f)); }
DI f32x16 mfma32(bf16x8 a, bf16x8 b, f32x16 c) { return __builtin_amdgcn_mfma_f32_32x32x16_bf16(a, b, c, 0, 0, 0); }
DI f32x4 mfma16(bf16x8 a, bf16x8 b, f32x4 c) { return __builtin_amdgcn_mfma_f32_16x16x32_bf16(a, b, c, 0, 0, 0); }
DI float swap_max(float m) { auto r = __builtin_amdgcn_permlane32_swap(__float_as_uint(m), __float_as_uint(m), false, false); return __builtin_fmaxf(__uint_as_float(r[0]), __uint_as_float(r[1])); }
DI float swap_add(float m) { auto r = __builtin_amdgcn_permlane32_swap(__float_as_uint(m), __float_as_uint(m), false, false); return __uint_as_float(r[0]) + __uint_as_float(r[1]); }

DI void lds_barrier() { asm volatile("s_waitcnt lgkmcnt(0)\n\ts_barrier" ::: "memory"); }
template <int CTRL> DI float dpp_mov(float v) { return __builtin_bit_cast(float, __builtin_amdgcn_update_dpp(0, __builtin_bit_cast(int, v), CTRL, 0xf, 0xf, true)); }
template <int M> DI float xor_add(float v) {
  if constexpr (M == 1) return v + dpp_mov<0xB1>(v);
  else if constexpr (M == 2) return v + dpp_mov<0x4E>(v);
  else if constexpr (M == 4) return v + dpp_mov<0x141>(dpp_mov<0x1B>(v));
  else if constexpr (M == 8) return v + dpp_mov<0x128>(v);
  else if constexpr (M == 16) { const auto r = __builtin_amdgcn_permlane16_swap(__float_as_uint(v), __float_as_uint(v), false, false); return __uint_as_float(r[0]) + __uint_as_float(r[1]); }
  else { const auto r = __builtin_amdgcn_permlane32_swap(__float_as_uint(v), __float_as_uint(v), false, false); return __uint_as_float(r[0]) + __uint_as_float(r[1]); }
}
typedef GAS unsigned gu32;
typedef GAS unsigned long long gu64;
#define RLX_AGENT __ATOMIC_RELAXED, __HIP_MEMORY_SCOPE_AGENT
constexpr unsigned SPIN_CAP = 1u << 18;
constexpr int SC1 = 16;
constexpr int SY_STARTED = 64, SY_GRAN = 2048  , SY_HF = 20480, SY_RF = 20992, SY_OF = 21504, SY_CF = 22016  ;
DI void st16(__amdgpu_buffer_rsrc_t r, unsigned voff, u32x4 v) { __builtin_amdgcn_raw_buffer_store_b128(v, r, voff, 0, SC1); }
DI u32x4 ld16c(__amdgpu_buffer_rsrc_t r, unsigned voff) { return __builtin_amdgcn_raw_buffer_load_b128(r, voff, 0, SC1); }
DI void publish(unsigned* flag, unsigned tag) {
  asm volatile("s_waitcnt vmcnt(0)" ::: "memory");
  __syncthreads();
  if (threadIdx.x == 0) __hip_atomic_store((gu32*)flag, tag, RLX_AGENT);
}
template <int N> DI void await_flags(const unsigned* flags, unsigned tag) {
  if (threadIdx.x < 64) {
    gu32* p = (gu32*)flags + (threadIdx.x < N ? threadIdx.x : 0);
    unsigned a = __hip_atomic_load(p, RLX_AGENT); __builtin_amdgcn_s_sleep(2);
    unsigned b = __hip_atomic_load(p, RLX_AGENT);
    for (unsigned spins = 0; spins < SPIN_CAP; ++spins) {
      if (__all(a == tag)) break;
      a = __hip_atomic_load(p, RLX_AGENT); __builtin_amdgcn_s_sleep(2);
      if (__all(b == tag)) break;
      b = __hip_atomic_load(p, RLX_AGENT); __builtin_amdgcn_s_sleep(2);
    }
  }
  asm volatile("" ::: "memory");
  __syncthreads();
}
template <class T> DI T* karg(int i) {
  const __attribute__((address_space(4))) char* p = (const __attribute__((address_space(4))) char*)__builtin_amdgcn_kernarg_segment_ptr();
  asm volatile("" : "+s"(p));
  return *(T* const __attribute__((address_space(4)))*)(p + 8 * i);
}
enum { KA_X = 0, KA_KC, KA_VC, KA_WQ, KA_WO, KA_W1, KA_B1, KA_W2, KA_B2, KA_C, KA_WMOD, KA_OUT, KA_WS };

DI u32x4 pair16(u32x2 x, u32x2 y) {
  const auto a = __builtin_amdgcn_permlane16_swap(x[0], y[0], false, false), b = __builtin_amdgcn_permlane16_swap(x[1], y[1], false, false);
  return u32x4{a[0], b[0], a[1], b[1]};
}
#define A_NS 8
#define A_PD 5
#define O_AUX 16
constexpr int NS = A_NS, SLOT = 16384;
constexpr float QSCALE = 0.088388347648318440f * 1.4426950408889634f;
constexpr float PBIAS = 96.f;
#define PLIM_V 1073741824.f
constexpr float PLIM = PLIM_V;
constexpr int tile_K(int t) { return t == 0 ? 10 : 9 + 2 * t; }
constexpr int tile_V(int t) { return t == 7 ? 25 : 12 + 2 * t; }
constexpr int slot_of(int idx) { return (idx % NS) * SLOT; }
constexpr int lastuse(int idx) { return idx < 10 ? idx : (idx == 10 ? 10 : (idx == 25 ? 18 : ((idx & 1) ? 10 + (idx - 9) / 2 : 11 + (idx - 12) / 2))); }
constexpr int need_bar(int idx) {
  if (idx < 10) return idx == 0 ? 0 : idx - 1;
  if (idx == 10) return 9;
  if (idx == 25) return 17;
  return (idx & 1) ? 10 + (idx - 9) / 2 - 1 : 11 + (idx - 12) / 2;
}
constexpr int rt_step(int u) { return u == 0 ? 9 : (u == 1 ? 10 : 12); }
constexpr int rt_proc(int u) { return u == 0 ? 11 : (u == 1 ? 12 : 14); }
constexpr int rt_slot(int u) { return (u == 1 ? NS + 1 : NS) * SLOT; }
struct Sched { int npro; int pro[16]; int nops[19]; int op[19][16]; int wait[19]; int rt_done; };
constexpr Sched make_sched() {
  Sched p{}; int pos = 0; int tile_end[26] = {}; int h_end[10] = {}; int u_end[3] = {}; int next_tile = 3;
  auto pro_h = [&](int i) { for (int k = 0; k < 4; ++k) p.pro[p.npro++] = 100 + 4 * i + k; pos += 4; h_end[i] = pos; };
  pro_h(0); pro_h(1);
  tile_end[0] = tile_end[1] = tile_end[2] = 0; int last_rt_store = 1 << 20; p.rt_done = -1;
  for (int g = 0; g < 19; ++g) {
    int need = 0;
    for (int idx = 0; idx < next_tile; ++idx) if (need_bar(idx) <= g && tile_end[idx] > need) need = tile_end[idx];
    if (g < 10 && h_end[g] > need) need = h_end[g];
    for (int u = 0; u < 3; ++u) if (rt_proc(u) <= g && u_end[u] > need) need = u_end[u];
    p.wait[g] = pos - need;
    if (p.rt_done < 0 && need >= last_rt_store) p.rt_done = g;
    int n = 0;
    for (int u = 0; u < 3; ++u) if (g == rt_proc(u)) { p.op[g][n++] = 300 + 2 * u; p.op[g][n++] = 301 + 2 * u; }
    if (g == 0) { for (int k = 0; k < 4; ++k) p.op[g][n++] = 100 + 4 * 2 + k; pos += 4; h_end[2] = pos; }
    const int maxt = g < 7 ? 1 : 2;
    for (int c = 0; c < maxt && next_tile < 26; ++c) {
      if (next_tile >= NS && lastuse(next_tile - NS) >= g) break;
      p.op[g][n++] = 2 * next_tile; p.op[g][n++] = 2 * next_tile + 1; pos += 2; tile_end[next_tile] = pos; ++next_tile;
    }
    if (g + 3 < 10) { for (int k = 0; k < 4; ++k) p.op[g][n++] = 100 + 4 * (g + 3) + k; pos += 4; h_end[g + 3] = pos; }
    for (int u = 0; u < 3; ++u) if (g == rt_step(u)) { p.op[g][n++] = 200 + 2 * u; p.op[g][n++] = 201 + 2 * u; pos += 2; u_end[u] = pos; }
    for (int u = 0; u < 3; ++u) if (g == rt_proc(u)) { p.op[g][n++] = 310 + 2 * u; p.op[g][n++] = 311 + 2 * u; pos += 2; if (u == 2) last_rt_store = pos; }
    p.nops[g] = n;
  }
  for (int idx = 3; idx < 26; ++idx) if (tile_end[idx] == 0) p.wait[0] = -1;
  if (p.rt_done < 0) p.wait[0] = -1;
  return p;
}
constexpr Sched SCH = make_sched();
static_assert(SCH.wait[0] >= 0, "schedule incomplete");
constexpr bool sched_ok() {
  int step_of[26] = {}; for (int i = 0; i < 26; ++i) step_of[i] = -1;
  for (int g = 0; g < 19; ++g) for (int j = 0; j < SCH.nops[g]; ++j) if (SCH.op[g][j] < 100) step_of[SCH.op[g][j] >> 1] = g;
  for (int i = 3; i < 26; ++i) if (step_of[i] >= need_bar(i)) return false;
  return true;
}
static_assert(sched_ok(), "a tile is issued too late");

DI void dma16(unsigned ldsaddr, unsigned voff, __amdgpu_buffer_rsrc_t srd, unsigned soff) {
  asm volatile("s_mov_b32 m0, %0\n\ts_nop 0\n\tbuffer_load_dwordx4 %1, %2, %3 offen lds" ::"s"(ldsaddr), "v"(voff), "s"(srd), "s"(soff) : "memory");
}
template <int OFF> DI void bload16(bf16x8& dst, unsigned voff, __amdgpu_buffer_rsrc_t srd, unsigned soff) {
  asm volatile("buffer_load_dwordx4 %0, %1, %2, %3 offen offset:%4" : "=v"(dst) : "v"(voff), "s"(srd), "s"(soff), "i"(OFF) : "memory");
}
template <int OFF> DI void bload16c(bf16x8& dst, unsigned voff, __amdgpu_buffer_rsrc_t srd, unsigned soff) {
  asm volatile("buffer_load_dwordx4 %0, %1, %2, %3 offen offset:%4 sc1" : "=v"(dst) : "v"(voff), "s"(srd), "s"(soff), "i"(OFF) : "memory");
}
DI void dma16c(unsigned ldsaddr, unsigned voff, __amdgpu_buffer_rsrc_t srd, unsigned soff) {
  asm volatile("s_mov_b32 m0, %0\n\ts_nop 0\n\tbuffer_load_dwordx4 %1, %2, %3 offen sc1 lds" ::"s"(ldsaddr), "v"(voff), "s"(srd), "s"(soff) : "memory");
}
typedef LAS const char* lds_cptr;
typedef short v4i16_t __attribute__((ext_vector_type(4)));
DI s16x4 vtr(lds_cptr p) { return __builtin_bit_cast(s16x4, __builtin_amdgcn_ds_read_tr16_b64_v4i16((LAS v4i16_t*)p)); }
DI bf16x8 trfrag(lds_cptr p0, lds_cptr p1) { const s16x4 lo = vtr(p0), hi = vtr(p1); return __builtin_shufflevector(lo, hi, 0, 1, 2, 3, 4, 5, 6, 7); }
DI bf16x8 rowfrag(lds_cptr p) { return *(LAS const bf16x8*)p; }
DI float max3(float a, float b, float c) { return __builtin_fmaxf(__builtin_fmaxf(a, b), c); }

template <int N> DI void wait_bar4(bf16x8 (&b)[4]) {
  asm volatile("s_waitcnt vmcnt(%4)\n\ts_barrier" : "+v"(b[0]), "+v"(b[1]), "+v"(b[2]), "+v"(b[3]) : "i"(N) : "memory");
}
template <int N> DI void wait_bar() { asm volatile("s_waitcnt vmcnt(%0)\n\ts_barrier" ::"i"(N) : "memory"); }
template <int V> using IC = std::integral_constant<int, V>;

template <int N> DI void wait_bar4b(bf16x8 (&b)[2][2]) {
  asm volatile("s_waitcnt vmcnt(%4)\n\ts_barrier" : "+v"(b[0][0]), "+v"(b[0][1]), "+v"(b[1][0]), "+v"(b[1][1]) : "i"(N) : "memory");
}
template <int NQB, int NT, int KS> struct Ring {
  static constexpr int KSP = 8 / NQB, SK = 2 * KSP, NSTG = (KS + SK - 1) / SK, NP = SK * NT, PA = (NP + 7) / 8, SB = PA * 8 * 1024, FR = 2 * NT;
};
template <int NQB, int NT, int KS, int KC_A, int NSLOT> struct RingA {
  using R = Ring<NQB, NT, KS>;
  static constexpr int NS_ = NSLOT;
  unsigned voff[R::PA]; unsigned ldsw;
  DI void init(unsigned lds0, int wid, int lane) {
    ldsw = lds0 + wid * 1024;
#pragma unroll
    for (int i = 0; i < R::PA; ++i) { const int p = i * 8 + wid, ks = p / NT, nt = p - ks * NT; voff[i] = p < R::NP ? (unsigned)(nt * KC_A * 256 + ks * 1024 + lane * 16) : 0x7ff00000u; }
  }
  template <int S, int I> DI void issue(__amdgpu_buffer_rsrc_t ra) const { dma16c(ldsw + ((S % NSLOT) * R::SB + I * 8192), voff[I], ra, S * R::SK * 1024); }
  template <int S0, int S1> DI void pre(__amdgpu_buffer_rsrc_t ra) const { static_for<S1 - S0>(LAMBDA(sc) { static_for<R::PA>(LAMBDA(ic) { this->template issue<S0 + sc.value, ic.value>(ra); }); }); }
};
template <int NQB, int NT, int KS, int KC_A, int KC_B, int NPRE, int NSLOT, int NHOOK, class HOOK>
DI void ring_gemm(f32x4 (&acc)[NT][2], const RingA<NQB, NT, KS, KC_A, NSLOT>& RA, __amdgpu_buffer_rsrc_t ra, __amdgpu_buffer_rsrc_t rb, const char* lds, int wid, int lane, HOOK&& hook) {
  using R = Ring<NQB, NT, KS>;
  constexpr int SK = R::SK, NSTG = R::NSTG, PA = R::PA, FR = R::FR, PD = 5;
  static_assert(NSTG >= 3 && FR > PD + 1, "");
  const int kq = wid / NQB;
  bf16x8 B[3][2][2];
  const unsigned voffB = lane * 16 + 2 * kq * 1024;
  auto issueA1 = [&](auto sc, auto ic) __attribute__((always_inline)) { RA.template issue<sc.value, ic.value>(ra); };
  auto issueB1 = [&](auto sc, auto ic) __attribute__((always_inline)) {
    constexpr int s = sc.value, i = ic.value, ks = i >> 1, q = i & 1;
    bload16c<0>(B[s % 3][ks][q], voffB, rb, q * KC_B * 256 + (s * SK + ks) * 1024);
  };
  if constexpr (NPRE == 0) static_for<PA>(LAMBDA(ic) { issueA1(IC<0>{}, ic); });
  static_for<4>(LAMBDA(ic) { issueB1(IC<0>{}, ic); });
  if constexpr (NPRE > 1) { static_for<4>(LAMBDA(ic) { issueB1(IC<1>{}, ic); }); wait_bar4b<4>(B[0]); } else wait_bar4b<0>(B[0]);
  const LAS bf16x8* pl = (const LAS bf16x8*)lds + lane + 2 * kq * NT * 64;
  bf16x8 A[PD + 1];
  auto rd = [&](auto mc) __attribute__((always_inline)) { constexpr int M = mc.value, s = M / FR, m = M % FR; A[M % (PD + 1)] = pl[((s % NSLOT) * PA * 8 + m) * 64]; };
  static_for<PD>(LAMBDA(jc) { rd(jc); });
  SBAR();
  static_for<NSTG>(LAMBDA(sc) {
    constexpr int s = sc.value;
    constexpr int n0 = (NPRE <= 1 && s == 0) ? PA + 4 : 0;
    constexpr int sa = s + 2;
    constexpr int nA = (sa < NSTG && sa >= NPRE) ? PA : 0, nB = (s + 2 < NSTG) ? 4 : 0, NI = n0 + nA + nB, NGI = FR - PD - 1;
    constexpr bool full = (s + 1) * SK <= KS;
    static_for<FR>(LAMBDA(mc) {
      constexpr int m = mc.value, M = s * FR + m, ks = m / NT, nt = m % NT;
      if constexpr (m == FR - PD && s + 1 < NSTG) wait_bar4b<nA + nB>(B[(s + 1) % 3]);
      if constexpr (M + PD < NSTG * FR) rd(IC<M + PD>{});
      if constexpr (m < NGI) {
        constexpr int i0 = (m * NI + NGI - 1) / NGI, i1 = ((m + 1) * NI + NGI - 1) / NGI;
        static_for<i1 - i0>(LAMBDA(uc) {
          constexpr int i = i0 + uc.value;
          if constexpr (i < n0) { if constexpr (i < PA) issueA1(IC<1>{}, IC<i>{}); else issueB1(IC<1>{}, IC<i - PA>{}); }
          else if constexpr (i - n0 < nA) issueA1(IC<sa>{}, IC<i - n0>{});
          else issueB1(IC<s + 2>{}, IC<i - n0 - nA>{});
        });
      }
      if constexpr (NHOOK > 0 && M >= (NSTG - 1) * FR - PD) {
        constexpr int j = M - ((NSTG - 1) * FR - PD), HG = FR + PD - 2, i0 = (j * NHOOK + HG - 1) / HG, i1 = ((j + 1) * NHOOK + HG - 1) / HG;
        if constexpr (j < HG) static_for<i1 - i0>(LAMBDA(uc) { hook(IC<i0 + uc.value>{}); });
      }
      if (full || s * SK + 2 * kq + ks < KS) { acc[nt][0] = mfma16(A[M % (PD + 1)], B[s % 3][ks][0], acc[nt][0]); acc[nt][1] = mfma16(A[M % (PD + 1)], B[s % 3][ks][1], acc[nt][1]); }
      SBAR();
    });
  });
}
template <int NT> DI void pair_reduce(f32x4 (&acc)[NT][2], char* lds, int wid, int lane) {
  constexpr int H0 = (NT + 1) / 2;
  f32x4* red = (f32x4*)lds + (size_t)wid * H0 * 2 * 64 + lane;
  const f32x4* in = (const f32x4*)lds + (size_t)(wid ^ 4) * H0 * 2 * 64 + lane;
  lds_barrier();
  if (wid < 4) {
    asm volatile("; lo-send" ::: "memory");
#pragma unroll
    for (int nt = H0; nt < NT; ++nt) { red[((nt - H0) * 2) * 64] = acc[nt][0]; red[((nt - H0) * 2 + 1) * 64] = acc[nt][1]; }
    asm volatile("; lo-sent" ::: "memory");
  } else {
    asm volatile("; hi-send" ::: "memory");
#pragma unroll
    for (int nt = 0; nt < H0; ++nt) { red[(nt * 2) * 64] = acc[nt][0]; red[(nt * 2 + 1) * 64] = acc[nt][1]; }
    asm volatile("; hi-sent" ::: "memory");
  }
  lds_barrier();
  if (wid < 4) {
    asm volatile("; lo-recv" ::: "memory");
#pragma unroll
    for (int nt = 0; nt < H0; ++nt) { acc[nt][0] += in[(nt * 2) * 64]; acc[nt][1] += in[(nt * 2 + 1) * 64]; PIN(acc[nt][0]); PIN(acc[nt][1]); }
    asm volatile("; lo-done" ::: "memory");
  } else {
    asm volatile("; hi-recv" ::: "memory");
#pragma unroll
    for (int nt = H0; nt < NT; ++nt) { acc[nt][0] += in[((nt - H0) * 2) * 64]; acc[nt][1] += in[((nt - H0) * 2 + 1) * 64]; PIN(acc[nt][0]); PIN(acc[nt][1]); }
    asm volatile("; hi-done" ::: "memory");
  }
}

__global__ __launch_bounds__(512, 2) void k_block(const u16* a_x, const u16* a_kc, const u16* a_vc, const u16* a_wq, const u16* a_wo, const u16* a_w1, const u16* a_b1, const u16* a_w2,
                                                   const u16* a_b2, const u16* a_c, const u16* a_wmod, u16* a_out, char* a_ws) {
  extern __shared__ __attribute__((aligned(16))) char lds[];
  const int tid = threadIdx.x, lane = tid & 63, r32 = lane & 31, hi = lane >> 5;
  const int wid = __builtin_amdgcn_readfirstlane(tid >> 6);
  const int b = blockIdx.x, xcd = b & 7, idx = b >> 3;
  const int head = (xcd & 3) * 8 + (idx & 7), qt = (xcd >> 2) * 4 + (idx >> 3);
  const unsigned lds0 = (unsigned)(uintptr_t)lds;
  char* const ws = karg<char>(KA_WS);
  unsigned* const sync = (unsigned*)(ws + WS_SYNC);
  const __amdgpu_buffer_rsrc_t srdW = __builtin_amdgcn_make_buffer_rsrc((void*)(karg<const u16>(KA_WQ) + head * DH), 0, (DM - 1) * 8192 + 256, 0x00020000);
  const int dr = (lane >> 2) & 7, dx = lane & 3, dc = lane >> 5, dz = (2 * wid + (dr >> 2)) & 3;
  const unsigned voffW = (unsigned)((8 * wid + dr) * 8192 + 64 * dc + 16 * (dx ^ dz));
  const unsigned ldsw = lds0 + 2048 * wid;
#pragma unroll
  for (int i = 0; i < 3; ++i) { dma16(ldsw + slot_of(i), voffW, srdW, i * 64 * 8192); dma16(ldsw + slot_of(i) + 1024, voffW, srdW, i * 64 * 8192 + 128); }
  unsigned tag;
  {
    float* sm = (float*)(lds + NS * SLOT);
    const u16* x = karg<const u16>(KA_X);
    const int row = b * 8 + wid; const bool act = lane < 40;
    u32x4 xv[2];
    xv[0] = *(const u32x4*)(x + (size_t)row * DM + (act ? lane : 0) * 8); xv[1] = *(const u32x4*)(x + (size_t)row * DM + (act ? lane + 40 : 0) * 8);
    u32x4 w[5]; unsigned cv = 0;
    const int cc = tid & 3, r0 = tid >> 2;
    if (b < 120) {
      const u16* wmod = karg<const u16>(KA_WMOD);
#pragma unroll
      for (int i = 0; i < 5; ++i) w[i] = *(const u32x4*)(wmod + (size_t)(r0 + 128 * i) * 3840 + b * 32 + cc * 8);
      cv = *(const unsigned*)(karg<const u16>(KA_C) + 2 * (tid < 320 ? tid : 0));
    }
    unsigned tg = __hip_atomic_load((gu32*)sync, RLX_AGENT) + 1u;
    if (b < 120 && tid < 320) {
      const float v0 = bf_lo(cv), v1 = bf_hi(cv);
      const unsigned pk = cvtpk(v0 * __builtin_amdgcn_rcpf(1.f + __expf(-v0)), v1 * __builtin_amdgcn_rcpf(1.f + __expf(-v1)));
      *(f32x2*)(sm + 512 + 2 * tid) = f32x2{bf_lo(pk), bf_hi(pk)};
    }
    tag = __builtin_amdgcn_readfirstlane(tg);
    lds_barrier();
    if (tid == 0) __hip_atomic_store((gu32*)sync + SY_STARTED + b, tag, RLX_AGENT);
    if (b < 120) {
      float a[8] = {};
#pragma unroll
      for (int i = 0; i < 5; ++i) {
        const float sv = sm[512 + r0 + 128 * i];
#pragma unroll
        for (int j = 0; j < 4; ++j) { a[2 * j] += sv * bf_lo(w[i][j]); a[2 * j + 1] += sv * bf_hi(w[i][j]); }
      }
#pragma unroll
      for (int j = 0; j < 8; ++j) a[j] = xor_add<32>(xor_add<16>(xor_add<8>(xor_add<4>(a[j]))));
      if (lane < 4) {
#pragma unroll
        for (int j = 0; j < 8; ++j) sm[wid * 32 + cc * 8 + j] = a[j];
      }
      lds_barrier();
      if (tid < 128) {
        const int u = tid & 15;
        float lo = 0.f, hi_ = 0.f;
#pragma unroll
        for (int w8 = 0; w8 < 8; ++w8) { lo += sm[w8 * 32 + 2 * u]; hi_ += sm[w8 * 32 + 2 * u + 1]; }
        unsigned pk = cvtpk(lo, hi_);
        if ((b >= 20 && b < 40) || (b >= 80 && b < 100)) pk = cvtpk(1.f + bf_lo(pk), 1.f + bf_hi(pk));
        if (b < 40) __hip_atomic_store((gu64*)(sync + SY_GRAN + (tid >> 4) * 2048) + b * 16 + u, ((unsigned long long)tag << 32) | pk, RLX_AGENT);
        else if (tid < 16) __hip_atomic_store((gu32*)(ws + WS_MOD) + b * 16 + u, pk, RLX_AGENT);
      }
    }
    float f[16]; float s = 0;
#pragma unroll
    for (int j = 0; j < 2; ++j)
#pragma unroll
      for (int e = 0; e < 4; ++e) { f[j * 8 + 2 * e] = act ? bf_lo(xv[j][e]) : 0.f; f[j * 8 + 2 * e + 1] = act ? bf_hi(xv[j][e]) : 0.f; s += f[j * 8 + 2 * e] + f[j * 8 + 2 * e + 1]; }
    s = xor_add<32>(xor_add<16>(xor_add<8>(xor_add<4>(xor_add<2>(xor_add<1>(s))))));
    const float mean = s * (1.f / DM); float q = 0;
#pragma unroll
    for (int e = 0; e < 16; ++e) { const float d = act ? f[e] - mean : 0.f; q += d * d; }
    q = xor_add<32>(xor_add<16>(xor_add<8>(xor_add<4>(xor_add<2>(xor_add<1>(q))))));
    const float mb = rbf(mean), rsd = 1.f / rbf(sqrtf(rbf(rbf(q * (1.f / DM)) + 1e-5f)));
#pragma unroll
    for (int e = 0; e < 16; e += 2) {
      const unsigned d = cvtpk(f[e] - mb, f[e + 1] - mb), n = cvtpk(bf_lo(d) * rsd, bf_hi(d) * rsd);
      f[e] = bf_lo(n); f[e + 1] = bf_hi(n);
    }
    if (wid == 0) {
      const __amdgpu_buffer_rsrc_t rg = __builtin_amdgcn_make_buffer_rsrc((void*)(sync + SY_GRAN + xcd * 2048), 0, 5120, 0x00020000);
      u32x4 gv[5];
      for (unsigned spins = 0; spins < SPIN_CAP; ++spins) {
        bool ok = true;
#pragma unroll
        for (int k = 0; k < 5; ++k) { gv[k] = ld16c(rg, (lane + 64 * k) * 16); ok &= gv[k][1] == tag && gv[k][3] == tag; }
        if (__all(ok)) break;
        __builtin_amdgcn_s_sleep(2);
      }
#pragma unroll
      for (int k = 0; k < 5; ++k) *(f32x4*)(sm + 1280 + 4 * (lane + 64 * k)) = f32x4{bf_lo(gv[k][0]), bf_hi(gv[k][0]), bf_lo(gv[k][2]), bf_hi(gv[k][2])};
    }
    lds_barrier();
    u32x4* hs = (u32x4*)(lds + (NS + 1) * SLOT);
    if (act) {
#pragma unroll
      for (int j = 0; j < 2; ++j) {
        const int k0 = (lane + 40 * j) * 8;
        const f32x4 s0 = *(const f32x4*)(sm + 1280 + k0), s1 = *(const f32x4*)(sm + 1284 + k0), c0 = *(const f32x4*)(sm + 1920 + k0), c1 = *(const f32x4*)(sm + 1924 + k0);
        const float sa[8] = {s0[0], s0[1], s0[2], s0[3], s1[0], s1[1], s1[2], s1[3]}, oc[8] = {c0[0], c0[1], c0[2], c0[3], c1[0], c1[1], c1[2], c1[3]};
        u32x4 o;
#pragma unroll
        for (int e = 0; e < 4; ++e) {
          const unsigned m = cvtpk(f[j * 8 + 2 * e] * oc[2 * e], f[j * 8 + 2 * e + 1] * oc[2 * e + 1]);
          o[e] = cvtpk(bf_lo(m) + sa[2 * e], bf_hi(m) + sa[2 * e + 1]);
        }
        hs[(lane + 40 * j) * 8 + wid] = o;
      }
    }
    lds_barrier();
    {
      const __amdgpu_buffer_rsrc_t rh = __builtin_amdgcn_make_buffer_rsrc((void*)(ws + WS_H), 0, SQ * DM * 2, 0x00020000);
      const unsigned base = (unsigned)(((b >> 1) * 80 * 16 + (b & 1) * 8) * 16);
      st16(rh, base + (tid >> 3) * 256 + (tid & 7) * 16, hs[tid]);
      if (tid < 128) st16(rh, base + (64 + (tid >> 3)) * 256 + (tid & 7) * 16, hs[512 + tid]);
    }
    publish(sync + SY_HF + b, tag);
    await_flags<32>(sync + SY_HF + qt * 32, tag);
  }
  const __amdgpu_buffer_rsrc_t srdK = __builtin_amdgcn_make_buffer_rsrc((void*)(karg<const u16>(KA_KC) + ((size_t)head * SK + (SK - WIN)) * DH), 0, WIN * 256, 0x00020000);
  const __amdgpu_buffer_rsrc_t srdV = __builtin_amdgcn_make_buffer_rsrc((void*)(karg<const u16>(KA_VC) + ((size_t)head * SK + (SK - WIN)) * DH), 0, WIN * 256, 0x00020000);
  const __amdgpu_buffer_rsrc_t srdH = __builtin_amdgcn_make_buffer_rsrc((void*)(ws + WS_H + (size_t)(qt * 16 + 2 * wid) * 80 * 256), 0, 2 * 80 * 256, 0x00020000);
  const unsigned voffKV = (unsigned)((8 * wid + dr) * 256 + 64 * dc + 16 * (dx ^ dz));
  const unsigned voffH = (unsigned)(((lane >> 4) & 1) * 20480 + (lane >> 5) * 256 + (lane & 15) * 16);
  bf16x8 hf[10][4], rtf[2];
  __amdgpu_buffer_rsrc_t rsrc_u[3]; unsigned pitch_u[3], dst_u[3];
  const __amdgpu_buffer_rsrc_t srdT = __builtin_amdgcn_make_buffer_rsrc((void*)(ws + WS_WOT), 0, (int)(WS_W2T - WS_WOT) + DM * FF * 2, 0x00020000);
  auto vmem_op = [&](auto oc) __attribute__((always_inline)) {
    constexpr int op = oc.value;
    if constexpr (op >= 310) {
      constexpr int u = (op - 310) >> 1, ks = op & 1;
      __builtin_amdgcn_raw_buffer_store_b128(__builtin_bit_cast(u32x4, rtf[ks]), srdT, lane * 16 + ks * 1024, dst_u[u], O_AUX);
    } else if constexpr (op >= 300) {
      constexpr int u = (op - 300) >> 1, ks = op & 1;
      const int g4 = lane >> 4, chq = 2 * wid + ((lane & 3) >> 1), q4_ = (lane & 15) >> 2;
      const lds_cptr p0 = (lds_cptr)lds + rt_slot(u) + 2048 * (4 * ks + g4) + 512 * (chq >> 2) + 64 * q4_ + 16 * ((chq & 3) ^ ((2 * g4) & 3)) + 8 * (lane & 1);
      const lds_cptr p1 = (lds_cptr)lds + rt_slot(u) + 2048 * (4 * ks + g4) + 512 * (chq >> 2) + 64 * (4 + q4_) + 16 * ((chq & 3) ^ ((2 * g4 + 1) & 3)) + 8 * (lane & 1);
      rtf[ks] = trfrag(p0, p1);
    } else if constexpr (op >= 200) {
      constexpr int u = (op - 200) >> 1, half = op & 1;
      dma16(ldsw + rt_slot(u) + 1024 * half, (unsigned)(8 * wid + dr) * pitch_u[u] + (unsigned)(64 * dc + 16 * (dx ^ dz)), rsrc_u[u], 128 * half);
    } else if constexpr (op >= 100) { constexpr int i = (op - 100) >> 2, k = (op - 100) & 3; bload16c<512 * k>(hf[i][k], voffH, srdH, i * 2048); }
    else {
      constexpr int i = op >> 1, half = op & 1; constexpr unsigned so = slot_of(i) + 1024 * half;
      if constexpr (i < 10) dma16(ldsw + so, voffW, srdW, i * 64 * 8192 + 128 * half);
      else {
        constexpr bool isK = (i == 10) || (i != 25 && (i & 1)); constexpr int t = i == 10 ? 0 : (i == 25 ? 7 : (isK ? (i - 9) / 2 : (i - 12) / 2));
        dma16(ldsw + so, voffKV, isK ? srdK : srdV, t * 16384 + 128 * half);
      }
    }
  };
  static_for<SCH.npro>(LAMBDA(jc) { vmem_op(IC<SCH.pro[jc.value]>{}); });
  SBAR();
#pragma unroll
  for (int u = 0; u < 3; ++u) {
    const int id = blockIdx.x + 256 * u;
    const u16* src; int pitch, rows, kt, nt, kc, nsub; unsigned dbase;
    if (id < 320) { src = karg<const u16>(KA_WO); pitch = 1280; rows = 4096; kt = id / 5; nt = id - kt * 5; kc = 512; nsub = 40; dbase = 0; }
    else if (id < 455) { const int v = id - 320; src = karg<const u16>(KA_W2); pitch = 1280; rows = 1728; kt = v / 5; nt = v - kt * 5; kc = 216; nsub = 40; dbase = (unsigned)(WS_W2T - WS_WOT); }
    else { const int v = id < 595 ? id - 455 : 0; src = karg<const u16>(KA_W1); pitch = 3456; rows = id < 595 ? 640 : 0; kt = v / 14; nt = v - kt * 14; kc = 80; nsub = id < 595 ? 108 : 0; dbase = (unsigned)(WS_W1T - WS_WOT); }
    const int first = kt * 64 * pitch + nt * 256;
    rsrc_u[u] = __builtin_amdgcn_make_buffer_rsrc((void*)((const char*)src + first), 0, rows ? rows * pitch - first : 0, 0x00020000);
    pitch_u[u] = pitch;
    dst_u[u] = nt * 8 + wid < nsub ? dbase + ((nt * 8 + wid) * kc + kt * 8) * 256 : 0x7ff00000u;
  }
  auto vmem_gap = [&](auto gc, auto jc, auto nc) __attribute__((always_inline)) {
    constexpr int g = gc.value, j = jc.value, n = nc.value, no = SCH.nops[g];
    constexpr int i0 = (j * no + n - 1) / n, i1 = ((j + 1) * no + n - 1) / n;
    static_for<i1 - i0>(LAMBDA(uc) { vmem_op(IC<SCH.op[g][i0 + uc.value]>{}); });
  };

  const int blk = (lane >> 4) & 1, q4 = (lane & 15) >> 2, p4 = lane & 3;
  constexpr int NR = (NS * SLOT + 65535) / 65536;
  const lds_cptr L = (lds_cptr)lds;
  lds_cptr wb[NR][2], vb[NR][2], kb_[NR][2];
#pragma unroll
  for (int r = 0; r < NR; ++r)
#pragma unroll
    for (int t = 0; t < 2; ++t) {
      wb[r][t] = L + 65536 * r + 2048 * hi + 64 * (4 * t + q4) + 16 * ((2 * blk + (p4 & 1)) ^ (2 * hi + t)) + 8 * (p4 >> 1);
      PIN(wb[r][t]);
      vb[r][t] = L + 65536 * r + 64 * (4 * hi + q4) + 16 * ((2 * blk + (p4 & 1)) ^ (2 * t + hi)) + 8 * (p4 >> 1) + 2048 * t;
      kb_[r][t] = L + 65536 * r + 2048 * (r32 >> 3) + 64 * (r32 & 7) + 16 * ((2 * t + hi) ^ ((r32 >> 2) & 3));
    }
  constexpr int PD = A_PD, NG = 416;
  bf16x8 fr[PD + 1];
  auto ldfrag_g = [&](auto Gc) __attribute__((always_inline)) {
    constexpr int G = Gc.value;
    if constexpr (G < NG) {
      if constexpr (G < 160) {
        constexpr int i = G >> 4, ks = (G >> 2) & 3, db = G & 3, o = slot_of(i) + 4096 * ks + 512 * db;
        fr[G % (PD + 1)] = trfrag(wb[o >> 16][0] + (o & 65535), wb[o >> 16][1] + (o & 65535));
      } else {
        constexpr int u = G - 176, t = G < 176 ? 0 : (G >= 400 ? 8 : 1 + u / 32), g = G < 176 ? G - 160 : (G >= 400 ? G - 400 + 16 : u % 32);
        if constexpr (g < 16) {
          constexpr int kb = g >> 3, s = g & 7, o = slot_of(tile_K(t)) + 8192 * kb + 512 * (s >> 1);
          fr[G % (PD + 1)] = rowfrag(kb_[o >> 16][s & 1] + (o & 65535));
        } else {
          constexpr int gg = g - 16, ks = t == 8 ? (gg & 3) : (gg >> 2), db = t == 8 ? (gg >> 2) : (gg & 3), o = slot_of(tile_V(t - 1)) + 4096 * ks + 512 * db;
          fr[G % (PD + 1)] = trfrag(vb[o >> 16][0] + (o & 65535), vb[o >> 16][1] + (o & 65535));
        }
      }
    }
  };
  auto barrier = [&](auto gc) __attribute__((always_inline)) {
    constexpr int g = gc.value;
    if constexpr (g < 10) wait_bar4<SCH.wait[g]>(hf[g]); else wait_bar<SCH.wait[g]>();
    if constexpr (g == SCH.rt_done) { if (tid == 0) __hip_atomic_store((gu32*)sync + SY_RF + b, tag, RLX_AGENT); }
    SBAR();
  };

  f32x16 acc[4] = {};
  static_for<10>(LAMBDA(ic) {
    constexpr int i = ic.value;
    barrier(ic);
    if constexpr (i == 0) { static_for<PD>(LAMBDA(jc) { ldfrag_g(jc); }); SBAR(); }
    static_for<16>(LAMBDA(mc) {
      constexpr int m = mc.value, G = 16 * i + m;
      ldfrag_g(IC<G + PD>{});
      vmem_gap(ic, mc, IC<16>{});
      acc[m & 3] = mfma32(fr[G % (PD + 1)], hf[i][m >> 2], acc[m & 3]);
      SBAR();
    });
  });
  bf16x8 qf[8];
#pragma unroll
  for (int s = 0; s < 8; ++s) {
    u32x4 w;
#pragma unroll
    for (int e = 0; e < 4; ++e) w[e] = cvtpk(acc[s >> 1][8 * (s & 1) + 2 * e] * QSCALE, acc[s >> 1][8 * (s & 1) + 2 * e + 1] * QSCALE);
    qf[s] = __builtin_bit_cast(bf16x8, w);
  }
#pragma unroll
  for (int r = 0; r < NR; ++r)
#pragma unroll
    for (int t = 0; t < 2; ++t) { PIN(vb[r][t]); PIN(kb_[r][t]); }

  const int qrow = qt * 256 + wid * 32 + r32;
  const __amdgpu_buffer_rsrc_t srdO = __builtin_amdgcn_make_buffer_rsrc((void*)(ws + WS_O), 0, SQ * 4096 * 2, 0x00020000);
  const unsigned voffO = (((qrow >> 4) * 512 + head * 16 + hi) * 16 + (qrow & 15)) * 16;
  f32x16 o[4] = {}; f32x16 sA[2], sB[2], negm; u32x4 pw[4];
  float mref, lsum = 0.f; bool bad = false;
  barrier(IC<10>{});
  {
    const f32x16 zero = {};
    static_for<16>(LAMBDA(gc) {
      constexpr int g = gc.value, G = 160 + g;
      ldfrag_g(IC<G + PD>{});
      vmem_gap(IC<10>{}, gc, IC<16>{});
      sA[g >> 3] = mfma32(fr[G % (PD + 1)], qf[g & 7], (g & 7) == 0 ? zero : sA[g >> 3]);
      SBAR();
    });
    float a = max3(sA[0][0], sA[0][1], sA[1][0]), b = max3(sA[0][2], sA[0][3], sA[1][1]); a = max3(a, sA[1][2], sA[1][3]);
#pragma unroll
    for (int r = 4; r < 16; r += 4) { a = max3(a, sA[0][r], sA[0][r + 1]); b = max3(b, sA[0][r + 2], sA[0][r + 3]); a = max3(a, sA[1][r], sA[1][r + 1]); b = max3(b, sA[1][r + 2], sA[1][r + 3]); }
    mref = swap_max(__builtin_fmaxf(a, b)) + PBIAS;
#pragma unroll
    for (int r = 0; r < 16; ++r) negm[r] = -mref;
    PIN(negm);
    sA[0][0] = __builtin_amdgcn_exp2f(sA[0][0] - mref); sA[0][1] = __builtin_amdgcn_exp2f(sA[0][1] - mref);
    SBAR();
  }
  auto step = [&](auto tc, f32x16(&C)[2], f32x16(&P)[2]) __attribute__((always_inline)) {
    constexpr int t = tc.value;
    constexpr int G0 = 176 + 32 * (t - 1), GB = t < 8 ? G0 + 16 : 400;
    barrier(IC<10 + t>{});
    float sacc = 0.f;
    static_for<16>(LAMBDA(gc) {
      constexpr int g = gc.value;
      if constexpr (t < 8) {
        ldfrag_g(IC<G0 + g + PD>{});
        vmem_gap(IC<10 + t>{}, gc, IC<32>{});
        C[g >> 3] = mfma32(fr[(G0 + g) % (PD + 1)], qf[g & 7], (g & 7) == 0 ? negm : C[g >> 3]);
      }
      if constexpr (t == 1 && g < 15) {
        constexpr int e1 = 2 * (g + 1);
        P[e1 >> 4][e1 & 15] = __builtin_amdgcn_exp2f(P[e1 >> 4][e1 & 15] - mref); P[e1 >> 4][(e1 & 15) + 1] = __builtin_amdgcn_exp2f(P[e1 >> 4][(e1 & 15) + 1] - mref);
        PIN(P[e1 >> 4]);
      }
      constexpr int ks = g >> 2, w = g & 3, e = 8 * (ks & 1) + 2 * w;
      pw[ks][w] = cvtpk(P[ks >> 1][e], P[ks >> 1][e + 1]);
      sacc += P[ks >> 1][e]; sacc += P[ks >> 1][e + 1];
      PIN(pw[ks]); PIN(sacc);
      SBAR();
    });
    bad |= !(sacc <= PLIM);
    lsum += sacc;
    if constexpr (t < 8) {
      static_for<16>(LAMBDA(gc) {
        constexpr int g = gc.value;
        ldfrag_g(IC<GB + g + PD>{});
        vmem_gap(IC<10 + t>{}, IC<16 + g>{}, IC<32>{});
        o[g & 3] = mfma32(fr[(GB + g) % (PD + 1)], __builtin_bit_cast(bf16x8, pw[g >> 2]), o[g & 3]);
        C[g >> 3][(2 * g) & 15] = __builtin_amdgcn_exp2f(C[g >> 3][(2 * g) & 15]); C[g >> 3][(2 * g + 1) & 15] = __builtin_amdgcn_exp2f(C[g >> 3][(2 * g + 1) & 15]);
        PIN(C[g >> 3]);
        SBAR();
      });
    }
  };
  auto out_db = [&](auto dc, float inv) __attribute__((always_inline)) {
    constexpr int db = dc.value;
#pragma unroll
    for (int s = 0; s < 2; ++s) {
      u32x4 w;
#pragma unroll
      for (int e = 0; e < 4; ++e) w[e] = cvtpk(o[db][8 * s + 2 * e] * inv, o[db][8 * s + 2 * e + 1] * inv);
      __builtin_amdgcn_raw_buffer_store_b128(w, srdO, voffO + (4 * db + 2 * s) * 256, 0, O_AUX);
    }
  };
  step(IC<1>{}, sB, sA);
  step(IC<2>{}, sA, sB);
  step(IC<3>{}, sB, sA);
  step(IC<4>{}, sA, sB);
  step(IC<5>{}, sB, sA);
  step(IC<6>{}, sA, sB);
  step(IC<7>{}, sB, sA);
  step(IC<8>{}, sA, sB);
  if (__builtin_expect(!__any(bad), 1)) {
    const float inv = 1.f / swap_add(lsum);
    static_for<16>(LAMBDA(gc) {
      constexpr int g = gc.value;
      ldfrag_g(IC<400 + g + PD>{});
      o[g >> 2] = mfma32(fr[(400 + g) % (PD + 1)], __builtin_bit_cast(bf16x8, pw[g & 3]), o[g >> 2]);
      if constexpr (g >= 5 && (g & 3) == 1) out_db(IC<(g >> 2) - 1>{}, inv);
      SBAR();
    });
    out_db(IC<3>{}, inv);
  } else {
    const unsigned reg = (unsigned)((wid == 0 ? 0 : wid + 1) * SLOT);
    static_assert(slot_of(tile_V(7)) == SLOT, "");
    const lds_cptr kbs[2] = {kb_[0][0] + reg, kb_[0][1] + reg}, vbs[2] = {vb[0][0] + reg + 8192, vb[0][1] + reg + 8192};
    float m = -__builtin_inff(), l = 0.f;
#pragma unroll
    for (int d = 0; d < 4; ++d) o[d] = f32x16{};
#pragma unroll 1
    for (int kb = 0; kb < WIN / 32; ++kb) {
#pragma unroll
      for (int R = 0; R < 4; ++R) {
        const unsigned vo = (unsigned)((8 * R + dr) * 256 + 64 * dc + 16 * (dx ^ ((2 * R + (dr >> 2)) & 3)));
        const unsigned so = __builtin_amdgcn_readfirstlane(kb * 8192), ld = __builtin_amdgcn_readfirstlane(lds0 + reg + 2048 * R);
        asm volatile("s_nop 4" ::: "memory");
        dma16(ld, vo, srdK, so); dma16(ld + 1024, vo + 128, srdK, so); dma16(ld + 8192, vo, srdV, so); dma16(ld + 9216, vo + 128, srdV, so);
      }
      asm volatile("s_waitcnt vmcnt(0)" ::: "memory");
      f32x16 sc = {};
#pragma unroll
      for (int s = 0; s < 8; ++s) { sc = mfma32(rowfrag(kbs[s & 1] + 512 * (s >> 1)), qf[s], sc); SBAR(); }
      float mx = sc[0];
#pragma unroll
      for (int r = 1; r < 16; ++r) mx = __builtin_fmaxf(mx, sc[r]);
      const float mn = __builtin_fmaxf(m, swap_max(mx)), al = __builtin_amdgcn_exp2f(m - mn);
      m = mn; l *= al;
#pragma unroll
      for (int r = 0; r < 16; ++r) { sc[r] = __builtin_amdgcn_exp2f(sc[r] - mn); l += sc[r]; }
#pragma unroll
      for (int d = 0; d < 4; ++d)
#pragma unroll
        for (int r = 0; r < 16; ++r) o[d][r] *= al;
#pragma unroll
      for (int ks = 0; ks < 2; ++ks) {
        const u32x4 pk = {cvtpk(sc[8 * ks], sc[8 * ks + 1]), cvtpk(sc[8 * ks + 2], sc[8 * ks + 3]), cvtpk(sc[8 * ks + 4], sc[8 * ks + 5]), cvtpk(sc[8 * ks + 6], sc[8 * ks + 7])};
#pragma unroll
        for (int d = 0; d < 4; ++d) { o[d] = mfma32(trfrag(vbs[0] + 4096 * ks + 512 * d, vbs[1] + 4096 * ks + 512 * d), __builtin_bit_cast(bf16x8, pk), o[d]); SBAR(); }
      }
      asm volatile("s_waitcnt lgkmcnt(0)" ::: "memory");
    }
    const float inv = 1.f / swap_add(l);
    static_for<4>(LAMBDA(dc_) { out_db(dc_, inv); });
  }
  const int rt = (xcd >> 2) * 8 + (idx >> 2), gi = (xcd & 3) * 4 + (idx & 3);
  {
    unsigned rf[4] = {tag, tag, tag, tag};
    if (wid == 0) {
#pragma unroll
      for (int k = 0; k < 4; ++k) rf[k] = __hip_atomic_load((gu32*)sync + SY_RF + lane + 64 * k, RLX_AGENT);
    }
    asm volatile("s_waitcnt vmcnt(0)" ::: "memory");
    if (wid == 0) {
      for (unsigned spins = 0; spins < SPIN_CAP; ++spins) {
        if (__all(rf[0] == tag && rf[1] == tag && rf[2] == tag && rf[3] == tag)) break;
#pragma unroll
        for (int k = 0; k < 4; ++k) rf[k] = __hip_atomic_load((gu32*)sync + SY_RF + lane + 64 * k, RLX_AGENT);
      }
    }
    asm volatile("" ::: "memory");
    __syncthreads();
    if (tid == 0) __hip_atomic_store((gu32*)sync + SY_OF + qt * 32 + head, tag, RLX_AGENT);
  }
  unsigned* const c1 = sync + SY_CF + rt * 16, * const c2 = c1 + 256, * const c3 = c1 + 512;
  const u16* const mod = (const u16*)(ws + WS_MOD);
  const __amdgpu_buffer_rsrc_t rmod = __builtin_amdgcn_make_buffer_rsrc((void*)mod, 0, 3840 * 2, 0x00020000);
  u16 *const obuf = (u16*)(ws + WS_O), *const wot = (u16*)(ws + WS_WOT), *const w1t = (u16*)(ws + WS_W1T), *const w2t = (u16*)(ws + WS_W2T);
  u16 *const x1 = (u16*)(ws + WS_X1), *const hid = (u16*)(ws + WS_HID), *const h2 = (u16*)(ws + WS_H2), *const part = (u16*)(ws + WS_P);
  const int nft = gi < 15 ? 7 : 3;
  const __amdgpu_buffer_rsrc_t raD = __builtin_amdgcn_make_buffer_rsrc((void*)(w1t + (size_t)(7 * gi) * 80 * 128), 0, nft * 80 * 256, 0x00020000);
  RingA<4, 7, 20, 80, 5> RD; RD.init(lds0, wid, lane);
  {
    const int kq = gi >> 2, j = gi & 3, qb = wid & 3;
    const __amdgpu_buffer_rsrc_t ra = __builtin_amdgcn_make_buffer_rsrc((void*)(wot + ((size_t)(10 * j) * 512 + kq * 128) * 128), 0, 10 * 512 * 256, 0x00020000);
    const __amdgpu_buffer_rsrc_t rb = __builtin_amdgcn_make_buffer_rsrc((void*)(obuf + ((size_t)(8 * rt + 2 * qb) * 512 + kq * 128) * 128), 0, 2 * 512 * 256, 0x00020000);
    const __amdgpu_buffer_rsrc_t rp = __builtin_amdgcn_make_buffer_rsrc((void*)part, 0, 4 * SQ * DM * 2, 0x00020000);
    f32x4 acc[10][2] = {};
    RingA<4, 10, 32, 512, 4> RA; RA.init(lds0, wid, lane);
    RA.pre<0, 1>(ra);
    await_flags<8>(sync + SY_OF + (rt >> 1) * 32 + kq * 8, tag);
    ring_gemm<4, 10, 32, 512, 512, 1, 4, 8>(acc, RA, ra, rb, lds, wid, lane, [&](auto ic) __attribute__((always_inline)) { RD.template issue<ic.value / 4, ic.value % 4>(raD); });
    pair_reduce<10>(acc, lds + 81920, wid, lane);
    const int nt0 = wid < 4 ? 0 : 5;
    const unsigned vo = ((((8 * rt + 2 * qb + ((lane >> 4) & 1)) * 80 + j * 20 + nt0 * 2 + (lane >> 5)) * 4 + kq) * 16 + (lane & 15)) * 16;
    auto outp = [&](auto ntc) __attribute__((always_inline)) {
      constexpr int nt = ntc.value, o = nt % 5;
      const u32x2 w0 = {cvtpk(acc[nt][0][0], acc[nt][0][1]), cvtpk(acc[nt][0][2], acc[nt][0][3])}, w1 = {cvtpk(acc[nt][1][0], acc[nt][1][1]), cvtpk(acc[nt][1][2], acc[nt][1][3])};
      st16(rp, vo + o * 2 * 1024, pair16(w0, w1));
    };
    if (wid < 4) { asm volatile("; lo-out" ::: "memory"); static_for<5>(LAMBDA(c) { outp(c); }); asm volatile("; lo-end" ::: "memory"); }
    else { asm volatile("; hi-out" ::: "memory"); static_for<5>(LAMBDA(c) { outp(IC<5 + c.value>{}); }); asm volatile("; hi-end" ::: "memory"); }
  }
  publish(c1 + gi, tag);
  {
    float* red = (float*)(lds + 98304);
    const int r = tid & 7, row = rt * 128 + gi * 8 + r;
    const bool two = wid < 2;
    const __amdgpu_buffer_rsrc_t rp = __builtin_amdgcn_make_buffer_rsrc((void*)part, 0, 4 * SQ * DM * 2, 0x00020000);
    const __amdgpu_buffer_rsrc_t rx1 = __builtin_amdgcn_make_buffer_rsrc((void*)x1, 0, SQ * DM * 2, 0x00020000);
    const __amdgpu_buffer_rsrc_t rh2 = __builtin_amdgcn_make_buffer_rsrc((void*)h2, 0, SQ * DM * 2, 0x00020000);
    const u16* x = karg<const u16>(KA_X);
    u32x4 p[2][4], xv[2], gv[2], sbv[2], cbv[2]; unsigned blk[2], pblk[2];
#pragma unroll
    for (int u = 0; u < 2; ++u) {
      const int c = (u == 0 || two) ? (tid >> 3) + 64 * u : (tid >> 3);
      blk[u] = (((row >> 4) * 80 + c) * 16 + (row & 15)) * 16; pblk[u] = (((row >> 4) * 80 + c) * 64 + (row & 15)) * 16;
      xv[u] = *(const u32x4*)(x + (size_t)row * DM + c * 8);
      gv[u] = ld16c(rmod, (2 * 640 + c * 8) * 2); sbv[u] = ld16c(rmod, (3 * 640 + c * 8) * 2); cbv[u] = ld16c(rmod, (4 * 640 + c * 8) * 2);
    }
    RD.pre<2, 3>(raD);
    if (b == 119 && wid == 7) {
      bool ok = false;
      for (unsigned spins = 0; spins < SPIN_CAP && !ok; ++spins) {
        bool all = true;
#pragma unroll
        for (int k = 0; k < 4; ++k) all &= __hip_atomic_load((gu32*)sync + SY_STARTED + lane + 64 * k, RLX_AGENT) == tag;
        ok = __all(all);
      }
      if (lane == 0) __hip_atomic_store((gu32*)sync, tag, RLX_AGENT);
    }
    await_flags<16>(c1, tag);
#pragma unroll
    for (int k = 0; k < 4; ++k) p[0][k] = ld16c(rp, pblk[0] + k * 256);
    if (two) {
#pragma unroll
      for (int k = 0; k < 4; ++k) p[1][k] = ld16c(rp, pblk[1] + k * 256);
    }
    float v[2][8]; float s = 0; u32x4 x1v[2];
    auto resid = [&](auto uc) __attribute__((always_inline)) {
      constexpr int u = uc.value;
#pragma unroll
      for (int e = 0; e < 4; ++e) {
        const float a = (bf_lo(p[u][0][e]) + bf_lo(p[u][1][e])) + (bf_lo(p[u][2][e]) + bf_lo(p[u][3][e])), b = (bf_hi(p[u][0][e]) + bf_hi(p[u][1][e])) + (bf_hi(p[u][2][e]) + bf_hi(p[u][3][e]));
        x1v[u][e] = cvtpk(__builtin_fmaf(bf_lo(gv[u][e]), a, bf_lo(xv[u][e])), __builtin_fmaf(bf_hi(gv[u][e]), b, bf_hi(xv[u][e])));
        v[u][2 * e] = bf_lo(x1v[u][e]); v[u][2 * e + 1] = bf_hi(x1v[u][e]); s += v[u][2 * e] + v[u][2 * e + 1];
      }
    };
    resid(IC<0>{});
    if (two) resid(IC<1>{});
    s = xor_add<32>(xor_add<16>(xor_add<8>(s)));
    if (lane < 8) red[wid * 8 + r] = s;
    lds_barrier();
    float tot = 0;
#pragma unroll
    for (int i = 0; i < 8; ++i) tot += red[i * 8 + r];
    const float mean = tot * (1.f / DM); float q = 0;
#pragma unroll
    for (int e = 0; e < 8; ++e) { v[0][e] -= mean; q += v[0][e] * v[0][e]; }
    if (two) {
#pragma unroll
      for (int e = 0; e < 8; ++e) { v[1][e] -= mean; q += v[1][e] * v[1][e]; }
    }
    q = xor_add<32>(xor_add<16>(xor_add<8>(q)));
    if (lane < 8) red[64 + wid * 8 + r] = q;
    lds_barrier();
    float qt = 0;
#pragma unroll
    for (int i = 0; i < 8; ++i) qt += red[64 + i * 8 + r];
    const float rstd = __builtin_amdgcn_rsqf(qt * (1.f / DM) + 1e-5f);
    auto norm = [&](auto uc) __attribute__((always_inline)) {
      constexpr int u = uc.value;
      u32x4 hv;
#pragma unroll
      for (int e = 0; e < 4; ++e)
        hv[e] = cvtpk(__builtin_fmaf(v[u][2 * e] * rstd, bf_lo(cbv[u][e]), bf_lo(sbv[u][e])), __builtin_fmaf(v[u][2 * e + 1] * rstd, bf_hi(cbv[u][e]), bf_hi(sbv[u][e])));
      st16(rh2, blk[u], hv); st16(rx1, blk[u], x1v[u]);
    };
    norm(IC<0>{});
    if (two) norm(IC<1>{});
  }
  publish(c2 + gi, tag);
  const int jE = gi & 7, rtE = rt * 2 + (gi >> 3);
  const __amdgpu_buffer_rsrc_t raE = __builtin_amdgcn_make_buffer_rsrc((void*)(w2t + (size_t)(5 * jE) * 216 * 128), 0, 5 * 216 * 256, 0x00020000);
  RingA<2, 5, 54, 216, 4> RE; RE.init(lds0, wid, lane);
  {
    const int qb = wid & 3, nt0 = wid < 4 ? 0 : 4;
    const __amdgpu_buffer_rsrc_t rb = __builtin_amdgcn_make_buffer_rsrc((void*)(h2 + (size_t)(8 * rt + 2 * qb) * 80 * 128), 0, 2 * 80 * 256, 0x00020000);
    const __amdgpu_buffer_rsrc_t rh = __builtin_amdgcn_make_buffer_rsrc((void*)hid, 0, SQ * FF * 2, 0x00020000);
    const u16* b1 = karg<const u16>(KA_B1);
    u32x2 bv[4];
#pragma unroll
    for (int o = 0; o < 4; ++o) bv[o] = *(const u32x2*)(b1 + (nt0 + o < nft ? (7 * gi + nt0 + o) * 16 + 4 * (lane >> 4) : 0));
    f32x4 acc[7][2] = {};
    await_flags<16>(c2, tag);
    ring_gemm<4, 7, 20, 80, 80, 3, 5, 10>(acc, RD, raD, rb, lds, wid, lane, [&](auto ic) __attribute__((always_inline)) { RE.template issue<ic.value / 5, ic.value % 5>(raE); });
    pair_reduce<7>(acc, lds + 98304, wid, lane);
    const unsigned vo = (((8 * rt + 2 * qb + ((lane >> 4) & 1)) * 216 + (7 * gi + nt0) * 2 + (lane >> 5)) * 16 + (lane & 15)) * 16;
    auto outp = [&](auto ntc, auto oc) __attribute__((always_inline)) {
      constexpr int nt = ntc.value, o = oc.value;
      if (nt < nft) {
        const float b0 = bf_lo(bv[o][0]), b1_ = bf_hi(bv[o][0]), b2_ = bf_lo(bv[o][1]), b3 = bf_hi(bv[o][1]);
        u32x2 w[2];
#pragma unroll
        for (int q = 0; q < 2; ++q) {
          const float v0 = rbf(__builtin_fmaxf(rbf(acc[nt][q][0]) + b0, 0.f)), v1 = rbf(__builtin_fmaxf(rbf(acc[nt][q][1]) + b1_, 0.f));
          const float v2 = rbf(__builtin_fmaxf(rbf(acc[nt][q][2]) + b2_, 0.f)), v3 = rbf(__builtin_fmaxf(rbf(acc[nt][q][3]) + b3, 0.f));
          w[q] = u32x2{cvtpk(v0 * v0, v1 * v1), cvtpk(v2 * v2, v3 * v3)};
        }
        st16(rh, vo + o * 2 * 256, pair16(w[0], w[1]));
      }
    };
    if (wid < 4) { asm volatile("; lo-out" ::: "memory"); static_for<4>(LAMBDA(c) { outp(c, c); }); asm volatile("; lo-end" ::: "memory"); }
    else { asm volatile("; hi-out" ::: "memory"); static_for<3>(LAMBDA(c) { outp(IC<4 + c.value>{}, c); }); asm volatile("; hi-end" ::: "memory"); }
  }
  publish(c3 + gi, tag);
  {
    const int qb = wid & 1;
    const __amdgpu_buffer_rsrc_t rb = __builtin_amdgcn_make_buffer_rsrc((void*)(hid + (size_t)(4 * rtE + 2 * qb) * 216 * 128), 0, 2 * 216 * 256, 0x00020000);
    const __amdgpu_buffer_rsrc_t rx1 = __builtin_amdgcn_make_buffer_rsrc((void*)x1, 0, SQ * DM * 2, 0x00020000);
    const u16* b2 = karg<const u16>(KA_B2);
    u16* out = karg<u16>(KA_OUT);
    u32x4 xv[2], gv[2], bv[2];
#pragma unroll
    for (int it = 0; it < 2; ++it) {
      const int item = tid + it * 512, q = item & 63, c = item < 640 ? item >> 6 : 0, row = rtE * 64 + q;
      xv[it] = ld16c(rx1, (((row >> 4) * 80 + jE * 10 + c) * 16 + (row & 15)) * 16);
      gv[it] = ld16c(rmod, (5 * 640 + jE * 80 + c * 8) * 2); bv[it] = *(const u32x4*)(b2 + jE * 80 + c * 8);
    }
    f32x4 acc[5][2] = {};
    RE.pre<2, 3>(raE);
    await_flags<16>(c3, tag);
    ring_gemm<2, 5, 54, 216, 216, 3, 4, 0>(acc, RE, raE, rb, lds, wid, lane, [&](auto) {});
    f32x4* red = (f32x4*)lds;
    constexpr int HI = (3 * 40960 - 4 * 10240) / 16;
    {
      f32x4* mine = red + (wid * 10) * 64 + (wid >= 4 ? HI : 0) + lane;
#pragma unroll
      for (int n = 0; n < 5; ++n) { mine[(n * 2) * 64] = acc[n][0]; mine[(n * 2 + 1) * 64] = acc[n][1]; }
    }
    lds_barrier();
#pragma unroll
    for (int it = 0; it < 2; ++it) {
      const int item = tid + it * 512;
      if (item < 640) {
        const int q = item & 63, c = item >> 6, n = c >> 1, l0 = (q & 15) + 32 * (c & 1), base = (((q >> 5) * 5 + n) * 2 + ((q >> 4) & 1)) * 64 + l0;
        f32x4 lo = {0, 0, 0, 0}, hi = {0, 0, 0, 0};
#pragma unroll
        for (int w = 0; w < 4; ++w) { const int o_ = base + w * 2 * 5 * 2 * 64 + (w >= 2 ? HI : 0); lo += red[o_]; hi += red[o_ + 16]; }
        const float v[8] = {lo[0], lo[1], lo[2], lo[3], hi[0], hi[1], hi[2], hi[3]};
        u32x4 o;
#pragma unroll
        for (int e = 0; e < 4; ++e) {
          const float a = bf_lo(xv[it][e]) + rbf(bf_lo(gv[it][e]) * rbf(rbf(v[2 * e]) + bf_lo(bv[it][e]))), b = bf_hi(xv[it][e]) + rbf(bf_hi(gv[it][e]) * rbf(rbf(v[2 * e + 1]) + bf_hi(bv[it][e])));
          o[e] = cvtpk(a, b);
        }
        *(u32x4*)(out + (size_t)(rtE * 64 + q) * DM + jE * 80 + c * 8) = o;
      }
    }
  }
}

extern "C" void kernel_launch(void* const* d_in, const int* in_sizes, int n_in, void* d_out, int out_size, void* d_ws, size_t ws_size, hipStream_t stream) {
  char* ws = (char*)d_ws;
  (void)hipFuncSetAttribute((const void*)k_block, hipFuncAttributeMaxDynamicSharedMemorySize, 163840);
  k_block<<<256, 512, 163840, stream>>>((const u16*)d_in[0], (const u16*)d_in[1], (const u16*)d_in[2], (const u16*)d_in[3], (const u16*)d_in[4], (const u16*)d_in[6], (const u16*)d_in[7],
                                        (const u16*)d_in[8], (const u16*)d_in[9], (const u16*)d_in[10], (const u16*)d_in[11], (u16*)d_out, ws);
}
```

```cpp
#include <hip/hip_runtime.h>
#include <stdint.h>
#include <utility>

typedef unsigned short u16;
typedef short bf16x8 __attribute__((ext_vector_type(8)));
typedef short s16x4 __attribute__((ext_vector_type(4)));
typedef float f32x16 __attribute__((ext_vector_type(16)));
typedef float f32x4 __attribute__((ext_vector_type(4)));
typedef float f32x2 __attribute__((ext_vector_type(2)));
typedef __bf16 bf16x2 __attribute__((ext_vector_type(2)));
typedef unsigned u32x4 __attribute__((ext_vector_type(4)));
typedef unsigned u32x2 __attribute__((ext_vector_type(2)));
#define DI __device__ __forceinline__
#define GAS __attribute__((address_space(1)))
#define LAS __attribute__((address_space(3)))
#define SBAR() __builtin_amdgcn_sched_barrier(0)
#define PIN(x) asm volatile("" : "+v"(x))

constexpr int SQ = 2048, DM = 640, NH = 32, DH = 128, SK = 2048, WIN = 512, FF = 1728;
constexpr size_t WS_MOD = 0;
constexpr size_t WS_SYNC = 32768;
constexpr size_t WS_H = 524288;
constexpr size_t WS_O = WS_H + (size_t)SQ * DM * 2;
constexpr size_t WS_WOT = WS_O + (size_t)SQ * 4096 * 2;
constexpr size_t WS_W1T = WS_WOT + (size_t)4096 * DM * 2;
constexpr size_t WS_W2T = WS_W1T + (size_t)DM * FF * 2;
constexpr size_t WS_X1 = WS_W2T + (size_t)DM * FF * 2 + 65536;
constexpr size_t WS_HID = WS_X1 + (size_t)SQ * DM * 2;
constexpr size_t WS_H2 = WS_HID + (size_t)SQ * FF * 2 + 65536;
constexpr size_t WS_P = WS_H2 + (size_t)SQ * DM * 2;
constexpr size_t WS_END = WS_P + (size_t)4 * SQ * DM * 2;

template <int... I, class F> DI void static_for_impl(std::integer_sequence<int, I...>, F&& f) { (f(std::integral_constant<int, I>{}), ...); }
template <int N, class F> DI void static_for(F&& f) { static_for_impl(std::make_integer_sequence<int, N>{}, f); }
#define LAMBDA(ic) [&](auto ic) __attribute__((always_inline))

DI float bf_lo(unsigned w) { return __uint_as_float(w << 16); }
DI float bf_hi(unsigned w) { return __uint_as_float(w & 0xffff0000u); }
DI float bf2f(u16 v) { return __uint_as_float((unsigned)v << 16); }
DI unsigned cvtpk(float lo, float hi) { f32x2 v = {lo, hi}; bf16x2 b = __builtin_convertvector(v, bf16x2); return __builtin_bit_cast(unsigned, b); }
DI u16 f2bf(float x) { return (u16)(cvtpk(x, 0.f) & 0xffffu); }
DI float rbf(float x) { return bf_lo(cvtpk(x, 0.f)); }
DI f32x16 mfma32(bf16x8 a, bf16x8 b, f32x16 c) { return __builtin_amdgcn_mfma_f32_32x32x16_bf16(a, b, c, 0, 0, 0); }
DI f32x4 mfma16(bf16x8 a, bf16x8 b, f32x4 c) { return __builtin_amdgcn_mfma_f32_16x16x32_bf16(a, b, c, 0, 0, 0); }
DI float swap_max(float m) { auto r = __builtin_amdgcn_permlane32_swap(__float_as_uint(m), __float_as_uint(m), false, false); return __builtin_fmaxf(__uint_as_float(r[0]), __uint_as_float(r[1])); }
DI float swap_add(float m) { auto r = __builtin_amdgcn_permlane32_swap(__float_as_uint(m), __float_as_uint(m), false, false); return __uint_as_float(r[0]) + __uint_as_float(r[1]); }

DI void lds_barrier() { asm volatile("s_waitcnt lgkmcnt(0)\n\ts_barrier" ::: "memory"); }
template <int CTRL> DI float dpp_mov(float v) { return __builtin_bit_cast(float, __builtin_amdgcn_update_dpp(0, __builtin_bit_cast(int, v), CTRL, 0xf, 0xf, true)); }
template <int M> DI float xor_add(float v) {
  if constexpr (M == 1) return v + dpp_mov<0xB1>(v);
  else if constexpr (M == 2) return v + dpp_mov<0x4E>(v);
  else if constexpr (M == 4) return v + dpp_mov<0x141>(dpp_mov<0x1B>(v));
  else if constexpr (M == 8) return v + dpp_mov<0x128>(v);
  else if constexpr (M == 16) { const auto r = __builtin_amdgcn_permlane16_swap(__float_as_uint(v), __float_as_uint(v), false, false); return __uint_as_float(r[0]) + __uint_as_float(r[1]); }
  else { const auto r = __builtin_amdgcn_permlane32_swap(__float_as_uint(v), __float_as_uint(v), false, false); return __uint_as_float(r[0]) + __uint_as_float(r[1]); }
}
typedef GAS unsigned gu32;
typedef GAS unsigned long long gu64;
#define RLX_AGENT __ATOMIC_RELAXED, __HIP_MEMORY_SCOPE_AGENT
constexpr unsigned SPIN_CAP = 1u << 18;
constexpr int SC1 = 16;
constexpr int FS = 32;
constexpr int SY_STARTED = 64, SY_RF = 512, SY_GRAN = 1024  , SY_HF = 36864, SY_OF = SY_HF + 256 * FS, SY_CF = SY_OF + 256 * FS  ;
#define NREP 16
#define GR_SLEEP 4
DI void st16(__amdgpu_buffer_rsrc_t r, unsigned voff, u32x4 v) { __builtin_amdgcn_raw_buffer_store_b128(v, r, voff, 0, SC1); }
DI u32x4 ld16c(__amdgpu_buffer_rsrc_t r, unsigned voff) { return __builtin_amdgcn_raw_buffer_load_b128(r, voff, 0, SC1); }
DI void publish(unsigned* flag, unsigned tag) {
  asm volatile("s_waitcnt vmcnt(0)" ::: "memory");
  __syncthreads();
  if (threadIdx.x == 0) __hip_atomic_store((gu32*)flag, tag, RLX_AGENT);
}
template <int N> DI void await_flags(const unsigned* flags, unsigned tag) {
  if (threadIdx.x < 64) {
    gu32* p = (gu32*)flags + (threadIdx.x < N ? threadIdx.x : 0) * FS;
    unsigned a = __hip_atomic_load(p, RLX_AGENT); __builtin_amdgcn_s_sleep(2);
    unsigned b = __hip_atomic_load(p, RLX_AGENT);
    for (unsigned spins = 0; spins < SPIN_CAP; ++spins) {
      if (__all(a == tag)) break;
      a = __hip_atomic_load(p, RLX_AGENT); __builtin_amdgcn_s_sleep(2);
      if (__all(b == tag)) break;
      b = __hip_atomic_load(p, RLX_AGENT); __builtin_amdgcn_s_sleep(2);
    }
  }
  asm volatile("" ::: "memory");
  __syncthreads();
}
template <class T> DI T* karg(int i) {
  const __attribute__((address_space(4))) char* p = (const __attribute__((address_space(4))) char*)__builtin_amdgcn_kernarg_segment_ptr();
  asm volatile("" : "+s"(p));
  return *(T* const __attribute__((address_space(4)))*)(p + 8 * i);
}
enum { KA_X = 0, KA_KC, KA_VC, KA_WQ, KA_WO, KA_W1, KA_B1, KA_W2, KA_B2, KA_C, KA_WMOD, KA_OUT, KA_WS };

DI u32x4 pair16(u32x2 x, u32x2 y) {
  const auto a = __builtin_amdgcn_permlane16_swap(x[0], y[0], false, false), b = __builtin_amdgcn_permlane16_swap(x[1], y[1], false, false);
  return u32x4{a[0], b[0], a[1], b[1]};
}
#define A_NS 8
#define A_PD 5
#define O_AUX 16
constexpr int NS = A_NS, SLOT = 16384;
constexpr float QSCALE = 0.088388347648318440f * 1.4426950408889634f;
constexpr float PBIAS = 96.f;
#define PLIM_V 1073741824.f
constexpr float PLIM = PLIM_V;
constexpr int tile_K(int t) { return t == 0 ? 10 : 9 + 2 * t; }
constexpr int tile_V(int t) { return t == 7 ? 25 : 12 + 2 * t; }
constexpr int slot_of(int idx) { return (idx % NS) * SLOT; }
constexpr int lastuse(int idx) { return idx < 10 ? idx : (idx == 10 ? 10 : (idx == 25 ? 18 : ((idx & 1) ? 10 + (idx - 9) / 2 : 11 + (idx - 12) / 2))); }
constexpr int need_bar(int idx) {
  if (idx < 10) return idx == 0 ? 0 : idx - 1;
  if (idx == 10) return 9;
  if (idx == 25) return 17;
  return (idx & 1) ? 10 + (idx - 9) / 2 - 1 : 11 + (idx - 12) / 2;
}
constexpr int rt_step(int u) { return u == 0 ? 9 : (u == 1 ? 10 : 12); }
constexpr int rt_proc(int u) { return u == 0 ? 11 : (u == 1 ? 12 : 14); }
constexpr int rt_slot(int u) { return (u == 1 ? NS + 1 : NS) * SLOT; }
struct Sched { int npro; int pro[16]; int nops[19]; int op[19][16]; int wait[19]; int rt_done; };
constexpr Sched make_sched() {
  Sched p{}; int pos = 0; int tile_end[26] = {}; int h_end[10] = {}; int u_end[3] = {}; int next_tile = 3;
  auto pro_h = [&](int i) { for (int k = 0; k < 4; ++k) p.pro[p.npro++] = 100 + 4 * i + k; pos += 4; h_end[i] = pos; };
  pro_h(0); pro_h(1);
  tile_end[0] = tile_end[1] = tile_end[2] = 0; int last_rt_store = 1 << 20; p.rt_done = -1;
  for (int g = 0; g < 19; ++g) {
    int need = 0;
    for (int idx = 0; idx < next_tile; ++idx) if (need_bar(idx) <= g && tile_end[idx] > need) need = tile_end[idx];
    if (g < 10 && h_end[g] > need) need = h_end[g];
    for (int u = 0; u < 3; ++u) if (rt_proc(u) <= g && u_end[u] > need) need = u_end[u];
    p.wait[g] = pos - need;
    if (p.rt_done < 0 && need >= last_rt_store) p.rt_done = g;
    int n = 0;
    for (int u = 0; u < 3; ++u) if (g == rt_proc(u)) { p.op[g][n++] = 300 + 2 * u; p.op[g][n++] = 301 + 2 * u; }
    if (g == 0) { for (int k = 0; k < 4; ++k) p.op[g][n++] = 100 + 4 * 2 + k; pos += 4; h_end[2] = pos; }
    const int maxt = g < 7 ? 1 : 2;
    for (int c = 0; c < maxt && next_tile < 26; ++c) {
      if (next_tile >= NS && lastuse(next_tile - NS) >= g) break;
      p.op[g][n++] = 2 * next_tile; p.op[g][n++] = 2 * next_tile + 1; pos += 2; tile_end[next_tile] = pos; ++next_tile;
    }
    if (g + 3 < 10) { for (int k = 0; k < 4; ++k) p.op[g][n++] = 100 + 4 * (g + 3) + k; pos += 4; h_end[g + 3] = pos; }
    for (int u = 0; u < 3; ++u) if (g == rt_step(u)) { p.op[g][n++] = 200 + 2 * u; p.op[g][n++] = 201 + 2 * u; pos += 2; u_end[u] = pos; }
    for (int u = 0; u < 3; ++u) if (g == rt_proc(u)) { p.op[g][n++] = 310 + 2 * u; p.op[g][n++] = 311 + 2 * u; pos += 2; if (u == 2) last_rt_store = pos; }
    p.nops[g] = n;
  }
  for (int idx = 3; idx < 26; ++idx) if (tile_end[idx] == 0) p.wait[0] = -1;
  if (p.rt_done < 0) p.wait[0] = -1;
  return p;
}
constexpr Sched SCH = make_sched();
static_assert(SCH.wait[0] >= 0, "schedule incomplete");
constexpr bool sched_ok() {
  int step_of[26] = {}; for (int i = 0; i < 26; ++i) step_of[i] = -1;
  for (int g = 0; g < 19; ++g) for (int j = 0; j < SCH.nops[g]; ++j) if (SCH.op[g][j] < 100) step_of[SCH.op[g][j] >> 1] = g;
  for (int i = 3; i < 26; ++i) if (step_of[i] >= need_bar(i)) return false;
  return true;
}
static_assert(sched_ok(), "a tile is issued too late");

DI void dma16(unsigned ldsaddr, unsigned voff, __amdgpu_buffer_rsrc_t srd, unsigned soff) {
  asm volatile("s_mov_b32 m0, %0\n\ts_nop 0\n\tbuffer_load_dwordx4 %1, %2, %3 offen lds" ::"s"(ldsaddr), "v"(voff), "s"(srd), "s"(soff) : "memory");
}
template <int OFF> DI void bload16(bf16x8& dst, unsigned voff, __amdgpu_buffer_rsrc_t srd, unsigned soff) {
  asm volatile("buffer_load_dwordx4 %0, %1, %2, %3 offen offset:%4" : "=v"(dst) : "v"(voff), "s"(srd), "s"(soff), "i"(OFF) : "memory");
}
template <int OFF> DI void bload16c(bf16x8& dst, unsigned voff, __amdgpu_buffer_rsrc_t srd, unsigned soff) {
  asm volatile("buffer_load_dwordx4 %0, %1, %2, %3 offen offset:%4 sc1" : "=v"(dst) : "v"(voff), "s"(srd), "s"(soff), "i"(OFF) : "memory");
}
DI void dma16c(unsigned ldsaddr, unsigned voff, __amdgpu_buffer_rsrc_t srd, unsigned soff) {
  asm volatile("s_mov_b32 m0, %0\n\ts_nop 0\n\tbuffer_load_dwordx4 %1, %2, %3 offen sc1 lds" ::"s"(ldsaddr), "v"(voff), "s"(srd), "s"(soff) : "memory");
}
typedef LAS const char* lds_cptr;
typedef short v4i16_t __attribute__((ext_vector_type(4)));
DI s16x4 vtr(lds_cptr p) { return __builtin_bit_cast(s16x4, __builtin_amdgcn_ds_read_tr16_b64_v4i16((LAS v4i16_t*)p)); }
DI bf16x8 trfrag(lds_cptr p0, lds_cptr p1) { const s16x4 lo = vtr(p0), hi = vtr(p1); return __builtin_shufflevector(lo, hi, 0, 1, 2, 3, 4, 5, 6, 7); }
DI bf16x8 rowfrag(lds_cptr p) { return *(LAS const bf16x8*)p; }
DI float max3(float a, float b, float c) { return __builtin_fmaxf(__builtin_fmaxf(a, b), c); }

template <int N> DI void wait_bar4(bf16x8 (&b)[4]) {
  asm volatile("s_waitcnt vmcnt(%4)\n\ts_barrier" : "+v"(b[0]), "+v"(b[1]), "+v"(b[2]), "+v"(b[3]) : "i"(N) : "memory");
}
template <int N> DI void wait_bar() { asm volatile("s_waitcnt vmcnt(%0)\n\ts_barrier" ::"i"(N) : "memory"); }
template <int V> using IC = std::integral_constant<int, V>;

template <int N> DI void wait_bar4b(bf16x8 (&b)[2][2]) {
  asm volatile("s_waitcnt vmcnt(%4)\n\ts_barrier" : "+v"(b[0][0]), "+v"(b[0][1]), "+v"(b[1][0]), "+v"(b[1][1]) : "i"(N) : "memory");
}
template <int NQB, int NT, int KS> struct Ring {
  static constexpr int KSP = 8 / NQB, SK = 2 * KSP, NSTG = (KS + SK - 1) / SK, NP = SK * NT, PA = (NP + 7) / 8, SB = PA * 8 * 1024, FR = 2 * NT;
};
template <int NQB, int NT, int KS, int KC_A, int NSLOT> struct RingA {
  using R = Ring<NQB, NT, KS>;
  static constexpr int NS_ = NSLOT;
  unsigned voff[R::PA]; unsigned ldsw;
  DI void init(unsigned lds0, int wid, int lane) {
    ldsw = lds0 + wid * 1024;
#pragma unroll
    for (int i = 0; i < R::PA; ++i) { const int p = i * 8 + wid, ks = p / NT, nt = p - ks * NT; voff[i] = p < R::NP ? (unsigned)(nt * KC_A * 256 + ks * 1024 + lane * 16) : 0x7ff00000u; }
  }
  template <int S, int I> DI void issue(__amdgpu_buffer_rsrc_t ra) const { dma16c(ldsw + ((S % NSLOT) * R::SB + I * 8192), voff[I], ra, S * R::SK * 1024); }
  template <int S0, int S1> DI void pre(__amdgpu_buffer_rsrc_t ra) const { static_for<S1 - S0>(LAMBDA(sc) { static_for<R::PA>(LAMBDA(ic) { this->template issue<S0 + sc.value, ic.value>(ra); }); }); }
};
template <int NQB, int NT, int KS, int KC_A, int KC_B, int NPRE, int NSLOT, int NHOOK, class HOOK>
DI void ring_gemm(f32x4 (&acc)[NT][2], const RingA<NQB, NT, KS, KC_A, NSLOT>& RA, __amdgpu_buffer_rsrc_t ra, __amdgpu_buffer_rsrc_t rb, const char* lds, int wid, int lane, HOOK&& hook) {
  using R = Ring<NQB, NT, KS>;
  constexpr int SK = R::SK, NSTG = R::NSTG, PA = R::PA, FR = R::FR, PD = 5;
  static_assert(NSTG >= 3 && FR > PD + 1, "");
  const int kq = wid / NQB;
  bf16x8 B[3][2][2];
  const unsigned voffB = lane * 16 + 2 * kq * 1024;
  auto issueA1 = [&](auto sc, auto ic) __attribute__((always_inline)) { RA.template issue<sc.value, ic.value>(ra); };
  auto issueB1 = [&](auto sc, auto ic) __attribute__((always_inline)) {
    constexpr int s = sc.value, i = ic.value, ks = i >> 1, q = i & 1;
    bload16c<0>(B[s % 3][ks][q], voffB, rb, q * KC_B * 256 + (s * SK + ks) * 1024);
  };
  if constexpr (NPRE == 0) static_for<PA>(LAMBDA(ic) { issueA1(IC<0>{}, ic); });
  static_for<4>(LAMBDA(ic) { issueB1(IC<0>{}, ic); });
  if constexpr (NPRE > 1) { static_for<4>(LAMBDA(ic) { issueB1(IC<1>{}, ic); }); wait_bar4b<4>(B[0]); } else wait_bar4b<0>(B[0]);
  const LAS bf16x8* pl = (const LAS bf16x8*)lds + lane + 2 * kq * NT * 64;
  bf16x8 A[PD + 1];
  auto rd = [&](auto mc) __attribute__((always_inline)) { constexpr int M = mc.value, s = M / FR, m = M % FR; A[M % (PD + 1)] = pl[((s % NSLOT) * PA * 8 + m) * 64]; };
  static_for<PD>(LAMBDA(jc) { rd(jc); });
  SBAR();
  static_for<NSTG>(LAMBDA(sc) {
    constexpr int s = sc.value;
    constexpr int n0 = (NPRE <= 1 && s == 0) ? PA + 4 : 0;
    constexpr int sa = s + 2;
    constexpr int nA = (sa < NSTG && sa >= NPRE) ? PA : 0, nB = (s + 2 < NSTG) ? 4 : 0, NI = n0 + nA + nB, NGI = FR - PD - 1;
    constexpr bool full = (s + 1) * SK <= KS;
    static_for<FR>(LAMBDA(mc) {
      constexpr int m = mc.value, M = s * FR + m, ks = m / NT, nt = m % NT;
      if constexpr (m == FR - PD && s + 1 < NSTG) wait_bar4b<nA + nB>(B[(s + 1) % 3]);
      if constexpr (M + PD < NSTG * FR) rd(IC<M + PD>{});
      if constexpr (m < NGI) {
        constexpr int i0 = (m * NI + NGI - 1) / NGI, i1 = ((m + 1) * NI + NGI - 1) / NGI;
        static_for<i1 - i0>(LAMBDA(uc) {
          constexpr int i = i0 + uc.value;
          if constexpr (i < n0) { if constexpr (i < PA) issueA1(IC<1>{}, IC<i>{}); else issueB1(IC<1>{}, IC<i - PA>{}); }
          else if constexpr (i - n0 < nA) issueA1(IC<sa>{}, IC<i - n0>{});
          else issueB1(IC<s + 2>{}, IC<i - n0 - nA>{});
        });
      }
      if constexpr (NHOOK > 0 && M >= (NSTG - 1) * FR - PD) {
        constexpr int j = M - ((NSTG - 1) * FR - PD), HG = FR + PD - 2, i0 = (j * NHOOK + HG - 1) / HG, i1 = ((j + 1) * NHOOK + HG - 1) / HG;
        if constexpr (j < HG) static_for<i1 - i0>(LAMBDA(uc) { hook(IC<i0 + uc.value>{}); });
      }
      if (full || s * SK + 2 * kq + ks < KS) { acc[nt][0] = mfma16(A[M % (PD + 1)], B[s % 3][ks][0], acc[nt][0]); acc[nt][1] = mfma16(A[M % (PD + 1)], B[s % 3][ks][1], acc[nt][1]); }
      SBAR();
    });
  });
}
template <int NT> DI void pair_reduce(f32x4 (&acc)[NT][2], char* lds, int wid, int lane) {
  constexpr int H0 = (NT + 1) / 2;
  f32x4* red = (f32x4*)lds + (size_t)wid * H0 * 2 * 64 + lane;
  const f32x4* in = (const f32x4*)lds + (size_t)(wid ^ 4) * H0 * 2 * 64 + lane;
  lds_barrier();
  if (wid < 4) {
    asm volatile("; lo-send" ::: "memory");
#pragma unroll
    for (int nt = H0; nt < NT; ++nt) { red[((nt - H0) * 2) * 64] = acc[nt][0]; red[((nt - H0) * 2 + 1) * 64] = acc[nt][1]; }
    asm volatile("; lo-sent" ::: "memory");
  } else {
    asm volatile("; hi-send" ::: "memory");
#pragma unroll
    for (int nt = 0; nt < H0; ++nt) { red[(nt * 2) * 64] = acc[nt][0]; red[(nt * 2 + 1) * 64] = acc[nt][1]; }
    asm volatile("; hi-sent" ::: "memory");
  }
  lds_barrier();
  if (wid < 4) {
    asm volatile("; lo-recv" ::: "memory");
#pragma unroll
    for (int nt = 0; nt < H0; ++nt) { acc[nt][0] += in[(nt * 2) * 64]; acc[nt][1] += in[(nt * 2 + 1) * 64]; PIN(acc[nt][0]); PIN(acc[nt][1]); }
    asm volatile("; lo-done" ::: "memory");
  } else {
    asm volatile("; hi-recv" ::: "memory");
#pragma unroll
    for (int nt = H0; nt < NT; ++nt) { acc[nt][0] += in[((nt - H0) * 2) * 64]; acc[nt][1] += in[((nt - H0) * 2 + 1) * 64]; PIN(acc[nt][0]); PIN(acc[nt][1]); }
    asm volatile("; hi-done" ::: "memory");
  }
}

__global__ __launch_bounds__(512, 2) void k_block(const u16* a_x, const u16* a_kc, const u16* a_vc, const u16* a_wq, const u16* a_wo, const u16* a_w1, const u16* a_b1, const u16* a_w2,
                                                   const u16* a_b2, const u16* a_c, const u16* a_wmod, u16* a_out, char* a_ws) {
  extern __shared__ __attribute__((aligned(16))) char lds[];
  const int tid = threadIdx.x, lane = tid & 63, r32 = lane & 31, hi = lane >> 5;
  const int wid = __builtin_amdgcn_readfirstlane(tid >> 6);
  const int b = blockIdx.x, xcd = b & 7, idx = b >> 3;
  const int head = (xcd & 3) * 8 + (idx & 7), qt = (xcd >> 2) * 4 + (idx >> 3);
  const unsigned lds0 = (unsigned)(uintptr_t)lds;
  char* const ws = karg<char>(KA_WS);
  unsigned* const sync = (unsigned*)(ws + WS_SYNC);
  const __amdgpu_buffer_rsrc_t srdW = __builtin_amdgcn_make_buffer_rsrc((void*)(karg<const u16>(KA_WQ) + head * DH), 0, (DM - 1) * 8192 + 256, 0x00020000);
  const int dr = (lane >> 2) & 7, dx = lane & 3, dc = lane >> 5, dz = (2 * wid + (dr >> 2)) & 3;
  const unsigned voffW = (unsigned)((8 * wid + dr) * 8192 + 64 * dc + 16 * (dx ^ dz));
  const unsigned ldsw = lds0 + 2048 * wid;
  unsigned tag;
  {
    float* sm = (float*)(lds + NS * SLOT);
    const u16* x = karg<const u16>(KA_X);
    const int row = b * 8 + wid; const bool act = lane < 40;
    u32x4 w[5]; unsigned cv = 0;
    const int cc = tid & 3, r0 = tid >> 2;
    if (b < 120) {
      const u16* wmod = karg<const u16>(KA_WMOD);
#pragma unroll
      for (int i = 0; i < 5; ++i) w[i] = *(const u32x4*)(wmod + (size_t)(r0 + 128 * i) * 3840 + b * 32 + cc * 8);
      cv = *(const unsigned*)(karg<const u16>(KA_C) + 2 * (tid < 320 ? tid : 0));
    }
    u32x4 xv[2];
    xv[0] = *(const u32x4*)(x + (size_t)row * DM + (act ? lane : 0) * 8); xv[1] = *(const u32x4*)(x + (size_t)row * DM + (act ? lane + 40 : 0) * 8);
    unsigned tg = __hip_atomic_load((gu32*)sync, RLX_AGENT) + 1u;
    if (b < 120 && tid < 320) {
      const float v0 = bf_lo(cv), v1 = bf_hi(cv);
      const unsigned pk = cvtpk(v0 * __builtin_amdgcn_rcpf(1.f + __expf(-v0)), v1 * __builtin_amdgcn_rcpf(1.f + __expf(-v1)));
      *(f32x2*)(sm + 512 + 2 * tid) = f32x2{bf_lo(pk), bf_hi(pk)};
    }
    PIN(tg);
    tag = __builtin_amdgcn_readfirstlane(tg);
    lds_barrier();
    if (tid == 0) __hip_atomic_store((gu32*)sync + SY_STARTED + b, tag, RLX_AGENT);
#pragma unroll
    for (int i = 0; i < 3; ++i) { dma16(ldsw + slot_of(i), voffW, srdW, i * 64 * 8192); dma16(ldsw + slot_of(i) + 1024, voffW, srdW, i * 64 * 8192 + 128); }
    if (b < 120) {
      float a[8] = {};
#pragma unroll
      for (int i = 0; i < 5; ++i) {
        const float sv = sm[512 + r0 + 128 * i];
#pragma unroll
        for (int j = 0; j < 4; ++j) { a[2 * j] += sv * bf_lo(w[i][j]); a[2 * j + 1] += sv * bf_hi(w[i][j]); }
      }
#pragma unroll
      for (int j = 0; j < 8; ++j) a[j] = xor_add<32>(xor_add<16>(xor_add<8>(xor_add<4>(a[j]))));
      if (lane < 4) {
#pragma unroll
        for (int j = 0; j < 8; ++j) sm[wid * 32 + cc * 8 + j] = a[j];
      }
      lds_barrier();
      {
        const int u = tid & 15;
        float lo = 0.f, hi_ = 0.f;
#pragma unroll
        for (int w8 = 0; w8 < 8; ++w8) { lo += sm[w8 * 32 + 2 * u]; hi_ += sm[w8 * 32 + 2 * u + 1]; }
        unsigned pk = cvtpk(lo, hi_);
        if ((b >= 20 && b < 40) || (b >= 80 && b < 100)) pk = cvtpk(1.f + bf_lo(pk), 1.f + bf_hi(pk));
        if (b < 40) { if (tid < 16 * NREP) __hip_atomic_store((gu64*)(sync + SY_GRAN + (tid >> 4) * 2048) + b * 16 + u, ((unsigned long long)tag << 32) | pk, RLX_AGENT); }
        else if (tid < 16) __hip_atomic_store((gu32*)(ws + WS_MOD) + b * 16 + u, pk, RLX_AGENT);
      }
    }
    float f[16]; float s = 0;
#pragma unroll
    for (int j = 0; j < 2; ++j)
#pragma unroll
      for (int e = 0; e < 4; ++e) { f[j * 8 + 2 * e] = act ? bf_lo(xv[j][e]) : 0.f; f[j * 8 + 2 * e + 1] = act ? bf_hi(xv[j][e]) : 0.f; s += f[j * 8 + 2 * e] + f[j * 8 + 2 * e + 1]; }
    s = xor_add<32>(xor_add<16>(xor_add<8>(xor_add<4>(xor_add<2>(xor_add<1>(s))))));
    const float mean = s * (1.f / DM); float q = 0;
#pragma unroll
    for (int e = 0; e < 16; ++e) { const float d = act ? f[e] - mean : 0.f; q += d * d; }
    q = xor_add<32>(xor_add<16>(xor_add<8>(xor_add<4>(xor_add<2>(xor_add<1>(q))))));
    const float mb = rbf(mean), rsd = 1.f / rbf(sqrtf(rbf(rbf(q * (1.f / DM)) + 1e-5f)));
#pragma unroll
    for (int e = 0; e < 16; e += 2) {
      const unsigned d = cvtpk(f[e] - mb, f[e + 1] - mb), n = cvtpk(bf_lo(d) * rsd, bf_hi(d) * rsd);
      f[e] = bf_lo(n); f[e + 1] = bf_hi(n);
    }
    if (wid == 0) {
      const __amdgpu_buffer_rsrc_t rg = __builtin_amdgcn_make_buffer_rsrc((void*)(sync + SY_GRAN + (b % NREP) * 2048), 0, 5120, 0x00020000);
      u32x4 gv[5];
      for (unsigned spins = 0; spins < SPIN_CAP; ++spins) {
        bool ok = true;
#pragma unroll
        for (int k = 0; k < 5; ++k) { gv[k] = ld16c(rg, (lane + 64 * k) * 16); ok &= gv[k][1] == tag && gv[k][3] == tag; }
        if (__all(ok)) break;
        __builtin_amdgcn_s_sleep(GR_SLEEP);
      }
#pragma unroll
      for (int k = 0; k < 5; ++k) *(f32x4*)(sm + 1280 + 4 * (lane + 64 * k)) = f32x4{bf_lo(gv[k][0]), bf_hi(gv[k][0]), bf_lo(gv[k][2]), bf_hi(gv[k][2])};
    }
    lds_barrier();
    u32x4* hs = (u32x4*)(lds + (NS + 1) * SLOT);
    if (act) {
#pragma unroll
      for (int j = 0; j < 2; ++j) {
        const int k0 = (lane + 40 * j) * 8;
        const f32x4 s0 = *(const f32x4*)(sm + 1280 + k0), s1 = *(const f32x4*)(sm + 1284 + k0), c0 = *(const f32x4*)(sm + 1920 + k0), c1 = *(const f32x4*)(sm + 1924 + k0);
        const float sa[8] = {s0[0], s0[1], s0[2], s0[3], s1[0], s1[1], s1[2], s1[3]}, oc[8] = {c0[0], c0[1], c0[2], c0[3], c1[0], c1[1], c1[2], c1[3]};
        u32x4 o;
#pragma unroll
        for (int e = 0; e < 4; ++e) {
          const unsigned m = cvtpk(f[j * 8 + 2 * e] * oc[2 * e], f[j * 8 + 2 * e + 1] * oc[2 * e + 1]);
          o[e] = cvtpk(bf_lo(m) + sa[2 * e], bf_hi(m) + sa[2 * e + 1]);
        }
        hs[(lane + 40 * j) * 8 + wid] = o;
      }
    }
    lds_barrier();
    {
      const __amdgpu_buffer_rsrc_t rh = __builtin_amdgcn_make_buffer_rsrc((void*)(ws + WS_H), 0, SQ * DM * 2, 0x00020000);
      const unsigned base = (unsigned)(((b >> 1) * 80 * 16 + (b & 1) * 8) * 16);
      st16(rh, base + (tid >> 3) * 256 + (tid & 7) * 16, hs[tid]);
      if (tid < 128) st16(rh, base + (64 + (tid >> 3)) * 256 + (tid & 7) * 16, hs[512 + tid]);
    }
    publish(sync + SY_HF + b * FS, tag);
    await_flags<32>(sync + SY_HF + qt * 32 * FS, tag);
  }
  const __amdgpu_buffer_rsrc_t srdK = __builtin_amdgcn_make_buffer_rsrc((void*)(karg<const u16>(KA_KC) + ((size_t)head * SK + (SK - WIN)) * DH), 0, WIN * 256, 0x00020000);
  const __amdgpu_buffer_rsrc_t srdV = __builtin_amdgcn_make_buffer_rsrc((void*)(karg<const u16>(KA_VC) + ((size_t)head * SK + (SK - WIN)) * DH), 0, WIN * 256, 0x00020000);
  const __amdgpu_buffer_rsrc_t srdH = __builtin_amdgcn_make_buffer_rsrc((void*)(ws + WS_H + (size_t)(qt * 16 + 2 * wid) * 80 * 256), 0, 2 * 80 * 256, 0x00020000);
  const unsigned voffKV = (unsigned)((8 * wid + dr) * 256 + 64 * dc + 16 * (dx ^ dz));
  const unsigned voffH = (unsigned)(((lane >> 4) & 1) * 20480 + (lane >> 5) * 256 + (lane & 15) * 16);
  bf16x8 hf[10][4], rtf[2];
  __amdgpu_buffer_rsrc_t rsrc_u[3]; unsigned pitch_u[3], dst_u[3];
  const __amdgpu_buffer_rsrc_t srdT = __builtin_amdgcn_make_buffer_rsrc((void*)(ws + WS_WOT), 0, (int)(WS_W2T - WS_WOT) + DM * FF * 2, 0x00020000);
  auto vmem_op = [&](auto oc) __attribute__((always_inline)) {
    constexpr int op = oc.value;
    if constexpr (op >= 310) {
      constexpr int u = (op - 310) >> 1, ks = op & 1;
      __builtin_amdgcn_raw_buffer_store_b128(__builtin_bit_cast(u32x4, rtf[ks]), srdT, lane * 16 + ks * 1024, dst_u[u], O_AUX);
    } else if constexpr (op >= 300) {
      constexpr int u = (op - 300) >> 1, ks = op & 1;
      const int g4 = lane >> 4, chq = 2 * wid + ((lane & 3) >> 1), q4_ = (lane & 15) >> 2;
      const lds_cptr p0 = (lds_cptr)lds + rt_slot(u) + 2048 * (4 * ks + g4) + 512 * (chq >> 2) + 64 * q4_ + 16 * ((chq & 3) ^ ((2 * g4) & 3)) + 8 * (lane & 1);
      const lds_cptr p1 = (lds_cptr)lds + rt_slot(u) + 2048 * (4 * ks + g4) + 512 * (chq >> 2) + 64 * (4 + q4_) + 16 * ((chq & 3) ^ ((2 * g4 + 1) & 3)) + 8 * (lane & 1);
      rtf[ks] = trfrag(p0, p1);
    } else if constexpr (op >= 200) {
      constexpr int u = (op - 200) >> 1, half = op & 1;
      dma16(ldsw + rt_slot(u) + 1024 * half, (unsigned)(8 * wid + dr) * pitch_u[u] + (unsigned)(64 * dc + 16 * (dx ^ dz)), rsrc_u[u], 128 * half);
    } else if constexpr (op >= 100) { constexpr int i = (op - 100) >> 2, k = (op - 100) & 3; bload16c<512 * k>(hf[i][k], voffH, srdH, i * 2048); }
    else {
      constexpr int i = op >> 1, half = op & 1; constexpr unsigned so = slot_of(i) + 1024 * half;
      if constexpr (i < 10) dma16(ldsw + so, voffW, srdW, i * 64 * 8192 + 128 * half);
      else {
        constexpr bool isK = (i == 10) || (i != 25 && (i & 1)); constexpr int t = i == 10 ? 0 : (i == 25 ? 7 : (isK ? (i - 9) / 2 : (i - 12) / 2));
        dma16(ldsw + so, voffKV, isK ? srdK : srdV, t * 16384 + 128 * half);
      }
    }
  };
  static_for<SCH.npro>(LAMBDA(jc) { vmem_op(IC<SCH.pro[jc.value]>{}); });
  SBAR();
#pragma unroll
  for (int u = 0; u < 3; ++u) {
    const int id = blockIdx.x + 256 * u;
    const u16* src; int pitch, rows, kt, nt, kc, nsub; unsigned dbase;
    if (id < 320) { src = karg<const u16>(KA_WO); pitch = 1280; rows = 4096; kt = id / 5; nt = id - kt * 5; kc = 512; nsub = 40; dbase = 0; }
    else if (id < 455) { const int v = id - 320; src = karg<const u16>(KA_W2); pitch = 1280; rows = 1728; kt = v / 5; nt = v - kt * 5; kc = 216; nsub = 40; dbase = (unsigned)(WS_W2T - WS_WOT); }
    else { const int v = id < 595 ? id - 455 : 0; src = karg<const u16>(KA_W1); pitch = 3456; rows = id < 595 ? 640 : 0; kt = v / 14; nt = v - kt * 14; kc = 80; nsub = id < 595 ? 108 : 0; dbase = (unsigned)(WS_W1T - WS_WOT); }
    const int first = kt * 64 * pitch + nt * 256;
    rsrc_u[u] = __builtin_amdgcn_make_buffer_rsrc((void*)((const char*)src + first), 0, rows ? rows * pitch - first : 0, 0x00020000);
    pitch_u[u] = pitch;
    dst_u[u] = nt * 8 + wid < nsub ? dbase + ((nt * 8 + wid) * kc + kt * 8) * 256 : 0x7ff00000u;
  }
  auto vmem_gap = [&](auto gc, auto jc, auto nc) __attribute__((always_inline)) {
    constexpr int g = gc.value, j = jc.value, n = nc.value, no = SCH.nops[g];
    constexpr int i0 = (j * no + n - 1) / n, i1 = ((j + 1) * no + n - 1) / n;
    static_for<i1 - i0>(LAMBDA(uc) { vmem_op(IC<SCH.op[g][i0 + uc.value]>{}); });
  };

  const int blk = (lane >> 4) & 1, q4 = (lane & 15) >> 2, p4 = lane & 3;
  constexpr int NR = (NS * SLOT + 65535) / 65536;
  const lds_cptr L = (lds_cptr)lds;
  lds_cptr wb[NR][2], vb[NR][2], kb_[NR][2];
#pragma unroll
  for (int r = 0; r < NR; ++r)
#pragma unroll
    for (int t = 0; t < 2; ++t) {
      wb[r][t] = L + 65536 * r + 2048 * hi + 64 * (4 * t + q4) + 16 * ((2 * blk + (p4 & 1)) ^ (2 * hi + t)) + 8 * (p4 >> 1);
      PIN(wb[r][t]);
      vb[r][t] = L + 65536 * r + 64 * (4 * hi + q4) + 16 * ((2 * blk + (p4 & 1)) ^ (2 * t + hi)) + 8 * (p4 >> 1) + 2048 * t;
      kb_[r][t] = L + 65536 * r + 2048 * (r32 >> 3) + 64 * (r32 & 7) + 16 * ((2 * t + hi) ^ ((r32 >> 2) & 3));
    }
  constexpr int PD = A_PD, NG = 416;
  bf16x8 fr[PD + 1];
  auto ldfrag_g = [&](auto Gc) __attribute__((always_inline)) {
    constexpr int G = Gc.value;
    if constexpr (G < NG) {
      if constexpr (G < 160) {
        constexpr int i = G >> 4, ks = (G >> 2) & 3, db = G & 3, o = slot_of(i) + 4096 * ks + 512 * db;
        fr[G % (PD + 1)] = trfrag(wb[o >> 16][0] + (o & 65535), wb[o >> 16][1] + (o & 65535));
      } else {
        constexpr int u = G - 176, t = G < 176 ? 0 : (G >= 400 ? 8 : 1 + u / 32), g = G < 176 ? G - 160 : (G >= 400 ? G - 400 + 16 : u % 32);
        if constexpr (g < 16) {
          constexpr int kb = g >> 3, s = g & 7, o = slot_of(tile_K(t)) + 8192 * kb + 512 * (s >> 1);
          fr[G % (PD + 1)] = rowfrag(kb_[o >> 16][s & 1] + (o & 65535));
        } else {
          constexpr int gg = g - 16, ks = t == 8 ? (gg & 3) : (gg >> 2), db = t == 8 ? (gg >> 2) : (gg & 3), o = slot_of(tile_V(t - 1)) + 4096 * ks + 512 * db;
          fr[G % (PD + 1)] = trfrag(vb[o >> 16][0] + (o & 65535), vb[o >> 16][1] + (o & 65535));
        }
      }
    }
  };
  auto barrier = [&](auto gc) __attribute__((always_inline)) {
    constexpr int g = gc.value;
    if constexpr (g < 10) wait_bar4<SCH.wait[g]>(hf[g]); else wait_bar<SCH.wait[g]>();
    if constexpr (g == SCH.rt_done) { if (tid == 0) __hip_atomic_store((gu32*)sync + SY_RF + b, tag, RLX_AGENT); }
    SBAR();
  };

  f32x16 acc[4] = {};
  static_for<10>(LAMBDA(ic) {
    constexpr int i = ic.value;
    barrier(ic);
    if constexpr (i == 0) { static_for<PD>(LAMBDA(jc) { ldfrag_g(jc); }); SBAR(); }
    static_for<16>(LAMBDA(mc) {
      constexpr int m = mc.value, G = 16 * i + m;
      ldfrag_g(IC<G + PD>{});
      vmem_gap(ic, mc, IC<16>{});
      acc[m & 3] = mfma32(fr[G % (PD + 1)], hf[i][m >> 2], acc[m & 3]);
      SBAR();
    });
  });
  bf16x8 qf[8];
#pragma unroll
  for (int s = 0; s < 8; ++s) {
    u32x4 w;
#pragma unroll
    for (int e = 0; e < 4; ++e) w[e] = cvtpk(acc[s >> 1][8 * (s & 1) + 2 * e] * QSCALE, acc[s >> 1][8 * (s & 1) + 2 * e + 1] * QSCALE);
    qf[s] = __builtin_bit_cast(bf16x8, w);
  }
#pragma unroll
  for (int r = 0; r < NR; ++r)
#pragma unroll
    for (int t = 0; t < 2; ++t) { PIN(vb[r][t]); PIN(kb_[r][t]); }

  const int qrow = qt * 256 + wid * 32 + r32;
  const __amdgpu_buffer_rsrc_t srdO = __builtin_amdgcn_make_buffer_rsrc((void*)(ws + WS_O), 0, SQ * 4096 * 2, 0x00020000);
  const unsigned voffO = (((qrow >> 4) * 512 + head * 16 + hi) * 16 + (qrow & 15)) * 16;
  f32x16 o[4] = {}; f32x16 sA[2], sB[2], negm; u32x4 pw[4];
  float mref, lsum = 0.f; bool bad = false;
  barrier(IC<10>{});
  {
    const f32x16 zero = {};
    static_for<16>(LAMBDA(gc) {
      constexpr int g = gc.value, G = 160 + g;
      ldfrag_g(IC<G + PD>{});
      vmem_gap(IC<10>{}, gc, IC<16>{});
      sA[g >> 3] = mfma32(fr[G % (PD + 1)], qf[g & 7], (g & 7) == 0 ? zero : sA[g >> 3]);
      SBAR();
    });
    float a = max3(sA[0][0], sA[0][1], sA[1][0]), b = max3(sA[0][2], sA[0][3], sA[1][1]); a = max3(a, sA[1][2], sA[1][3]);
#pragma unroll
    for (int r = 4; r < 16; r += 4) { a = max3(a, sA[0][r], sA[0][r + 1]); b = max3(b, sA[0][r + 2], sA[0][r + 3]); a = max3(a, sA[1][r], sA[1][r + 1]); b = max3(b, sA[1][r + 2], sA[1][r + 3]); }
    mref = swap_max(__builtin_fmaxf(a, b)) + PBIAS;
#pragma unroll
    for (int r = 0; r < 16; ++r) negm[r] = -mref;
    PIN(negm);
    sA[0][0] = __builtin_amdgcn_exp2f(sA[0][0] - mref); sA[0][1] = __builtin_amdgcn_exp2f(sA[0][1] - mref);
    SBAR();
  }
  auto step = [&](auto tc, f32x16(&C)[2], f32x16(&P)[2]) __attribute__((always_inline)) {
    constexpr int t = tc.value;
    constexpr int G0 = 176 + 32 * (t - 1), GB = t < 8 ? G0 + 16 : 400;
    barrier(IC<10 + t>{});
    float sacc = 0.f;
    static_for<16>(LAMBDA(gc) {
      constexpr int g = gc.value;
      if constexpr (t < 8) {
        ldfrag_g(IC<G0 + g + PD>{});
        vmem_gap(IC<10 + t>{}, gc, IC<32>{});
        C[g >> 3] = mfma32(fr[(G0 + g) % (PD + 1)], qf[g & 7], (g & 7) == 0 ? negm : C[g >> 3]);
      }
      if constexpr (t == 1 && g < 15) {
        constexpr int e1 = 2 * (g + 1);
        P[e1 >> 4][e1 & 15] = __builtin_amdgcn_exp2f(P[e1 >> 4][e1 & 15] - mref); P[e1 >> 4][(e1 & 15) + 1] = __builtin_amdgcn_exp2f(P[e1 >> 4][(e1 & 15) + 1] - mref);
        PIN(P[e1 >> 4]);
      }
      constexpr int ks = g >> 2, w = g & 3, e = 8 * (ks & 1) + 2 * w;
      pw[ks][w] = cvtpk(P[ks >> 1][e], P[ks >> 1][e + 1]);
      sacc += P[ks >> 1][e]; sacc += P[ks >> 1][e + 1];
      PIN(pw[ks]); PIN(sacc);
      SBAR();
    });
    bad |= !(sacc <= PLIM);
    lsum += sacc;
    if constexpr (t < 8) {
      static_for<16>(LAMBDA(gc) {
        constexpr int g = gc.value;
        ldfrag_g(IC<GB + g + PD>{});
        vmem_gap(IC<10 + t>{}, IC<16 + g>{}, IC<32>{});
        o[g & 3] = mfma32(fr[(GB + g) % (PD + 1)], __builtin_bit_cast(bf16x8, pw[g >> 2]), o[g & 3]);
        C[g >> 3][(2 * g) & 15] = __builtin_amdgcn_exp2f(C[g >> 3][(2 * g) & 15]); C[g >> 3][(2 * g + 1) & 15] = __builtin_amdgcn_exp2f(C[g >> 3][(2 * g + 1) & 15]);
        PIN(C[g >> 3]);
        SBAR();
      });
    }
  };
  auto out_db = [&](auto dc, float inv) __attribute__((always_inline)) {
    constexpr int db = dc.value;
#pragma unroll
    for (int s = 0; s < 2; ++s) {
      u32x4 w;
#pragma unroll
      for (int e = 0; e < 4; ++e) w[e] = cvtpk(o[db][8 * s + 2 * e] * inv, o[db][8 * s + 2 * e + 1] * inv);
      __builtin_amdgcn_raw_buffer_store_b128(w, srdO, voffO + (4 * db + 2 * s) * 256, 0, O_AUX);
    }
  };
  step(IC<1>{}, sB, sA);
  step(IC<2>{}, sA, sB);
  step(IC<3>{}, sB, sA);
  step(IC<4>{}, sA, sB);
  step(IC<5>{}, sB, sA);
  step(IC<6>{}, sA, sB);
  step(IC<7>{}, sB, sA);
  step(IC<8>{}, sA, sB);
  if (__builtin_expect(!__any(bad), 1)) {
    const float inv = 1.f / swap_add(lsum);
    static_for<16>(LAMBDA(gc) {
      constexpr int g = gc.value;
      ldfrag_g(IC<400 + g + PD>{});
      o[g >> 2] = mfma32(fr[(400 + g) % (PD + 1)], __builtin_bit_cast(bf16x8, pw[g & 3]), o[g >> 2]);
      if constexpr (g >= 5 && (g & 3) == 1) out_db(IC<(g >> 2) - 1>{}, inv);
      SBAR();
    });
    out_db(IC<3>{}, inv);
  } else {
    const unsigned reg = (unsigned)((wid == 0 ? 0 : wid + 1) * SLOT);
    static_assert(slot_of(tile_V(7)) == SLOT, "");
    const lds_cptr kbs[2] = {kb_[0][0] + reg, kb_[0][1] + reg}, vbs[2] = {vb[0][0] + reg + 8192, vb[0][1] + reg + 8192};
    float m = -__builtin_inff(), l = 0.f;
#pragma unroll
    for (int d = 0; d < 4; ++d) o[d] = f32x16{};
#pragma unroll 1
    for (int kb = 0; kb < WIN / 32; ++kb) {
#pragma unroll
      for (int R = 0; R < 4; ++R) {
        const unsigned vo = (unsigned)((8 * R + dr) * 256 + 64 * dc + 16 * (dx ^ ((2 * R + (dr >> 2)) & 3)));
        const unsigned so = __builtin_amdgcn_readfirstlane(kb * 8192), ld = __builtin_amdgcn_readfirstlane(lds0 + reg + 2048 * R);
        asm volatile("s_nop 4" ::: "memory");
        dma16(ld, vo, srdK, so); dma16(ld + 1024, vo + 128, srdK, so); dma16(ld + 8192, vo, srdV, so); dma16(ld + 9216, vo + 128, srdV, so);
      }
      asm volatile("s_waitcnt vmcnt(0)" ::: "memory");
      f32x16 sc = {};
#pragma unroll
      for (int s = 0; s < 8; ++s) { sc = mfma32(rowfrag(kbs[s & 1] + 512 * (s >> 1)), qf[s], sc); SBAR(); }
      float mx = sc[0];
#pragma unroll
      for (int r = 1; r < 16; ++r) mx = __builtin_fmaxf(mx, sc[r]);
      const float mn = __builtin_fmaxf(m, swap_max(mx)), al = __builtin_amdgcn_exp2f(m - mn);
      m = mn; l *= al;
#pragma unroll
      for (int r = 0; r < 16; ++r) { sc[r] = __builtin_amdgcn_exp2f(sc[r] - mn); l += sc[r]; }
#pragma unroll
      for (int d = 0; d < 4; ++d)
#pragma unroll
        for (int r = 0; r < 16; ++r) o[d][r] *= al;
#pragma unroll
      for (int ks = 0; ks < 2; ++ks) {
        const u32x4 pk = {cvtpk(sc[8 * ks], sc[8 * ks + 1]), cvtpk(sc[8 * ks + 2], sc[8 * ks + 3]), cvtpk(sc[8 * ks + 4], sc[8 * ks + 5]), cvtpk(sc[8 * ks + 6], sc[8 * ks + 7])};
#pragma unroll
        for (int d = 0; d < 4; ++d) { o[d] = mfma32(trfrag(vbs[0] + 4096 * ks + 512 * d, vbs[1] + 4096 * ks + 512 * d), __builtin_bit_cast(bf16x8, pk), o[d]); SBAR(); }
      }
      asm volatile("s_waitcnt lgkmcnt(0)" ::: "memory");
    }
    const float inv = 1.f / swap_add(l);
    static_for<4>(LAMBDA(dc_) { out_db(dc_, inv); });
  }
  const int rt = (xcd >> 2) * 8 + (idx >> 2), gi = (xcd & 3) * 4 + (idx & 3);
  {
    unsigned rf[4] = {tag, tag, tag, tag};
    if (wid == 0) {
#pragma unroll
      for (int k = 0; k < 4; ++k) rf[k] = __hip_atomic_load((gu32*)sync + SY_RF + lane + 64 * k, RLX_AGENT);
    }
    asm volatile("s_waitcnt vmcnt(0)" ::: "memory");
    if (wid == 0) {
      for (unsigned spins = 0; spins < SPIN_CAP; ++spins) {
        if (__all(rf[0] == tag && rf[1] == tag && rf[2] == tag && rf[3] == tag)) break;
#pragma unroll
        for (int k = 0; k < 4; ++k) rf[k] = __hip_atomic_load((gu32*)sync + SY_RF + lane + 64 * k, RLX_AGENT);
      }
    }
    asm volatile("" ::: "memory");
    __syncthreads();
    if (tid == 0) __hip_atomic_store((gu32*)sync + SY_OF + (qt * 32 + head) * FS, tag, RLX_AGENT);
  }
  unsigned* const c1 = sync + SY_CF + rt * 16 * FS, * const c2 = c1 + 256 * FS, * const c3 = c1 + 512 * FS;
  const u16* const mod = (const u16*)(ws + WS_MOD);
  const __amdgpu_buffer_rsrc_t rmod = __builtin_amdgcn_make_buffer_rsrc((void*)mod, 0, 3840 * 2, 0x00020000);
  u16 *const obuf = (u16*)(ws + WS_O), *const wot = (u16*)(ws + WS_WOT), *const w1t = (u16*)(ws + WS_W1T), *const w2t = (u16*)(ws + WS_W2T);
  u16 *const x1 = (u16*)(ws + WS_X1), *const hid = (u16*)(ws + WS_HID), *const h2 = (u16*)(ws + WS_H2), *const part = (u16*)(ws + WS_P);
  const int nft = gi < 15 ? 7 : 3;
  const __amdgpu_buffer_rsrc_t raD = __builtin_amdgcn_make_buffer_rsrc((void*)(w1t + (size_t)(7 * gi) * 80 * 128), 0, nft * 80 * 256, 0x00020000);
  RingA<4, 7, 20, 80, 5> RD; RD.init(lds0, wid, lane);
  {
    const int kq = gi >> 2, j = gi & 3, qb = wid & 3;
    const __amdgpu_buffer_rsrc_t ra = __builtin_amdgcn_make_buffer_rsrc((void*)(wot + ((size_t)(10 * j) * 512 + kq * 128) * 128), 0, 10 * 512 * 256, 0x00020000);
    const __amdgpu_buffer_rsrc_t rb = __builtin_amdgcn_make_buffer_rsrc((void*)(obuf + ((size_t)(8 * rt + 2 * qb) * 512 + kq * 128) * 128), 0, 2 * 512 * 256, 0x00020000);
    const __amdgpu_buffer_rsrc_t rp = __builtin_amdgcn_make_buffer_rsrc((void*)part, 0, 4 * SQ * DM * 2, 0x00020000);
    f32x4 acc[10][2] = {};
    RingA<4, 10, 32, 512, 4> RA; RA.init(lds0, wid, lane);
    RA.pre<0, 1>(ra);
    await_flags<8>(sync + SY_OF + ((rt >> 1) * 32 + kq * 8) * FS, tag);
    ring_gemm<4, 10, 32, 512, 512, 1, 4, 8>(acc, RA, ra, rb, lds, wid, lane, [&](auto ic) __attribute__((always_inline)) { RD.template issue<ic.value / 4, ic.value % 4>(raD); });
    pair_reduce<10>(acc, lds + 81920, wid, lane);
    const int nt0 = wid < 4 ? 0 : 5;
    const unsigned vo = ((((8 * rt + 2 * qb + ((lane >> 4) & 1)) * 80 + j * 20 + nt0 * 2 + (lane >> 5)) * 4 + kq) * 16 + (lane & 15)) * 16;
    auto outp = [&](auto ntc) __attribute__((always_inline)) {
      constexpr int nt = ntc.value, o = nt % 5;
      const u32x2 w0 = {cvtpk(acc[nt][0][0], acc[nt][0][1]), cvtpk(acc[nt][0][2], acc[nt][0][3])}, w1 = {cvtpk(acc[nt][1][0], acc[nt][1][1]), cvtpk(acc[nt][1][2], acc[nt][1][3])};
      st16(rp, vo + o * 2 * 1024, pair16(w0, w1));
    };
    if (wid < 4) { asm volatile("; lo-out" ::: "memory"); static_for<5>(LAMBDA(c) { outp(c); }); asm volatile("; lo-end" ::: "memory"); }
    else { asm volatile("; hi-out" ::: "memory"); static_for<5>(LAMBDA(c) { outp(IC<5 + c.value>{}); }); asm volatile("; hi-end" ::: "memory"); }
  }
  publish(c1 + gi * FS, tag);
  {
    float* red = (float*)(lds + 98304);
    const int r = tid & 7, row = rt * 128 + gi * 8 + r;
    const bool two = wid < 2;
    const __amdgpu_buffer_rsrc_t rp = __builtin_amdgcn_make_buffer_rsrc((void*)part, 0, 4 * SQ * DM * 2, 0x00020000);
    const __amdgpu_buffer_rsrc_t rx1 = __builtin_amdgcn_make_buffer_rsrc((void*)x1, 0, SQ * DM * 2, 0x00020000);
    const __amdgpu_buffer_rsrc_t rh2 = __builtin_amdgcn_make_buffer_rsrc((void*)h2, 0, SQ * DM * 2, 0x00020000);
    const u16* x = karg<const u16>(KA_X);
    u32x4 p[2][4], xv[2], gv[2], sbv[2], cbv[2]; unsigned blk[2], pblk[2];
#pragma unroll
    for (int u = 0; u < 2; ++u) {
      const int c = (u == 0 || two) ? (tid >> 3) + 64 * u : (tid >> 3);
      blk[u] = (((row >> 4) * 80 + c) * 16 + (row & 15)) * 16; pblk[u] = (((row >> 4) * 80 + c) * 64 + (row & 15)) * 16;
      xv[u] = *(const u32x4*)(x + (size_t)row * DM + c * 8);
      gv[u] = ld16c(rmod, (2 * 640 + c * 8) * 2); sbv[u] = ld16c(rmod, (3 * 640 + c * 8) * 2); cbv[u] = ld16c(rmod, (4 * 640 + c * 8) * 2);
    }
    RD.pre<2, 3>(raD);
    if (b == 119 && wid == 7) {
      bool ok = false;
      for (unsigned spins = 0; spins < SPIN_CAP && !ok; ++spins) {
        bool all = true;
#pragma unroll
        for (int k = 0; k < 4; ++k) all &= __hip_atomic_load((gu32*)sync + SY_STARTED + lane + 64 * k, RLX_AGENT) == tag;
        ok = __all(all);
      }
      if (lane == 0) __hip_atomic_store((gu32*)sync, tag, RLX_AGENT);
    }
    await_flags<16>(c1, tag);
#pragma unroll
    for (int k = 0; k < 4; ++k) p[0][k] = ld16c(rp, pblk[0] + k * 256);
    if (two) {
#pragma unroll
      for (int k = 0; k < 4; ++k) p[1][k] = ld16c(rp, pblk[1] + k * 256);
    }
    float v[2][8]; float s = 0; u32x4 x1v[2];
    auto resid = [&](auto uc) __attribute__((always_inline)) {
      constexpr int u = uc.value;
#pragma unroll
      for (int e = 0; e < 4; ++e) {
        const float a = (bf_lo(p[u][0][e]) + bf_lo(p[u][1][e])) + (bf_lo(p[u][2][e]) + bf_lo(p[u][3][e])), b = (bf_hi(p[u][0][e]) + bf_hi(p[u][1][e])) + (bf_hi(p[u][2][e]) + bf_hi(p[u][3][e]));
        x1v[u][e] = cvtpk(__builtin_fmaf(bf_lo(gv[u][e]), a, bf_lo(xv[u][e])), __builtin_fmaf(bf_hi(gv[u][e]), b, bf_hi(xv[u][e])));
        v[u][2 * e] = bf_lo(x1v[u][e]); v[u][2 * e + 1] = bf_hi(x1v[u][e]); s += v[u][2 * e] + v[u][2 * e + 1];
      }
    };
    resid(IC<0>{});
    if (two) resid(IC<1>{});
    s = xor_add<32>(xor_add<16>(xor_add<8>(s)));
    if (lane < 8) red[wid * 8 + r] = s;
    lds_barrier();
    float tot = 0;
#pragma unroll
    for (int i = 0; i < 8; ++i) tot += red[i * 8 + r];
    const float mean = tot * (1.f / DM); float q = 0;
#pragma unroll
    for (int e = 0; e < 8; ++e) { v[0][e] -= mean; q += v[0][e] * v[0][e]; }
    if (two) {
#pragma unroll
      for (int e = 0; e < 8; ++e) { v[1][e] -= mean; q += v[1][e] * v[1][e]; }
    }
    q = xor_add<32>(xor_add<16>(xor_add<8>(q)));
    if (lane < 8) red[64 + wid * 8 + r] = q;
    lds_barrier();
    float qt = 0;
#pragma unroll
    for (int i = 0; i < 8; ++i) qt += red[64 + i * 8 + r];
    const float rstd = __builtin_amdgcn_rsqf(qt * (1.f / DM) + 1e-5f);
    auto norm = [&](auto uc) __attribute__((always_inline)) {
      constexpr int u = uc.value;
      u32x4 hv;
#pragma unroll
      for (int e = 0; e < 4; ++e)
        hv[e] = cvtpk(__builtin_fmaf(v[u][2 * e] * rstd, bf_lo(cbv[u][e]), bf_lo(sbv[u][e])), __builtin_fmaf(v[u][2 * e + 1] * rstd, bf_hi(cbv[u][e]), bf_hi(sbv[u][e])));
      st16(rh2, blk[u], hv); st16(rx1, blk[u], x1v[u]);
    };
    norm(IC<0>{});
    if (two) norm(IC<1>{});
  }
  publish(c2 + gi * FS, tag);
  const int jE = gi & 7, rtE = rt * 2 + (gi >> 3);
  const __amdgpu_buffer_rsrc_t raE = __builtin_amdgcn_make_buffer_rsrc((void*)(w2t + (size_t)(5 * jE) * 216 * 128), 0, 5 * 216 * 256, 0x00020000);
  RingA<2, 5, 54, 216, 4> RE; RE.init(lds0, wid, lane);
  {
    const int qb = wid & 3, nt0 = wid < 4 ? 0 : 4;
    const __amdgpu_buffer_rsrc_t rb = __builtin_amdgcn_make_buffer_rsrc((void*)(h2 + (size_t)(8 * rt + 2 * qb) * 80 * 128), 0, 2 * 80 * 256, 0x00020000);
    const __amdgpu_buffer_rsrc_t rh = __builtin_amdgcn_make_buffer_rsrc((void*)hid, 0, SQ * FF * 2, 0x00020000);
    const u16* b1 = karg<const u16>(KA_B1);
    u32x2 bv[4];
#pragma unroll
    for (int o = 0; o < 4; ++o) bv[o] = *(const u32x2*)(b1 + (nt0 + o < nft ? (7 * gi + nt0 + o) * 16 + 4 * (lane >> 4) : 0));
    f32x4 acc[7][2] = {};
    await_flags<16>(c2, tag);
    ring_gemm<4, 7, 20, 80, 80, 3, 5, 10>(acc, RD, raD, rb, lds, wid, lane, [&](auto ic) __attribute__((always_inline)) { RE.template issue<ic.value / 5, ic.value % 5>(raE); });
    pair_reduce<7>(acc, lds + 98304, wid, lane);
    const unsigned vo = (((8 * rt + 2 * qb + ((lane >> 4) & 1)) * 216 + (7 * gi + nt0) * 2 + (lane >> 5)) * 16 + (lane & 15)) * 16;
    auto outp = [&](auto ntc, auto oc) __attribute__((always_inline)) {
      constexpr int nt = ntc.value, o = oc.value;
      if (nt < nft) {
        const float b0 = bf_lo(bv[o][0]), b1_ = bf_hi(bv[o][0]), b2_ = bf_lo(bv[o][1]), b3 = bf_hi(bv[o][1]);
        u32x2 w[2];
#pragma unroll
        for (int q = 0; q < 2; ++q) {
          const float v0 = rbf(__builtin_fmaxf(rbf(acc[nt][q][0]) + b0, 0.f)), v1 = rbf(__builtin_fmaxf(rbf(acc[nt][q][1]) + b1_, 0.f));
          const float v2 = rbf(__builtin_fmaxf(rbf(acc[nt][q][2]) + b2_, 0.f)), v3 = rbf(__builtin_fmaxf(rbf(acc[nt][q][3]) + b3, 0.f));
          w[q] = u32x2{cvtpk(v0 * v0, v1 * v1), cvtpk(v2 * v2, v3 * v3)};
        }
        st16(rh, vo + o * 2 * 256, pair16(w[0], w[1]));
      }
    };
    if (wid < 4) { asm volatile("; lo-out" ::: "memory"); static_for<4>(LAMBDA(c) { outp(c, c); }); asm volatile("; lo-end" ::: "memory"); }
    else { asm volatile("; hi-out" ::: "memory"); static_for<3>(LAMBDA(c) { outp(IC<4 + c.value>{}, c); }); asm volatile("; hi-end" ::: "memory"); }
  }
  publish(c3 + gi * FS, tag);
  {
    const int qb = wid & 1;
    const __amdgpu_buffer_rsrc_t rb = __builtin_amdgcn_make_buffer_rsrc((void*)(hid + (size_t)(4 * rtE + 2 * qb) * 216 * 128), 0, 2 * 216 * 256, 0x00020000);
    const __amdgpu_buffer_rsrc_t rx1 = __builtin_amdgcn_make_buffer_rsrc((void*)x1, 0, SQ * DM * 2, 0x00020000);
    const u16* b2 = karg<const u16>(KA_B2);
    u16* out = karg<u16>(KA_OUT);
    u32x4 xv[2], gv[2], bv[2];
#pragma unroll
    for (int it = 0; it < 2; ++it) {
      const int item = tid + it * 512, q = item & 63, c = item < 640 ? item >> 6 : 0, row = rtE * 64 + q;
      xv[it] = ld16c(rx1, (((row >> 4) * 80 + jE * 10 + c) * 16 + (row & 15)) * 16);
      gv[it] = ld16c(rmod, (5 * 640 + jE * 80 + c * 8) * 2); bv[it] = *(const u32x4*)(b2 + jE * 80 + c * 8);
    }
    f32x4 acc[5][2] = {};
    RE.pre<2, 3>(raE);
    await_flags<16>(c3, tag);
    ring_gemm<2, 5, 54, 216, 216, 3, 4, 0>(acc, RE, raE, rb, lds, wid, lane, [&](auto) {});
    f32x4* red = (f32x4*)lds;
    constexpr int HI = (3 * 40960 - 4 * 10240) / 16;
    {
      f32x4* mine = red + (wid * 10) * 64 + (wid >= 4 ? HI : 0) + lane;
#pragma unroll
      for (int n = 0; n < 5; ++n) { mine[(n * 2) * 64] = acc[n][0]; mine[(n * 2 + 1) * 64] = acc[n][1]; }
    }
    lds_barrier();
#pragma unroll
    for (int it = 0; it < 2; ++it) {
      const int item = tid + it * 512;
      if (item < 640) {
        const int q = item & 63, c = item >> 6, n = c >> 1, l0 = (q & 15) + 32 * (c & 1), base = (((q >> 5) * 5 + n) * 2 + ((q >> 4) & 1)) * 64 + l0;
        f32x4 lo = {0, 0, 0, 0}, hi = {0, 0, 0, 0};
#pragma unroll
        for (int w = 0; w < 4; ++w) { const int o_ = base + w * 2 * 5 * 2 * 64 + (w >= 2 ? HI : 0); lo += red[o_]; hi += red[o_ + 16]; }
        const float v[8] = {lo[0], lo[1], lo[2], lo[3], hi[0], hi[1], hi[2], hi[3]};
        u32x4 o;
#pragma unroll
        for (int e = 0; e < 4; ++e) {
          const float a = bf_lo(xv[it][e]) + rbf(bf_lo(gv[it][e]) * rbf(rbf(v[2 * e]) + bf_lo(bv[it][e]))), b = bf_hi(xv[it][e]) + rbf(bf_hi(gv[it][e]) * rbf(rbf(v[2 * e + 1]) + bf_hi(bv[it][e])));
          o[e] = cvtpk(a, b);
        }
        *(u32x4*)(out + (size_t)(rtE * 64 + q) * DM + jE * 80 + c * 8) = o;
      }
    }
  }
}

extern "C" void kernel_launch(void* const* d_in, const int* in_sizes, int n_in, void* d_out, int out_size, void* d_ws, size_t ws_size, hipStream_t stream) {
  char* ws = (char*)d_ws;
  (void)hipFuncSetAttribute((const void*)k_block, hipFuncAttributeMaxDynamicSharedMemorySize, 163840);
  k_block<<<256, 512, 163840, stream>>>((const u16*)d_in[0], (const u16*)d_in[1], (const u16*)d_in[2], (const u16*)d_in[3], (const u16*)d_in[4], (const u16*)d_in[6], (const u16*)d_in[7],
                                        (const u16*)d_in[8], (const u16*)d_in[9], (const u16*)d_in[10], (const u16*)d_in[11], (u16*)d_out, ws);
}
```

```cpp
#include <hip/hip_runtime.h>
#include <stdint.h>
#include <utility>

typedef unsigned short u16;
typedef short bf16x8 __attribute__((ext_vector_type(8)));
typedef short s16x4 __attribute__((ext_vector_type(4)));
typedef float f32x16 __attribute__((ext_vector_type(16)));
typedef float f32x4 __attribute__((ext_vector_type(4)));
typedef float f32x2 __attribute__((ext_vector_type(2)));
typedef __bf16 bf16x2 __attribute__((ext_vector_type(2)));
typedef unsigned u32x4 __attribute__((ext_vector_type(4)));
typedef unsigned u32x2 __attribute__((ext_vector_type(2)));
#define DI __device__ __forceinline__
#define GAS __attribute__((address_space(1)))
#define LAS __attribute__((address_space(3)))
#define SBAR() __builtin_amdgcn_sched_barrier(0)
#define PIN(x) asm volatile("" : "+v"(x))

constexpr int SQ = 2048, DM = 640, NH = 32, DH = 128, SK = 2048, WIN = 512, FF = 1728;
constexpr size_t WS_MOD = 0;
constexpr size_t WS_SYNC = 32768;
constexpr size_t WS_H = 524288;
constexpr size_t WS_O = WS_H + (size_t)SQ * DM * 2;
constexpr size_t WS_WOT = WS_O + (size_t)SQ * 4096 * 2;
constexpr size_t WS_W1T = WS_WOT + (size_t)4096 * DM * 2;
constexpr size_t WS_W2T = WS_W1T + (size_t)DM * FF * 2;
constexpr size_t WS_X1 = WS_W2T + (size_t)DM * FF * 2 + 65536;
constexpr size_t WS_HID = WS_X1 + (size_t)SQ * DM * 2;
constexpr size_t WS_H2 = WS_HID + (size_t)SQ * FF * 2 + 65536;
constexpr size_t WS_P = WS_H2 + (size_t)SQ * DM * 2;
constexpr size_t WS_END = WS_P + (size_t)4 * SQ * DM * 2;

template <int... I, class F> DI void static_for_impl(std::integer_sequence<int, I...>, F&& f) { (f(std::integral_constant<int, I>{}), ...); }
template <int N, class F> DI void static_for(F&& f) { static_for_impl(std::make_integer_sequence<int, N>{}, f); }
#define LAMBDA(ic) [&](auto ic) __attribute__((always_inline))

DI float bf_lo(unsigned w) { return __uint_as_float(w << 16); }
DI float bf_hi(unsigned w) { return __uint_as_float(w & 0xffff0000u); }
DI unsigned cvtpk(float lo, float hi) { f32x2 v = {lo, hi}; bf16x2 b = __builtin_convertvector(v, bf16x2); return __builtin_bit_cast(unsigned, b); }
DI float rbf(float x) { return bf_lo(cvtpk(x, 0.f)); }
DI f32x16 mfma32(bf16x8 a, bf16x8 b, f32x16 c) { return __builtin_amdgcn_mfma_f32_32x32x16_bf16(a, b, c, 0, 0, 0); }
DI f32x4 mfma16(bf16x8 a, bf16x8 b, f32x4 c) { return __builtin_amdgcn_mfma_f32_16x16x32_bf16(a, b, c, 0, 0, 0); }
DI float swap_max(float m) { auto r = __builtin_amdgcn_permlane32_swap(__float_as_uint(m), __float_as_uint(m), false, false); return __builtin_fmaxf(__uint_as_float(r[0]), __uint_as_float(r[1])); }
DI float swap_add(float m) { auto r = __builtin_amdgcn_permlane32_swap(__float_as_uint(m), __float_as_uint(m), false, false); return __uint_as_float(r[0]) + __uint_as_float(r[1]); }

DI void lds_barrier() { asm volatile("s_waitcnt lgkmcnt(0)\n\ts_barrier" ::: "memory"); }
template <int CTRL> DI float dpp_mov(float v) { return __builtin_bit_cast(float, __builtin_amdgcn_update_dpp(0, __builtin_bit_cast(int, v), CTRL, 0xf, 0xf, true)); }
template <int M> DI float xor_add(float v) {
  if constexpr (M == 1) return v + dpp_mov<0xB1>(v);
  else if constexpr (M == 2) return v + dpp_mov<0x4E>(v);
  else if constexpr (M == 4) return v + dpp_mov<0x141>(dpp_mov<0x1B>(v));
  else if constexpr (M == 8) return v + dpp_mov<0x128>(v);
  else if constexpr (M == 16) { const auto r = __builtin_amdgcn_permlane16_swap(__float_as_uint(v), __float_as_uint(v), false, false); return __uint_as_float(r[0]) + __uint_as_float(r[1]); }
  else { const auto r = __builtin_amdgcn_permlane32_swap(__float_as_uint(v), __float_as_uint(v), false, false); return __uint_as_float(r[0]) + __uint_as_float(r[1]); }
}
typedef GAS unsigned gu32;
typedef GAS unsigned long long gu64;
#define RLX_AGENT __ATOMIC_RELAXED, __HIP_MEMORY_SCOPE_AGENT
constexpr unsigned SPIN_CAP = 1u << 18;
constexpr int SC1 = 16;
constexpr int FS = 32;
constexpr int SY_STARTED = 64, SY_RF = 512, SY_GRAN = 1024  , SY_HF = 36864, SY_OF = SY_HF + 256 * FS, SY_CF = SY_OF + 256 * FS  ;
constexpr int NREP = 8;
DI void st16(__amdgpu_buffer_rsrc_t r, unsigned voff, u32x4 v) { __builtin_amdgcn_raw_buffer_store_b128(v, r, voff, 0, SC1); }
DI u32x4 ld16c(__amdgpu_buffer_rsrc_t r, unsigned voff) { return __builtin_amdgcn_raw_buffer_load_b128(r, voff, 0, SC1); }
DI void publish(unsigned* flag, unsigned tag) {
  asm volatile("s_waitcnt vmcnt(0)" ::: "memory");
  __syncthreads();
  if (threadIdx.x == 0) __hip_atomic_store((gu32*)flag, tag, RLX_AGENT);
}
template <int N> DI void await_flags(const unsigned* flags, unsigned tag) {
  if (threadIdx.x < 64) {
    gu32* p = (gu32*)flags + (threadIdx.x < N ? threadIdx.x : 0) * FS;
    unsigned a = __hip_atomic_load(p, RLX_AGENT); __builtin_amdgcn_s_sleep(2);
    unsigned b = __hip_atomic_load(p, RLX_AGENT);
    for (unsigned spins = 0; spins < SPIN_CAP; ++spins) {
      if (__all(a == tag)) break;
      a = __hip_atomic_load(p, RLX_AGENT); __builtin_amdgcn_s_sleep(2);
      if (__all(b == tag)) break;
      b = __hip_atomic_load(p, RLX_AGENT); __builtin_amdgcn_s_sleep(2);
    }
  }
  asm volatile("" ::: "memory");
  __syncthreads();
}
template <class T> DI T* karg(int i) {
  const __attribute__((address_space(4))) char* p = (const __attribute__((address_space(4))) char*)__builtin_amdgcn_kernarg_segment_ptr();
  asm volatile("" : "+s"(p));
  return *(T* const __attribute__((address_space(4)))*)(p + 8 * i);
}
enum { KA_X = 0, KA_KC, KA_VC, KA_WQ, KA_WO, KA_W1, KA_B1, KA_W2, KA_B2, KA_C, KA_WMOD, KA_OUT, KA_WS };

DI u32x4 pair16(u32x2 x, u32x2 y) {
  const auto a = __builtin_amdgcn_permlane16_swap(x[0], y[0], false, false), b = __builtin_amdgcn_permlane16_swap(x[1], y[1], false, false);
  return u32x4{a[0], b[0], a[1], b[1]};
}
constexpr int NS = 8, SLOT = 16384;
constexpr float QSCALE = 0.088388347648318440f * 1.4426950408889634f;
constexpr float PBIAS = 96.f;
#define PLIM_V 1073741824.f
constexpr float PLIM = PLIM_V;
constexpr int tile_K(int t) { return t == 0 ? 10 : 9 + 2 * t; }
constexpr int tile_V(int t) { return t == 7 ? 25 : 12 + 2 * t; }
constexpr int slot_of(int idx) { return (idx % NS) * SLOT; }
constexpr int lastuse(int idx) { return idx < 10 ? idx : (idx == 10 ? 10 : (idx == 25 ? 18 : ((idx & 1) ? 10 + (idx - 9) / 2 : 11 + (idx - 12) / 2))); }
constexpr int need_bar(int idx) {
  if (idx < 10) return idx == 0 ? 0 : idx - 1;
  if (idx == 10) return 9;
  if (idx == 25) return 17;
  return (idx & 1) ? 10 + (idx - 9) / 2 - 1 : 11 + (idx - 12) / 2;
}
constexpr int rt_step(int u) { return u == 0 ? 9 : (u == 1 ? 10 : 12); }
constexpr int rt_proc(int u) { return u == 0 ? 11 : (u == 1 ? 12 : 14); }
constexpr int rt_slot(int u) { return (u == 1 ? NS + 1 : NS) * SLOT; }
struct Sched { int npro; int pro[16]; int nops[19]; int op[19][16]; int wait[19]; int rt_done; };
constexpr Sched make_sched() {
  Sched p{}; int pos = 0; int tile_end[26] = {}; int h_end[10] = {}; int u_end[3] = {}; int next_tile = 3;
  auto pro_h = [&](int i) { for (int k = 0; k < 4; ++k) p.pro[p.npro++] = 100 + 4 * i + k; pos += 4; h_end[i] = pos; };
  pro_h(0); pro_h(1);
  tile_end[0] = tile_end[1] = tile_end[2] = 0; int last_rt_store = 1 << 20; p.rt_done = -1;
  for (int g = 0; g < 19; ++g) {
    int need = 0;
    for (int idx = 0; idx < next_tile; ++idx) if (need_bar(idx) <= g && tile_end[idx] > need) need = tile_end[idx];
    if (g < 10 && h_end[g] > need) need = h_end[g];
    for (int u = 0; u < 3; ++u) if (rt_proc(u) <= g && u_end[u] > need) need = u_end[u];
    p.wait[g] = pos - need;
    if (p.rt_done < 0 && need >= last_rt_store) p.rt_done = g;
    int n = 0;
    for (int u = 0; u < 3; ++u) if (g == rt_proc(u)) { p.op[g][n++] = 300 + 2 * u; p.op[g][n++] = 301 + 2 * u; }
    if (g == 0) { for (int k = 0; k < 4; ++k) p.op[g][n++] = 100 + 4 * 2 + k; pos += 4; h_end[2] = pos; }
    const int maxt = g < 7 ? 1 : 2;
    for (int c = 0; c < maxt && next_tile < 26; ++c) {
      if (next_tile >= NS && lastuse(next_tile - NS) >= g) break;
      p.op[g][n++] = 2 * next_tile; p.op[g][n++] = 2 * next_tile + 1; pos += 2; tile_end[next_tile] = pos; ++next_tile;
    }
    if (g + 3 < 10) { for (int k = 0; k < 4; ++k) p.op[g][n++] = 100 + 4 * (g + 3) + k; pos += 4; h_end[g + 3] = pos; }
    for (int u = 0; u < 3; ++u) if (g == rt_step(u)) { p.op[g][n++] = 200 + 2 * u; p.op[g][n++] = 201 + 2 * u; pos += 2; u_end[u] = pos; }
    for (int u = 0; u < 3; ++u) if (g == rt_proc(u)) { p.op[g][n++] = 310 + 2 * u; p.op[g][n++] = 311 + 2 * u; pos += 2; if (u == 2) last_rt_store = pos; }
    p.nops[g] = n;
  }
  for (int idx = 3; idx < 26; ++idx) if (tile_end[idx] == 0) p.wait[0] = -1;
  if (p.rt_done < 0) p.wait[0] = -1;
  return p;
}
constexpr Sched SCH = make_sched();
static_assert(SCH.wait[0] >= 0, "schedule incomplete");
constexpr bool sched_ok() {
  int step_of[26] = {}; for (int i = 0; i < 26; ++i) step_of[i] = -1;
  for (int g = 0; g < 19; ++g) for (int j = 0; j < SCH.nops[g]; ++j) if (SCH.op[g][j] < 100) step_of[SCH.op[g][j] >> 1] = g;
  for (int i = 3; i < 26; ++i) if (step_of[i] >= need_bar(i)) return false;
  return true;
}
static_assert(sched_ok(), "a tile is issued too late");

DI void dma16(unsigned ldsaddr, unsigned voff, __amdgpu_buffer_rsrc_t srd, unsigned soff) {
  asm volatile("s_mov_b32 m0, %0\n\ts_nop 0\n\tbuffer_load_dwordx4 %1, %2, %3 offen lds" ::"s"(ldsaddr), "v"(voff), "s"(srd), "s"(soff) : "memory");
}
template <int OFF> DI void bload16c(bf16x8& dst, unsigned voff, __amdgpu_buffer_rsrc_t srd, unsigned soff) {
  asm volatile("buffer_load_dwordx4 %0, %1, %2, %3 offen offset:%4 sc1" : "=v"(dst) : "v"(voff), "s"(srd), "s"(soff), "i"(OFF) : "memory");
}
DI void dma16c(unsigned ldsaddr, unsigned voff, __amdgpu_buffer_rsrc_t srd, unsigned soff) {
  asm volatile("s_mov_b32 m0, %0\n\ts_nop 0\n\tbuffer_load_dwordx4 %1, %2, %3 offen sc1 lds" ::"s"(ldsaddr), "v"(voff), "s"(srd), "s"(soff) : "memory");
}
typedef LAS const char* lds_cptr;
typedef short v4i16_t __attribute__((ext_vector_type(4)));
DI s16x4 vtr(lds_cptr p) { return __builtin_bit_cast(s16x4, __builtin_amdgcn_ds_read_tr16_b64_v4i16((LAS v4i16_t*)p)); }
DI bf16x8 trfrag(lds_cptr p0, lds_cptr p1) { const s16x4 lo = vtr(p0), hi = vtr(p1); return __builtin_shufflevector(lo, hi, 0, 1, 2, 3, 4, 5, 6, 7); }
DI bf16x8 rowfrag(lds_cptr p) { return *(LAS const bf16x8*)p; }
DI float max3(float a, float b, float c) { return __builtin_fmaxf(__builtin_fmaxf(a, b), c); }
template <int N> DI void wait_bar4(bf16x8 (&b)[4]) {
  asm volatile("s_waitcnt vmcnt(%4)\n\ts_barrier" : "+v"(b[0]), "+v"(b[1]), "+v"(b[2]), "+v"(b[3]) : "i"(N) : "memory");
}
template <int N> DI void wait_bar() { asm volatile("s_waitcnt vmcnt(%0)\n\ts_barrier" ::"i"(N) : "memory"); }
template <int V> using IC = std::integral_constant<int, V>;

template <int N> DI void wait_bar4b(bf16x8 (&b)[2][2]) {
  asm volatile("s_waitcnt vmcnt(%4)\n\ts_barrier" : "+v"(b[0][0]), "+v"(b[0][1]), "+v"(b[1][0]), "+v"(b[1][1]) : "i"(N) : "memory");
}
template <int NQB, int NT, int KS> struct Ring {
  static constexpr int KSP = 8 / NQB, SK = 2 * KSP, NSTG = (KS + SK - 1) / SK, NP = SK * NT, PA = (NP + 7) / 8, SB = PA * 8 * 1024, FR = 2 * NT;
};
template <int NQB, int NT, int KS, int KC_A, int NSLOT> struct RingA {
  using R = Ring<NQB, NT, KS>;
  unsigned voff[R::PA]; unsigned ldsw;
  DI void init(unsigned lds0, int wid, int lane) {
    ldsw = lds0 + wid * 1024;
#pragma unroll
    for (int i = 0; i < R::PA; ++i) { const int p = i * 8 + wid, ks = p / NT, nt = p - ks * NT; voff[i] = p < R::NP ? (unsigned)(nt * KC_A * 256 + ks * 1024 + lane * 16) : 0x7ff00000u; }
  }
  template <int S, int I> DI void issue(__amdgpu_buffer_rsrc_t ra) const { dma16c(ldsw + ((S % NSLOT) * R::SB + I * 8192), voff[I], ra, S * R::SK * 1024); }
  template <int S0, int S1> DI void pre(__amdgpu_buffer_rsrc_t ra) const { static_for<S1 - S0>(LAMBDA(sc) { static_for<R::PA>(LAMBDA(ic) { this->template issue<S0 + sc.value, ic.value>(ra); }); }); }
};
template <int NQB, int NT, int KS, int KC_A, int KC_B, int NPRE, int NSLOT, int NHOOK, class HOOK>
DI void ring_gemm(f32x4 (&acc)[NT][2], const RingA<NQB, NT, KS, KC_A, NSLOT>& RA, __amdgpu_buffer_rsrc_t ra, __amdgpu_buffer_rsrc_t rb, const char* lds, int wid, int lane, HOOK&& hook) {
  using R = Ring<NQB, NT, KS>;
  constexpr int SK = R::SK, NSTG = R::NSTG, PA = R::PA, FR = R::FR, PD = 5;
  static_assert(NSTG >= 3 && FR > PD + 1, "");
  const int kq = wid / NQB;
  bf16x8 B[3][2][2];
  const unsigned voffB = lane * 16 + 2 * kq * 1024;
  auto issueA1 = [&](auto sc, auto ic) __attribute__((always_inline)) { RA.template issue<sc.value, ic.value>(ra); };
  auto issueB1 = [&](auto sc, auto ic) __attribute__((always_inline)) {
    constexpr int s = sc.value, i = ic.value, ks = i >> 1, q = i & 1;
    bload16c<0>(B[s % 3][ks][q], voffB, rb, q * KC_B * 256 + (s * SK + ks) * 1024);
  };
  if constexpr (NPRE == 0) static_for<PA>(LAMBDA(ic) { issueA1(IC<0>{}, ic); });
  static_for<4>(LAMBDA(ic) { issueB1(IC<0>{}, ic); });
  if constexpr (NPRE > 1) { static_for<4>(LAMBDA(ic) { issueB1(IC<1>{}, ic); }); wait_bar4b<4>(B[0]); } else wait_bar4b<0>(B[0]);
  const LAS bf16x8* pl = (const LAS bf16x8*)lds + lane + 2 * kq * NT * 64;
  bf16x8 A[PD + 1];
  auto rd = [&](auto mc) __attribute__((always_inline)) { constexpr int M = mc.value, s = M / FR, m = M % FR; A[M % (PD + 1)] = pl[((s % NSLOT) * PA * 8 + m) * 64]; };
  static_for<PD>(LAMBDA(jc) { rd(jc); });
  SBAR();
  static_for<NSTG>(LAMBDA(sc) {
    constexpr int s = sc.value;
    constexpr int n0 = (NPRE <= 1 && s == 0) ? PA + 4 : 0;
    constexpr int sa = s + 2;
    constexpr int nA = (sa < NSTG && sa >= NPRE) ? PA : 0, nB = (s + 2 < NSTG) ? 4 : 0, NI = n0 + nA + nB, NGI = FR - PD - 1;
    constexpr bool full = (s + 1) * SK <= KS;
    static_for<FR>(LAMBDA(mc) {
      constexpr int m = mc.value, M = s * FR + m, ks = m / NT, nt = m % NT;
      if constexpr (m == FR - PD && s + 1 < NSTG) wait_bar4b<nA + nB>(B[(s + 1) % 3]);
      if constexpr (M + PD < NSTG * FR) rd(IC<M + PD>{});
      if constexpr (m < NGI) {
        constexpr int i0 = (m * NI + NGI - 1) / NGI, i1 = ((m + 1) * NI + NGI - 1) / NGI;
        static_for<i1 - i0>(LAMBDA(uc) {
          constexpr int i = i0 + uc.value;
          if constexpr (i < n0) { if constexpr (i < PA) issueA1(IC<1>{}, IC<i>{}); else issueB1(IC<1>{}, IC<i - PA>{}); }
          else if constexpr (i - n0 < nA) issueA1(IC<sa>{}, IC<i - n0>{});
          else issueB1(IC<s + 2>{}, IC<i - n0 - nA>{});
        });
      }
      if constexpr (NHOOK > 0 && M >= (NSTG - 1) * FR - PD) {
        constexpr int j = M - ((NSTG - 1) * FR - PD), HG = FR + PD - 2, i0 = (j * NHOOK + HG - 1) / HG, i1 = ((j + 1) * NHOOK + HG - 1) / HG;
        if constexpr (j < HG) static_for<i1 - i0>(LAMBDA(uc) { hook(IC<i0 + uc.value>{}); });
      }
      if (full || s * SK + 2 * kq + ks < KS) { acc[nt][0] = mfma16(A[M % (PD + 1)], B[s % 3][ks][0], acc[nt][0]); acc[nt][1] = mfma16(A[M % (PD + 1)], B[s % 3][ks][1], acc[nt][1]); }
      SBAR();
    });
  });
}
template <int NT> DI void pair_reduce(f32x4 (&acc)[NT][2], char* lds, int wid, int lane) {
  constexpr int H0 = (NT + 1) / 2;
  f32x4* red = (f32x4*)lds + (size_t)wid * H0 * 2 * 64 + lane;
  const f32x4* in = (const f32x4*)lds + (size_t)(wid ^ 4) * H0 * 2 * 64 + lane;
  lds_barrier();
  if (wid < 4) {
    asm volatile("; lo-send" ::: "memory");
#pragma unroll
    for (int nt = H0; nt < NT; ++nt) { red[((nt - H0) * 2) * 64] = acc[nt][0]; red[((nt - H0) * 2 + 1) * 64] = acc[nt][1]; }
    asm volatile("; lo-sent" ::: "memory");
  } else {
    asm volatile("; hi-send" ::: "memory");
#pragma unroll
    for (int nt = 0; nt < H0; ++nt) { red[(nt * 2) * 64] = acc[nt][0]; red[(nt * 2 + 1) * 64] = acc[nt][1]; }
    asm volatile("; hi-sent" ::: "memory");
  }
  lds_barrier();
  if (wid < 4) {
    asm volatile("; lo-recv" ::: "memory");
#pragma unroll
    for (int nt = 0; nt < H0; ++nt) { acc[nt][0] += in[(nt * 2) * 64]; acc[nt][1] += in[(nt * 2 + 1) * 64]; PIN(acc[nt][0]); PIN(acc[nt][1]); }
    asm volatile("; lo-done" ::: "memory");
  } else {
    asm volatile("; hi-recv" ::: "memory");
#pragma unroll
    for (int nt = H0; nt < NT; ++nt) { acc[nt][0] += in[((nt - H0) * 2) * 64]; acc[nt][1] += in[((nt - H0) * 2 + 1) * 64]; PIN(acc[nt][0]); PIN(acc[nt][1]); }
    asm volatile("; hi-done" ::: "memory");
  }
}

__global__ __launch_bounds__(512, 2) void k_block(const u16* a_x, const u16* a_kc, const u16* a_vc, const u16* a_wq, const u16* a_wo, const u16* a_w1, const u16* a_b1, const u16* a_w2,
                                                   const u16* a_b2, const u16* a_c, const u16* a_wmod, u16* a_out, char* a_ws) {
  extern __shared__ __attribute__((aligned(16))) char lds[];
  const int tid = threadIdx.x, lane = tid & 63, r32 = lane & 31, hi = lane >> 5;
  const int wid = __builtin_amdgcn_readfirstlane(tid >> 6);
  const int b = blockIdx.x, xcd = b & 7, idx = b >> 3;
  const int head = (xcd & 3) * 8 + (idx & 7), qt = (xcd >> 2) * 4 + (idx >> 3);
  const unsigned lds0 = (unsigned)(uintptr_t)lds;
  char* const ws = karg<char>(KA_WS);
  unsigned* const sync = (unsigned*)(ws + WS_SYNC);
  const __amdgpu_buffer_rsrc_t srdW = __builtin_amdgcn_make_buffer_rsrc((void*)(karg<const u16>(KA_WQ) + head * DH), 0, (DM - 1) * 8192 + 256, 0x00020000);
  const int dr = (lane >> 2) & 7, dx = lane & 3, dc = lane >> 5, dz = (2 * wid + (dr >> 2)) & 3;
  const unsigned voffW = (unsigned)((8 * wid + dr) * 8192 + 64 * dc + 16 * (dx ^ dz));
  const unsigned ldsw = lds0 + 2048 * wid;
  unsigned tag;
  {
    float* sm = (float*)(lds + NS * SLOT);
    const u16* x = karg<const u16>(KA_X);
    const int row = b * 8 + wid; const bool act = lane < 40;
    u32x4 w[5]; unsigned cv = 0;
    const int cc = tid & 3, r0 = tid >> 2;
    if (b < 120) {
      const u16* wmod = karg<const u16>(KA_WMOD);
#pragma unroll
      for (int i = 0; i < 5; ++i) w[i] = *(const u32x4*)(wmod + (size_t)(r0 + 128 * i) * 3840 + b * 32 + cc * 8);
      cv = *(const unsigned*)(karg<const u16>(KA_C) + 2 * (tid < 320 ? tid : 0));
    }
    u32x4 xv[2];
    xv[0] = *(const u32x4*)(x + (size_t)row * DM + (act ? lane : 0) * 8); xv[1] = *(const u32x4*)(x + (size_t)row * DM + (act ? lane + 40 : 0) * 8);
    unsigned tg = __hip_atomic_load((gu32*)sync, RLX_AGENT) + 1u;
    if (b < 120 && tid < 320) {
      const float v0 = bf_lo(cv), v1 = bf_hi(cv);
      const unsigned pk = cvtpk(v0 * __builtin_amdgcn_rcpf(1.f + __expf(-v0)), v1 * __builtin_amdgcn_rcpf(1.f + __expf(-v1)));
      *(f32x2*)(sm + 512 + 2 * tid) = f32x2{bf_lo(pk), bf_hi(pk)};
    }
    PIN(tg);
    tag = __builtin_amdgcn_readfirstlane(tg);
    lds_barrier();
    if (tid == 0) __hip_atomic_store((gu32*)sync + SY_STARTED + b, tag, RLX_AGENT);
    if (b < 120) {
      float a[8] = {};
#pragma unroll
      for (int i = 0; i < 5; ++i) {
        const float sv = sm[512 + r0 + 128 * i];
#pragma unroll
        for (int j = 0; j < 4; ++j) { a[2 * j] += sv * bf_lo(w[i][j]); a[2 * j + 1] += sv * bf_hi(w[i][j]); }
      }
      float v4[4], v2[2];
#pragma unroll
      for (int j = 0; j < 4; ++j) { const auto r = __builtin_amdgcn_permlane32_swap(__float_as_uint(a[j]), __float_as_uint(a[j + 4]), false, false); v4[j] = __uint_as_float(r[0]) + __uint_as_float(r[1]); }
#pragma unroll
      for (int j = 0; j < 2; ++j) { const auto r = __builtin_amdgcn_permlane16_swap(__float_as_uint(v4[j]), __float_as_uint(v4[j + 2]), false, false); v2[j] = xor_add<8>(xor_add<4>(__uint_as_float(r[0]) + __uint_as_float(r[1]))); }
      if ((lane & 12) == 0) *(f32x2*)(sm + wid * 32 + cc * 8 + 4 * (lane >> 5) + 2 * ((lane >> 4) & 1)) = f32x2{v2[0], v2[1]};
      lds_barrier();
      {
        const int u = tid & 15;
        float lo = 0.f, hi_ = 0.f;
#pragma unroll
        for (int w8 = 0; w8 < 8; ++w8) { const f32x2 t2 = *(const f32x2*)(sm + w8 * 32 + 2 * u); lo += t2[0]; hi_ += t2[1]; }
        unsigned pk = cvtpk(lo, hi_);
        if ((b >= 20 && b < 40) || (b >= 80 && b < 100)) pk = cvtpk(1.f + bf_lo(pk), 1.f + bf_hi(pk));
        if (b < 40) { if (tid < 16 * NREP) __hip_atomic_store((gu64*)(sync + SY_GRAN + (tid >> 4) * 2048) + b * 16 + u, ((unsigned long long)tag << 32) | pk, RLX_AGENT); }
        else if (tid < 16) __hip_atomic_store((gu32*)(ws + WS_MOD) + b * 16 + u, pk, RLX_AGENT);
      }
    }
#pragma unroll
    for (int i = 0; i < 3; ++i) { dma16(ldsw + slot_of(i), voffW, srdW, i * 64 * 8192); dma16(ldsw + slot_of(i) + 1024, voffW, srdW, i * 64 * 8192 + 128); }
    float f[16]; float s = 0;
#pragma unroll
    for (int j = 0; j < 2; ++j)
#pragma unroll
      for (int e = 0; e < 4; ++e) { f[j * 8 + 2 * e] = act ? bf_lo(xv[j][e]) : 0.f; f[j * 8 + 2 * e + 1] = act ? bf_hi(xv[j][e]) : 0.f; s += f[j * 8 + 2 * e] + f[j * 8 + 2 * e + 1]; }
    s = xor_add<32>(xor_add<16>(xor_add<8>(xor_add<4>(xor_add<2>(xor_add<1>(s))))));
    const float mean = s * (1.f / DM); float q = 0;
#pragma unroll
    for (int e = 0; e < 16; ++e) { const float d = act ? f[e] - mean : 0.f; q += d * d; }
    q = xor_add<32>(xor_add<16>(xor_add<8>(xor_add<4>(xor_add<2>(xor_add<1>(q))))));
    const float mb = rbf(mean), rsd = 1.f / rbf(sqrtf(rbf(rbf(q * (1.f / DM)) + 1e-5f)));
#pragma unroll
    for (int e = 0; e < 16; e += 2) {
      const unsigned d = cvtpk(f[e] - mb, f[e + 1] - mb), n = cvtpk(bf_lo(d) * rsd, bf_hi(d) * rsd);
      f[e] = bf_lo(n); f[e + 1] = bf_hi(n);
    }
    if (wid == 0) {
      const __amdgpu_buffer_rsrc_t rg = __builtin_amdgcn_make_buffer_rsrc((void*)(sync + SY_GRAN + (b % NREP) * 2048), 0, 5120, 0x00020000);
      u32x4 gv[5];
      for (unsigned spins = 0; spins < SPIN_CAP; ++spins) {
        bool ok = true;
#pragma unroll
        for (int k = 0; k < 5; ++k) { gv[k] = ld16c(rg, (lane + 64 * k) * 16); ok &= gv[k][1] == tag && gv[k][3] == tag; }
        if (__all(ok)) break;
        __builtin_amdgcn_s_sleep(4);
      }
#pragma unroll
      for (int k = 0; k < 5; ++k) *(f32x4*)(sm + 1280 + 4 * (lane + 64 * k)) = f32x4{bf_lo(gv[k][0]), bf_hi(gv[k][0]), bf_lo(gv[k][2]), bf_hi(gv[k][2])};
    }
    lds_barrier();
    u32x4* hs = (u32x4*)(lds + (NS + 1) * SLOT);
    if (act) {
#pragma unroll
      for (int j = 0; j < 2; ++j) {
        const int k0 = (lane + 40 * j) * 8;
        const f32x4 s0 = *(const f32x4*)(sm + 1280 + k0), s1 = *(const f32x4*)(sm + 1284 + k0), c0 = *(const f32x4*)(sm + 1920 + k0), c1 = *(const f32x4*)(sm + 1924 + k0);
        const float sa[8] = {s0[0], s0[1], s0[2], s0[3], s1[0], s1[1], s1[2], s1[3]}, oc[8] = {c0[0], c0[1], c0[2], c0[3], c1[0], c1[1], c1[2], c1[3]};
        u32x4 o;
#pragma unroll
        for (int e = 0; e < 4; ++e) {
          const unsigned m = cvtpk(f[j * 8 + 2 * e] * oc[2 * e], f[j * 8 + 2 * e + 1] * oc[2 * e + 1]);
          o[e] = cvtpk(bf_lo(m) + sa[2 * e], bf_hi(m) + sa[2 * e + 1]);
        }
        hs[(lane + 40 * j) * 8 + wid] = o;
      }
    }
    lds_barrier();
    {
      const __amdgpu_buffer_rsrc_t rh = __builtin_amdgcn_make_buffer_rsrc((void*)(ws + WS_H), 0, SQ * DM * 2, 0x00020000);
      const unsigned base = (unsigned)(((b >> 1) * 80 * 16 + (b & 1) * 8) * 16);
      st16(rh, base + (tid >> 3) * 256 + (tid & 7) * 16, hs[tid]);
      if (tid < 128) st16(rh, base + (64 + (tid >> 3)) * 256 + (tid & 7) * 16, hs[512 + tid]);
    }
    publish(sync + SY_HF + b * FS, tag);
    await_flags<32>(sync + SY_HF + qt * 32 * FS, tag);
  }
  const __amdgpu_buffer_rsrc_t srdK = __builtin_amdgcn_make_buffer_rsrc((void*)(karg<const u16>(KA_KC) + ((size_t)head * SK + (SK - WIN)) * DH), 0, WIN * 256, 0x00020000);
  const __amdgpu_buffer_rsrc_t srdV = __builtin_amdgcn_make_buffer_rsrc((void*)(karg<const u16>(KA_VC) + ((size_t)head * SK + (SK - WIN)) * DH), 0, WIN * 256, 0x00020000);
  const __amdgpu_buffer_rsrc_t srdH = __builtin_amdgcn_make_buffer_rsrc((void*)(ws + WS_H + (size_t)(qt * 16 + 2 * wid) * 80 * 256), 0, 2 * 80 * 256, 0x00020000);
  const unsigned voffKV = (unsigned)((8 * wid + dr) * 256 + 64 * dc + 16 * (dx ^ dz));
  const unsigned voffH = (unsigned)(((lane >> 4) & 1) * 20480 + (lane >> 5) * 256 + (lane & 15) * 16);
  bf16x8 hf[10][4], rtf[2];
  __amdgpu_buffer_rsrc_t rsrc_u[3]; unsigned pitch_u[3], dst_u[3];
  const __amdgpu_buffer_rsrc_t srdT = __builtin_amdgcn_make_buffer_rsrc((void*)(ws + WS_WOT), 0, (int)(WS_W2T - WS_WOT) + DM * FF * 2, 0x00020000);
  auto vmem_op = [&](auto oc) __attribute__((always_inline)) {
    constexpr int op = oc.value;
    if constexpr (op >= 310) {
      constexpr int u = (op - 310) >> 1, ks = op & 1;
      __builtin_amdgcn_raw_buffer_store_b128(__builtin_bit_cast(u32x4, rtf[ks]), srdT, lane * 16 + ks * 1024, dst_u[u], SC1);
    } else if constexpr (op >= 300) {
      constexpr int u = (op - 300) >> 1, ks = op & 1;
      const int g4 = lane >> 4, chq = 2 * wid + ((lane & 3) >> 1), q4_ = (lane & 15) >> 2;
      const lds_cptr p0 = (lds_cptr)lds + rt_slot(u) + 2048 * (4 * ks + g4) + 512 * (chq >> 2) + 64 * q4_ + 16 * ((chq & 3) ^ ((2 * g4) & 3)) + 8 * (lane & 1);
      const lds_cptr p1 = (lds_cptr)lds + rt_slot(u) + 2048 * (4 * ks + g4) + 512 * (chq >> 2) + 64 * (4 + q4_) + 16 * ((chq & 3) ^ ((2 * g4 + 1) & 3)) + 8 * (lane & 1);
      rtf[ks] = trfrag(p0, p1);
    } else if constexpr (op >= 200) {
      constexpr int u = (op - 200) >> 1, half = op & 1;
      dma16(ldsw + rt_slot(u) + 1024 * half, (unsigned)(8 * wid + dr) * pitch_u[u] + (unsigned)(64 * dc + 16 * (dx ^ dz)), rsrc_u[u], 128 * half);
    } else if constexpr (op >= 100) { constexpr int i = (op - 100) >> 2, k = (op - 100) & 3; bload16c<512 * k>(hf[i][k], voffH, srdH, i * 2048); }
    else {
      constexpr int i = op >> 1, half = op & 1; constexpr unsigned so = slot_of(i) + 1024 * half;
      if constexpr (i < 10) dma16(ldsw + so, voffW, srdW, i * 64 * 8192 + 128 * half);
      else {
        constexpr bool isK = (i == 10) || (i != 25 && (i & 1)); constexpr int t = i == 10 ? 0 : (i == 25 ? 7 : (isK ? (i - 9) / 2 : (i - 12) / 2));
        dma16(ldsw + so, voffKV, isK ? srdK : srdV, t * 16384 + 128 * half);
      }
    }
  };
  static_for<SCH.npro>(LAMBDA(jc) { vmem_op(IC<SCH.pro[jc.value]>{}); });
  SBAR();
#pragma unroll
  for (int u = 0; u < 3; ++u) {
    const int id = blockIdx.x + 256 * u;
    const u16* src; int pitch, rows, kt, nt, kc, nsub; unsigned dbase;
    if (id < 320) { src = karg<const u16>(KA_WO); pitch = 1280; rows = 4096; kt = id / 5; nt = id - kt * 5; kc = 512; nsub = 40; dbase = 0; }
    else if (id < 455) { const int v = id - 320; src = karg<const u16>(KA_W2); pitch = 1280; rows = 1728; kt = v / 5; nt = v - kt * 5; kc = 216; nsub = 40; dbase = (unsigned)(WS_W2T - WS_WOT); }
    else { const int v = id < 595 ? id - 455 : 0; src = karg<const u16>(KA_W1); pitch = 3456; rows = id < 595 ? 640 : 0; kt = v / 14; nt = v - kt * 14; kc = 80; nsub = id < 595 ? 108 : 0; dbase = (unsigned)(WS_W1T - WS_WOT); }
    const int first = kt * 64 * pitch + nt * 256;
    rsrc_u[u] = __builtin_amdgcn_make_buffer_rsrc((void*)((const char*)src + first), 0, rows ? rows * pitch - first : 0, 0x00020000);
    pitch_u[u] = pitch;
    dst_u[u] = nt * 8 + wid < nsub ? dbase + ((nt * 8 + wid) * kc + kt * 8) * 256 : 0x7ff00000u;
  }
  auto vmem_gap = [&](auto gc, auto jc, auto nc) __attribute__((always_inline)) {
    constexpr int g = gc.value, j = jc.value, n = nc.value, no = SCH.nops[g];
    constexpr int i0 = (j * no + n - 1) / n, i1 = ((j + 1) * no + n - 1) / n;
    static_for<i1 - i0>(LAMBDA(uc) { vmem_op(IC<SCH.op[g][i0 + uc.value]>{}); });
  };

  const int blk = (lane >> 4) & 1, q4 = (lane & 15) >> 2, p4 = lane & 3;
  constexpr int NR = (NS * SLOT + 65535) / 65536;
  const lds_cptr L = (lds_cptr)lds;
  lds_cptr wb[NR][2], vb[NR][2], kb_[NR][2];
#pragma unroll
  for (int r = 0; r < NR; ++r)
#pragma unroll
    for (int t = 0; t < 2; ++t) {
      wb[r][t] = L + 65536 * r + 2048 * hi + 64 * (4 * t + q4) + 16 * ((2 * blk + (p4 & 1)) ^ (2 * hi + t)) + 8 * (p4 >> 1);
      PIN(wb[r][t]);
      vb[r][t] = L + 65536 * r + 64 * (4 * hi + q4) + 16 * ((2 * blk + (p4 & 1)) ^ (2 * t + hi)) + 8 * (p4 >> 1) + 2048 * t;
      kb_[r][t] = L + 65536 * r + 2048 * (r32 >> 3) + 64 * (r32 & 7) + 16 * ((2 * t + hi) ^ ((r32 >> 2) & 3));
    }
  constexpr int PD = 5, NG = 416;
  bf16x8 fr[PD + 1];
  auto ldfrag_g = [&](auto Gc) __attribute__((always_inline)) {
    constexpr int G = Gc.value;
    if constexpr (G < NG) {
      if constexpr (G < 160) {
        constexpr int i = G >> 4, ks = (G >> 2) & 3, db = G & 3, o = slot_of(i) + 4096 * ks + 512 * db;
        fr[G % (PD + 1)] = trfrag(wb[o >> 16][0] + (o & 65535), wb[o >> 16][1] + (o & 65535));
      } else {
        constexpr int u = G - 176, t = G < 176 ? 0 : (G >= 400 ? 8 : 1 + u / 32), g = G < 176 ? G - 160 : (G >= 400 ? G - 400 + 16 : u % 32);
        if constexpr (g < 16) {
          constexpr int kb = g >> 3, s = g & 7, o = slot_of(tile_K(t)) + 8192 * kb + 512 * (s >> 1);
          fr[G % (PD + 1)] = rowfrag(kb_[o >> 16][s & 1] + (o & 65535));
        } else {
          constexpr int gg = g - 16, ks = t == 8 ? (gg & 3) : (gg >> 2), db = t == 8 ? (gg >> 2) : (gg & 3), o = slot_of(tile_V(t - 1)) + 4096 * ks + 512 * db;
          fr[G % (PD + 1)] = trfrag(vb[o >> 16][0] + (o & 65535), vb[o >> 16][1] + (o & 65535));
        }
      }
    }
  };
  auto barrier = [&](auto gc) __attribute__((always_inline)) {
    constexpr int g = gc.value;
    if constexpr (g < 10) wait_bar4<SCH.wait[g]>(hf[g]); else wait_bar<SCH.wait[g]>();
    if constexpr (g == SCH.rt_done) { if (tid == 0) __hip_atomic_store((gu32*)sync + SY_RF + b, tag, RLX_AGENT); }
    SBAR();
  };

  f32x16 acc[4] = {};
  static_for<10>(LAMBDA(ic) {
    constexpr int i = ic.value;
    barrier(ic);
    if constexpr (i == 0) { static_for<PD>(LAMBDA(jc) { ldfrag_g(jc); }); SBAR(); }
    static_for<16>(LAMBDA(mc) {
      constexpr int m = mc.value, G = 16 * i + m;
      ldfrag_g(IC<G + PD>{});
      vmem_gap(ic, mc, IC<16>{});
      acc[m & 3] = mfma32(fr[G % (PD + 1)], hf[i][m >> 2], acc[m & 3]);
      SBAR();
    });
  });
  bf16x8 qf[8];
#pragma unroll
  for (int s = 0; s < 8; ++s) {
    u32x4 w;
#pragma unroll
    for (int e = 0; e < 4; ++e) w[e] = cvtpk(acc[s >> 1][8 * (s & 1) + 2 * e] * QSCALE, acc[s >> 1][8 * (s & 1) + 2 * e + 1] * QSCALE);
    qf[s] = __builtin_bit_cast(bf16x8, w);
  }
#pragma unroll
  for (int r = 0; r < NR; ++r)
#pragma unroll
    for (int t = 0; t < 2; ++t) { PIN(vb[r][t]); PIN(kb_[r][t]); }

  const int qrow = qt * 256 + wid * 32 + r32;
  const __amdgpu_buffer_rsrc_t srdO = __builtin_amdgcn_make_buffer_rsrc((void*)(ws + WS_O), 0, SQ * 4096 * 2, 0x00020000);
  const unsigned voffO = (((qrow >> 4) * 512 + head * 16 + hi) * 16 + (qrow & 15)) * 16;
  f32x16 o[4] = {}; f32x16 sA[2], sB[2], negm; u32x4 pw[4];
  float mref, lsum = 0.f; bool bad = false;
  barrier(IC<10>{});
  {
    const f32x16 zero = {};
    static_for<16>(LAMBDA(gc) {
      constexpr int g = gc.value, G = 160 + g;
      ldfrag_g(IC<G + PD>{});
      vmem_gap(IC<10>{}, gc, IC<16>{});
      sA[g >> 3] = mfma32(fr[G % (PD + 1)], qf[g & 7], (g & 7) == 0 ? zero : sA[g >> 3]);
      SBAR();
    });
    float a = max3(sA[0][0], sA[0][1], sA[1][0]), b = max3(sA[0][2], sA[0][3], sA[1][1]); a = max3(a, sA[1][2], sA[1][3]);
#pragma unroll
    for (int r = 4; r < 16; r += 4) { a = max3(a, sA[0][r], sA[0][r + 1]); b = max3(b, sA[0][r + 2], sA[0][r + 3]); a = max3(a, sA[1][r], sA[1][r + 1]); b = max3(b, sA[1][r + 2], sA[1][r + 3]); }
    mref = swap_max(__builtin_fmaxf(a, b)) + PBIAS;
#pragma unroll
    for (int r = 0; r < 16; ++r) negm[r] = -mref;
    PIN(negm);
    sA[0][0] = __builtin_amdgcn_exp2f(sA[0][0] - mref); sA[0][1] = __builtin_amdgcn_exp2f(sA[0][1] - mref);
    SBAR();
  }
  auto step = [&](auto tc, f32x16(&C)[2], f32x16(&P)[2]) __attribute__((always_inline)) {
    constexpr int t = tc.value;
    constexpr int G0 = 176 + 32 * (t - 1), GB = t < 8 ? G0 + 16 : 400;
    barrier(IC<10 + t>{});
    float sacc = 0.f;
    static_for<16>(LAMBDA(gc) {
      constexpr int g = gc.value;
      if constexpr (t < 8) {
        ldfrag_g(IC<G0 + g + PD>{});
        vmem_gap(IC<10 + t>{}, gc, IC<32>{});
        C[g >> 3] = mfma32(fr[(G0 + g) % (PD + 1)], qf[g & 7], (g & 7) == 0 ? negm : C[g >> 3]);
      }
      if constexpr (t == 1 && g < 15) {
        constexpr int e1 = 2 * (g + 1);
        P[e1 >> 4][e1 & 15] = __builtin_amdgcn_exp2f(P[e1 >> 4][e1 & 15] - mref); P[e1 >> 4][(e1 & 15) + 1] = __builtin_amdgcn_exp2f(P[e1 >> 4][(e1 & 15) + 1] - mref);
        PIN(P[e1 >> 4]);
      }
      constexpr int ks = g >> 2, w = g & 3, e = 8 * (ks & 1) + 2 * w;
      pw[ks][w] = cvtpk(P[ks >> 1][e], P[ks >> 1][e + 1]);
      sacc += P[ks >> 1][e]; sacc += P[ks >> 1][e + 1];
      PIN(pw[ks]); PIN(sacc);
      SBAR();
    });
    bad |= !(sacc <= PLIM);
    lsum += sacc;
    if constexpr (t < 8) {
      static_for<16>(LAMBDA(gc) {
        constexpr int g = gc.value;
        ldfrag_g(IC<GB + g + PD>{});
        vmem_gap(IC<10 + t>{}, IC<16 + g>{}, IC<32>{});
        o[g & 3] = mfma32(fr[(GB + g) % (PD + 1)], __builtin_bit_cast(bf16x8, pw[g >> 2]), o[g & 3]);
        C[g >> 3][(2 * g) & 15] = __builtin_amdgcn_exp2f(C[g >> 3][(2 * g) & 15]); C[g >> 3][(2 * g + 1) & 15] = __builtin_amdgcn_exp2f(C[g >> 3][(2 * g + 1) & 15]);
        PIN(C[g >> 3]);
        SBAR();
      });
    }
  };
  auto out_db = [&](auto dc, float inv) __attribute__((always_inline)) {
    constexpr int db = dc.value;
#pragma unroll
    for (int s = 0; s < 2; ++s) {
      u32x4 w;
#pragma unroll
      for (int e = 0; e < 4; ++e) w[e] = cvtpk(o[db][8 * s + 2 * e] * inv, o[db][8 * s + 2 * e + 1] * inv);
      __builtin_amdgcn_raw_buffer_store_b128(w, srdO, voffO + (4 * db + 2 * s) * 256, 0, SC1);
    }
  };
  step(IC<1>{}, sB, sA);
  step(IC<2>{}, sA, sB);
  step(IC<3>{}, sB, sA);
  step(IC<4>{}, sA, sB);
  step(IC<5>{}, sB, sA);
  step(IC<6>{}, sA, sB);
  step(IC<7>{}, sB, sA);
  step(IC<8>{}, sA, sB);
  if (__builtin_expect(!__any(bad), 1)) {
    const float inv = 1.f / swap_add(lsum);
    static_for<16>(LAMBDA(gc) {
      constexpr int g = gc.value;
      ldfrag_g(IC<400 + g + PD>{});
      o[g >> 2] = mfma32(fr[(400 + g) % (PD + 1)], __builtin_bit_cast(bf16x8, pw[g & 3]), o[g >> 2]);
      if constexpr (g >= 5 && (g & 3) == 1) out_db(IC<(g >> 2) - 1>{}, inv);
      SBAR();
    });
    out_db(IC<3>{}, inv);
  } else {
    const unsigned reg = (unsigned)((wid == 0 ? 0 : wid + 1) * SLOT);
    static_assert(slot_of(tile_V(7)) == SLOT, "");
    const lds_cptr kbs[2] = {kb_[0][0] + reg, kb_[0][1] + reg}, vbs[2] = {vb[0][0] + reg + 8192, vb[0][1] + reg + 8192};
    float m = -__builtin_inff(), l = 0.f;
#pragma unroll
    for (int d = 0; d < 4; ++d) o[d] = f32x16{};
#pragma unroll 1
    for (int kb = 0; kb < WIN / 32; ++kb) {
#pragma unroll
      for (int R = 0; R < 4; ++R) {
        const unsigned vo = (unsigned)((8 * R + dr) * 256 + 64 * dc + 16 * (dx ^ ((2 * R + (dr >> 2)) & 3)));
        const unsigned so = __builtin_amdgcn_readfirstlane(kb * 8192), ld = __builtin_amdgcn_readfirstlane(lds0 + reg + 2048 * R);
        asm volatile("s_nop 4" ::: "memory");
        dma16(ld, vo, srdK, so); dma16(ld + 1024, vo + 128, srdK, so); dma16(ld + 8192, vo, srdV, so); dma16(ld + 9216, vo + 128, srdV, so);
      }
      asm volatile("s_waitcnt vmcnt(0)" ::: "memory");
      f32x16 sc = {};
#pragma unroll
      for (int s = 0; s < 8; ++s) { sc = mfma32(rowfrag(kbs[s & 1] + 512 * (s >> 1)), qf[s], sc); SBAR(); }
      float mx = sc[0];
#pragma unroll
      for (int r = 1; r < 16; ++r) mx = __builtin_fmaxf(mx, sc[r]);
      const float mn = __builtin_fmaxf(m, swap_max(mx)), al = __builtin_amdgcn_exp2f(m - mn);
      m = mn; l *= al;
#pragma unroll
      for (int r = 0; r < 16; ++r) { sc[r] = __builtin_amdgcn_exp2f(sc[r] - mn); l += sc[r]; }
#pragma unroll
      for (int d = 0; d < 4; ++d)
#pragma unroll
        for (int r = 0; r < 16; ++r) o[d][r] *= al;
#pragma unroll
      for (int ks = 0; ks < 2; ++ks) {
        const u32x4 pk = {cvtpk(sc[8 * ks], sc[8 * ks + 1]), cvtpk(sc[8 * ks + 2], sc[8 * ks + 3]), cvtpk(sc[8 * ks + 4], sc[8 * ks + 5]), cvtpk(sc[8 * ks + 6], sc[8 * ks + 7])};
#pragma unroll
        for (int d = 0; d < 4; ++d) { o[d] = mfma32(trfrag(vbs[0] + 4096 * ks + 512 * d, vbs[1] + 4096 * ks + 512 * d), __builtin_bit_cast(bf16x8, pk), o[d]); SBAR(); }
      }
      asm volatile("s_waitcnt lgkmcnt(0)" ::: "memory");
    }
    const float inv = 1.f / swap_add(l);
    static_for<4>(LAMBDA(dc_) { out_db(dc_, inv); });
  }
  const int rt = (xcd >> 2) * 8 + (idx >> 2), gi = (xcd & 3) * 4 + (idx & 3);
  {
    unsigned rf[4] = {tag, tag, tag, tag};
    if (wid == 0) {
#pragma unroll
      for (int k = 0; k < 4; ++k) rf[k] = __hip_atomic_load((gu32*)sync + SY_RF + lane + 64 * k, RLX_AGENT);
    }
    asm volatile("s_waitcnt vmcnt(0)" ::: "memory");
    if (wid == 0) {
      for (unsigned spins = 0; spins < SPIN_CAP; ++spins) {
        if (__all(rf[0] == tag && rf[1] == tag && rf[2] == tag && rf[3] == tag)) break;
#pragma unroll
        for (int k = 0; k < 4; ++k) rf[k] = __hip_atomic_load((gu32*)sync + SY_RF + lane + 64 * k, RLX_AGENT);
      }
    }
    asm volatile("" ::: "memory");
    __syncthreads();
    if (tid == 0) __hip_atomic_store((gu32*)sync + SY_OF + (qt * 32 + head) * FS, tag, RLX_AGENT);
  }
  unsigned* const c1 = sync + SY_CF + rt * 16 * FS, * const c2 = c1 + 256 * FS, * const c3 = c1 + 512 * FS;
  const u16* const mod = (const u16*)(ws + WS_MOD);
  const __amdgpu_buffer_rsrc_t rmod = __builtin_amdgcn_make_buffer_rsrc((void*)mod, 0, 3840 * 2, 0x00020000);
  u16 *const obuf = (u16*)(ws + WS_O), *const wot = (u16*)(ws + WS_WOT), *const w1t = (u16*)(ws + WS_W1T), *const w2t = (u16*)(ws + WS_W2T);
  u16 *const x1 = (u16*)(ws + WS_X1), *const hid = (u16*)(ws + WS_HID), *const h2 = (u16*)(ws + WS_H2), *const part = (u16*)(ws + WS_P);
  const int nft = gi < 15 ? 7 : 3;
  const __amdgpu_buffer_rsrc_t raD = __builtin_amdgcn_make_buffer_rsrc((void*)(w1t + (size_t)(7 * gi) * 80 * 128), 0, nft * 80 * 256, 0x00020000);
  RingA<4, 7, 20, 80, 5> RD; RD.init(lds0, wid, lane);
  {
    const int kq = gi >> 2, j = gi & 3, qb = wid & 3;
    const __amdgpu_buffer_rsrc_t ra = __builtin_amdgcn_make_buffer_rsrc((void*)(wot + ((size_t)(10 * j) * 512 + kq * 128) * 128), 0, 10 * 512 * 256, 0x00020000);
    const __amdgpu_buffer_rsrc_t rb = __builtin_amdgcn_make_buffer_rsrc((void*)(obuf + ((size_t)(8 * rt + 2 * qb) * 512 + kq * 128) * 128), 0, 2 * 512 * 256, 0x00020000);
    const __amdgpu_buffer_rsrc_t rp = __builtin_amdgcn_make_buffer_rsrc((void*)part, 0, 4 * SQ * DM * 2, 0x00020000);
    f32x4 acc[10][2] = {};
    RingA<4, 10, 32, 512, 4> RA; RA.init(lds0, wid, lane);
    RA.pre<0, 1>(ra);
    await_flags<8>(sync + SY_OF + ((rt >> 1) * 32 + kq * 8) * FS, tag);
    ring_gemm<4, 10, 32, 512, 512, 1, 4, 8>(acc, RA, ra, rb, lds, wid, lane, [&](auto ic) __attribute__((always_inline)) { RD.template issue<ic.value / 4, ic.value % 4>(raD); });
    pair_reduce<10>(acc, lds + 81920, wid, lane);
    const int nt0 = wid < 4 ? 0 : 5;
    const unsigned vo = ((((8 * rt + 2 * qb + ((lane >> 4) & 1)) * 80 + j * 20 + nt0 * 2 + (lane >> 5)) * 4 + kq) * 16 + (lane & 15)) * 16;
    auto outp = [&](auto ntc) __attribute__((always_inline)) {
      constexpr int nt = ntc.value, o = nt % 5;
      const u32x2 w0 = {cvtpk(acc[nt][0][0], acc[nt][0][1]), cvtpk(acc[nt][0][2], acc[nt][0][3])}, w1 = {cvtpk(acc[nt][1][0], acc[nt][1][1]), cvtpk(acc[nt][1][2], acc[nt][1][3])};
      st16(rp, vo + o * 2 * 1024, pair16(w0, w1));
    };
    if (wid < 4) { asm volatile("; lo-out" ::: "memory"); static_for<5>(LAMBDA(c) { outp(c); }); asm volatile("; lo-end" ::: "memory"); }
    else { asm volatile("; hi-out" ::: "memory"); static_for<5>(LAMBDA(c) { outp(IC<5 + c.value>{}); }); asm volatile("; hi-end" ::: "memory"); }
  }
  publish(c1 + gi * FS, tag);
  {
    float* red = (float*)(lds + 98304);
    const int r = tid & 7, row = rt * 128 + gi * 8 + r;
    const bool two = wid < 2;
    const __amdgpu_buffer_rsrc_t rp = __builtin_amdgcn_make_buffer_rsrc((void*)part, 0, 4 * SQ * DM * 2, 0x00020000);
    const __amdgpu_buffer_rsrc_t rx1 = __builtin_amdgcn_make_buffer_rsrc((void*)x1, 0, SQ * DM * 2, 0x00020000);
    const __amdgpu_buffer_rsrc_t rh2 = __builtin_amdgcn_make_buffer_rsrc((void*)h2, 0, SQ * DM * 2, 0x00020000);
    const u16* x = karg<const u16>(KA_X);
    u32x4 p[2][4], xv[2], gv[2], sbv[2], cbv[2]; unsigned blk[2], pblk[2];
#pragma unroll
    for (int u = 0; u < 2; ++u) {
      const int c = (u == 0 || two) ? (tid >> 3) + 64 * u : (tid >> 3);
      blk[u] = (((row >> 4) * 80 + c) * 16 + (row & 15)) * 16; pblk[u] = (((row >> 4) * 80 + c) * 64 + (row & 15)) * 16;
      xv[u] = *(const u32x4*)(x + (size_t)row * DM + c * 8);
      gv[u] = ld16c(rmod, (2 * 640 + c * 8) * 2); sbv[u] = ld16c(rmod, (3 * 640 + c * 8) * 2); cbv[u] = ld16c(rmod, (4 * 640 + c * 8) * 2);
    }
    RD.pre<2, 3>(raD);
    if (b == 119 && wid == 7) {
      bool ok = false;
      for (unsigned spins = 0; spins < SPIN_CAP && !ok; ++spins) {
        bool all = true;
#pragma unroll
        for (int k = 0; k < 4; ++k) all &= __hip_atomic_load((gu32*)sync + SY_STARTED + lane + 64 * k, RLX_AGENT) == tag;
        ok = __all(all);
      }
      if (lane == 0) __hip_atomic_store((gu32*)sync, tag, RLX_AGENT);
    }
    await_flags<16>(c1, tag);
#pragma unroll
    for (int k = 0; k < 4; ++k) p[0][k] = ld16c(rp, pblk[0] + k * 256);
    if (two) {
#pragma unroll
      for (int k = 0; k < 4; ++k) p[1][k] = ld16c(rp, pblk[1] + k * 256);
    }
    float v[2][8]; float s = 0; u32x4 x1v[2];
    auto resid = [&](auto uc) __attribute__((always_inline)) {
      constexpr int u = uc.value;
#pragma unroll
      for (int e = 0; e < 4; ++e) {
        const float a = (bf_lo(p[u][0][e]) + bf_lo(p[u][1][e])) + (bf_lo(p[u][2][e]) + bf_lo(p[u][3][e])), b = (bf_hi(p[u][0][e]) + bf_hi(p[u][1][e])) + (bf_hi(p[u][2][e]) + bf_hi(p[u][3][e]));
        x1v[u][e] = cvtpk(__builtin_fmaf(bf_lo(gv[u][e]), a, bf_lo(xv[u][e])), __builtin_fmaf(bf_hi(gv[u][e]), b, bf_hi(xv[u][e])));
        v[u][2 * e] = bf_lo(x1v[u][e]); v[u][2 * e + 1] = bf_hi(x1v[u][e]); s += v[u][2 * e] + v[u][2 * e + 1];
      }
    };
    resid(IC<0>{});
    if (two) resid(IC<1>{});
    s = xor_add<32>(xor_add<16>(xor_add<8>(s)));
    if (lane < 8) red[wid * 8 + r] = s;
    lds_barrier();
    float tot = 0;
#pragma unroll
    for (int i = 0; i < 8; ++i) tot += red[i * 8 + r];
    const float mean = tot * (1.f / DM); float q = 0;
#pragma unroll
    for (int e = 0; e < 8; ++e) { v[0][e] -= mean; q += v[0][e] * v[0][e]; }
    if (two) {
#pragma unroll
      for (int e = 0; e < 8; ++e) { v[1][e] -= mean; q += v[1][e] * v[1][e]; }
    }
    q = xor_add<32>(xor_add<16>(xor_add<8>(q)));
    if (lane < 8) red[64 + wid * 8 + r] = q;
    lds_barrier();
    float qt = 0;
#pragma unroll
    for (int i = 0; i < 8; ++i) qt += red[64 + i * 8 + r];
    const float rstd = __builtin_amdgcn_rsqf(qt * (1.f / DM) + 1e-5f);
    auto norm = [&](auto uc) __attribute__((always_inline)) {
      constexpr int u = uc.value;
      u32x4 hv;
#pragma unroll
      for (int e = 0; e < 4; ++e)
        hv[e] = cvtpk(__builtin_fmaf(v[u][2 * e] * rstd, bf_lo(cbv[u][e]), bf_lo(sbv[u][e])), __builtin_fmaf(v[u][2 * e + 1] * rstd, bf_hi(cbv[u][e]), bf_hi(sbv[u][e])));
      st16(rh2, blk[u], hv); st16(rx1, blk[u], x1v[u]);
    };
    norm(IC<0>{});
    if (two) norm(IC<1>{});
  }
  publish(c2 + gi * FS, tag);
  const int jE = gi & 7, rtE = rt * 2 + (gi >> 3);
  const __amdgpu_buffer_rsrc_t raE = __builtin_amdgcn_make_buffer_rsrc((void*)(w2t + (size_t)(5 * jE) * 216 * 128), 0, 5 * 216 * 256, 0x00020000);
  RingA<2, 5, 54, 216, 4> RE; RE.init(lds0, wid, lane);
  {
    const int qb = wid & 3, nt0 = wid < 4 ? 0 : 4;
    const __amdgpu_buffer_rsrc_t rb = __builtin_amdgcn_make_buffer_rsrc((void*)(h2 + (size_t)(8 * rt + 2 * qb) * 80 * 128), 0, 2 * 80 * 256, 0x00020000);
    const __amdgpu_buffer_rsrc_t rh = __builtin_amdgcn_make_buffer_rsrc((void*)hid, 0, SQ * FF * 2, 0x00020000);
    const u16* b1 = karg<const u16>(KA_B1);
    u32x2 bv[4];
#pragma unroll
    for (int o = 0; o < 4; ++o) bv[o] = *(const u32x2*)(b1 + (nt0 + o < nft ? (7 * gi + nt0 + o) * 16 + 4 * (lane >> 4) : 0));
    f32x4 acc[7][2] = {};
    await_flags<16>(c2, tag);
    ring_gemm<4, 7, 20, 80, 80, 3, 5, 10>(acc, RD, raD, rb, lds, wid, lane, [&](auto ic) __attribute__((always_inline)) { RE.template issue<ic.value / 5, ic.value % 5>(raE); });
    pair_reduce<7>(acc, lds + 98304, wid, lane);
    const unsigned vo = (((8 * rt + 2 * qb + ((lane >> 4) & 1)) * 216 + (7 * gi + nt0) * 2 + (lane >> 5)) * 16 + (lane & 15)) * 16;
    auto outp = [&](auto ntc, auto oc) __attribute__((always_inline)) {
      constexpr int nt = ntc.value, o = oc.value;
      if (nt < nft) {
        const float b0 = bf_lo(bv[o][0]), b1_ = bf_hi(bv[o][0]), b2_ = bf_lo(bv[o][1]), b3 = bf_hi(bv[o][1]);
        u32x2 w[2];
#pragma unroll
        for (int q = 0; q < 2; ++q) {
          const float v0 = rbf(__builtin_fmaxf(rbf(acc[nt][q][0]) + b0, 0.f)), v1 = rbf(__builtin_fmaxf(rbf(acc[nt][q][1]) + b1_, 0.f));
          const float v2 = rbf(__builtin_fmaxf(rbf(acc[nt][q][2]) + b2_, 0.f)), v3 = rbf(__builtin_fmaxf(rbf(acc[nt][q][3]) + b3, 0.f));
          w[q] = u32x2{cvtpk(v0 * v0, v1 * v1), cvtpk(v2 * v2, v3 * v3)};
        }
        st16(rh, vo + o * 2 * 256, pair16(w[0], w[1]));
      }
    };
    if (wid < 4) { asm volatile("; lo-out" ::: "memory"); static_for<4>(LAMBDA(c) { outp(c, c); }); asm volatile("; lo-end" ::: "memory"); }
    else { asm volatile("; hi-out" ::: "memory"); static_for<3>(LAMBDA(c) { outp(IC<4 + c.value>{}, c); }); asm volatile("; hi-end" ::: "memory"); }
  }
  publish(c3 + gi * FS, tag);
  {
    const int qb = wid & 1;
    const __amdgpu_buffer_rsrc_t rb = __builtin_amdgcn_make_buffer_rsrc((void*)(hid + (size_t)(4 * rtE + 2 * qb) * 216 * 128), 0, 2 * 216 * 256, 0x00020000);
    const __amdgpu_buffer_rsrc_t rx1 = __builtin_amdgcn_make_buffer_rsrc((void*)x1, 0, SQ * DM * 2, 0x00020000);
    const u16* b2 = karg<const u16>(KA_B2);
    u16* out = karg<u16>(KA_OUT);
    u32x4 xv[2], gv[2], bv[2];
#pragma unroll
    for (int it = 0; it < 2; ++it) {
      const int item = tid + it * 512, q = item & 63, c = item < 640 ? item >> 6 : 0, row = rtE * 64 + q;
      xv[it] = ld16c(rx1, (((row >> 4) * 80 + jE * 10 + c) * 16 + (row & 15)) * 16);
      gv[it] = ld16c(rmod, (5 * 640 + jE * 80 + c * 8) * 2); bv[it] = *(const u32x4*)(b2 + jE * 80 + c * 8);
    }
    f32x4 acc[5][2] = {};
    RE.pre<2, 3>(raE);
    await_flags<16>(c3, tag);
    ring_gemm<2, 5, 54, 216, 216, 3, 4, 0>(acc, RE, raE, rb, lds, wid, lane, [&](auto) {});
    f32x4* red = (f32x4*)lds;
    constexpr int HI = (3 * 40960 - 4 * 10240) / 16;
    {
      f32x4* mine = red + (wid * 10) * 64 + (wid >= 4 ? HI : 0) + lane;
#pragma unroll
      for (int n = 0; n < 5; ++n) { mine[(n * 2) * 64] = acc[n][0]; mine[(n * 2 + 1) * 64] = acc[n][1]; }
    }
    lds_barrier();
#pragma unroll
    for (int it = 0; it < 2; ++it) {
      const int item = tid + it * 512;
      if (item < 640) {
        const int q = item & 63, c = item >> 6, n = c >> 1, l0 = (q & 15) + 32 * (c & 1), base = (((q >> 5) * 5 + n) * 2 + ((q >> 4) & 1)) * 64 + l0;
        f32x4 lo = {0, 0, 0, 0}, hi = {0, 0, 0, 0};
#pragma unroll
        for (int w = 0; w < 4; ++w) { const int o_ = base + w * 2 * 5 * 2 * 64 + (w >= 2 ? HI : 0); lo += red[o_]; hi += red[o_ + 16]; }
        const float v[8] = {lo[0], lo[1], lo[2], lo[3], hi[0], hi[1], hi[2], hi[3]};
        u32x4 o;
#pragma unroll
        for (int e = 0; e < 4; ++e) {
          const float a = bf_lo(xv[it][e]) + rbf(bf_lo(gv[it][e]) * rbf(rbf(v[2 * e]) + bf_lo(bv[it][e]))), b = bf_hi(xv[it][e]) + rbf(bf_hi(gv[it][e]) * rbf(rbf(v[2 * e + 1]) + bf_hi(bv[it][e])));
          o[e] = cvtpk(a, b);
        }
        *(u32x4*)(out + (size_t)(rtE * 64 + q) * DM + jE * 80 + c * 8) = o;
      }
    }
  }
}

extern "C" void kernel_launch(void* const* d_in, const int* in_sizes, int n_in, void* d_out, int out_size, void* d_ws, size_t ws_size, hipStream_t stream) {
  char* ws = (char*)d_ws;
  (void)hipFuncSetAttribute((const void*)k_block, hipFuncAttributeMaxDynamicSharedMemorySize, 163840);
  k_block<<<256, 512, 163840, stream>>>((const u16*)d_in[0], (const u16*)d_in[1], (const u16*)d_in[2], (const u16*)d_in[3], (const u16*)d_in[4], (const u16*)d_in[6], (const u16*)d_in[7],
                                        (const u16*)d_in[8], (const u16*)d_in[9], (const u16*)d_in[10], (const u16*)d_in[11], (u16*)d_out, ws);
}
```

```cpp
#include <hip/hip_runtime.h>
#include <stdint.h>
#include <utility>

typedef unsigned short u16;
typedef short bf16x8 __attribute__((ext_vector_type(8)));
typedef short s16x4 __attribute__((ext_vector_type(4)));
typedef float f32x16 __attribute__((ext_vector_type(16)));
typedef float f32x4 __attribute__((ext_vector_type(4)));
typedef float f32x2 __attribute__((ext_vector_type(2)));
typedef __bf16 bf16x2 __attribute__((ext_vector_type(2)));
typedef unsigned u32x4 __attribute__((ext_vector_type(4)));
typedef unsigned u32x2 __attribute__((ext_vector_type(2)));
#define DI __device__ __forceinline__
#define GAS __attribute__((address_space(1)))
#define LAS __attribute__((address_space(3)))
#define SBAR() __builtin_amdgcn_sched_barrier(0)
#define PIN(x) asm volatile("" : "+v"(x))

constexpr int SQ = 2048, DM = 640, NH = 32, DH = 128, SK = 2048, WIN = 512, FF = 1728;
constexpr size_t WS_MOD = 0;
constexpr size_t WS_SYNC = 32768;
constexpr size_t WS_H = 524288;
constexpr size_t WS_O = WS_H + (size_t)SQ * DM * 2;
constexpr size_t WS_WOT = WS_O + (size_t)SQ * 4096 * 2;
constexpr size_t WS_W1T = WS_WOT + (size_t)4096 * DM * 2;
constexpr size_t WS_W2T = WS_W1T + (size_t)DM * FF * 2;
constexpr size_t WS_X1 = WS_W2T + (size_t)DM * FF * 2 + 65536;
constexpr size_t WS_HID = WS_X1 + (size_t)SQ * DM * 2;
constexpr size_t WS_H2 = WS_HID + (size_t)SQ * FF * 2 + 65536;
constexpr size_t WS_P = WS_H2 + (size_t)SQ * DM * 2;
constexpr size_t WS_END = WS_P + (size_t)4 * SQ * DM * 2;

template <int... I, class F> DI void static_for_impl(std::integer_sequence<int, I...>, F&& f) { (f(std::integral_constant<int, I>{}), ...); }
template <int N, class F> DI void static_for(F&& f) { static_for_impl(std::make_integer_sequence<int, N>{}, f); }
#define LAMBDA(ic) [&](auto ic) __attribute__((always_inline))

DI float bf_lo(unsigned w) { return __uint_as_float(w << 16); }
DI float bf_hi(unsigned w) { return __uint_as_float(w & 0xffff0000u); }
DI unsigned cvtpk(float lo, float hi) { f32x2 v = {lo, hi}; bf16x2 b = __builtin_convertvector(v, bf16x2); return __builtin_bit_cast(unsigned, b); }
DI float rbf(float x) { return bf_lo(cvtpk(x, 0.f)); }
DI f32x16 mfma32(bf16x8 a, bf16x8 b, f32x16 c) { return __builtin_amdgcn_mfma_f32_32x32x16_bf16(a, b, c, 0, 0, 0); }
DI f32x4 mfma16(bf16x8 a, bf16x8 b, f32x4 c) { return __builtin_amdgcn_mfma_f32_16x16x32_bf16(a, b, c, 0, 0, 0); }
DI float swap_max(float m) { auto r = __builtin_amdgcn_permlane32_swap(__float_as_uint(m), __float_as_uint(m), false, false); return __builtin_fmaxf(__uint_as_float(r[0]), __uint_as_float(r[1])); }
DI float swap_add(float m) { auto r = __builtin_amdgcn_permlane32_swap(__float_as_uint(m), __float_as_uint(m), false, false); return __uint_as_float(r[0]) + __uint_as_float(r[1]); }

DI void lds_barrier() { asm volatile("s_waitcnt lgkmcnt(0)\n\ts_barrier" ::: "memory"); }
template <int CTRL> DI float dpp_mov(float v) { return __builtin_bit_cast(float, __builtin_amdgcn_update_dpp(0, __builtin_bit_cast(int, v), CTRL, 0xf, 0xf, true)); }
template <int M> DI float xor_add(float v) {
  if constexpr (M == 1) return v + dpp_mov<0xB1>(v);
  else if constexpr (M == 2) return v + dpp_mov<0x4E>(v);
  else if constexpr (M == 4) return v + dpp_mov<0x141>(dpp_mov<0x1B>(v));
  else if constexpr (M == 8) return v + dpp_mov<0x128>(v);
  else if constexpr (M == 16) { const auto r = __builtin_amdgcn_permlane16_swap(__float_as_uint(v), __float_as_uint(v), false, false); return __uint_as_float(r[0]) + __uint_as_float(r[1]); }
  else { const auto r = __builtin_amdgcn_permlane32_swap(__float_as_uint(v), __float_as_uint(v), false, false); return __uint_as_float(r[0]) + __uint_as_float(r[1]); }
}
typedef GAS unsigned gu32;
typedef GAS unsigned long long gu64;
#define RLX_AGENT __ATOMIC_RELAXED, __HIP_MEMORY_SCOPE_AGENT
constexpr unsigned SPIN_CAP = 1u << 18;
constexpr int SC1 = 16;
constexpr int FS = 32;
constexpr int SY_STARTED = 64, SY_RF = 512, SY_GRAN = 1024  , SY_HF = 36864, SY_OF = SY_HF + 256 * FS, SY_CF = SY_OF + 256 * FS  ;
constexpr int NREP = 8;
DI void st16(__amdgpu_buffer_rsrc_t r, unsigned voff, u32x4 v) { __builtin_amdgcn_raw_buffer_store_b128(v, r, voff, 0, SC1); }
DI u32x4 ld16c(__amdgpu_buffer_rsrc_t r, unsigned voff) { return __builtin_amdgcn_raw_buffer_load_b128(r, voff, 0, SC1); }
DI void publish(unsigned* flag, unsigned tag) {
  asm volatile("s_waitcnt vmcnt(0)" ::: "memory");
  __syncthreads();
  if (threadIdx.x == 0) __hip_atomic_store((gu32*)flag, tag, RLX_AGENT);
}
template <int N> DI void await_flags(const unsigned* flags, unsigned tag) {
  if (threadIdx.x < 64) {
    gu32* p = (gu32*)flags + (threadIdx.x < N ? threadIdx.x : 0) * FS;
    unsigned a = __hip_atomic_load(p, RLX_AGENT); __builtin_amdgcn_s_sleep(2);
    unsigned b = __hip_atomic_load(p, RLX_AGENT);
    for (unsigned spins = 0; spins < SPIN_CAP; ++spins) {
      if (__all(a == tag)) break;
      a = __hip_atomic_load(p, RLX_AGENT); __builtin_amdgcn_s_sleep(2);
      if (__all(b == tag)) break;
      b = __hip_atomic_load(p, RLX_AGENT); __builtin_amdgcn_s_sleep(2);
    }
  }
  asm volatile("" ::: "memory");
  __syncthreads();
}
template <class T> DI T* karg(int i) {
  const __attribute__((address_space(4))) char* p = (const __attribute__((address_space(4))) char*)__builtin_amdgcn_kernarg_segment_ptr();
  asm volatile("" : "+s"(p));
  return *(T* const __attribute__((address_space(4)))*)(p + 8 * i);
}
enum { KA_X = 0, KA_KC, KA_VC, KA_WQ, KA_WO, KA_W1, KA_B1, KA_W2, KA_B2, KA_C, KA_WMOD, KA_OUT, KA_WS };

DI u32x4 pair16(u32x2 x, u32x2 y) {
  const auto a = __builtin_amdgcn_permlane16_swap(x[0], y[0], false, false), b = __builtin_amdgcn_permlane16_swap(x[1], y[1], false, false);
  return u32x4{a[0], b[0], a[1], b[1]};
}
constexpr int NS = 8, SLOT = 16384;
constexpr float QSCALE = 0.088388347648318440f * 1.4426950408889634f;
constexpr float PBIAS = 96.f;
#define PLIM_V 1073741824.f
constexpr float PLIM = PLIM_V;
constexpr int tile_K(int t) { return t == 0 ? 10 : 9 + 2 * t; }
constexpr int tile_V(int t) { return t == 7 ? 25 : 12 + 2 * t; }
constexpr int slot_of(int idx) { return (idx % NS) * SLOT; }
constexpr int lastuse(int idx) { return idx < 10 ? idx : (idx == 10 ? 10 : (idx == 25 ? 18 : ((idx & 1) ? 10 + (idx - 9) / 2 : 11 + (idx - 12) / 2))); }
constexpr int need_bar(int idx) {
  if (idx < 10) return idx == 0 ? 0 : idx - 1;
  if (idx == 10) return 9;
  if (idx == 25) return 17;
  return (idx & 1) ? 10 + (idx - 9) / 2 - 1 : 11 + (idx - 12) / 2;
}
constexpr int rt_step(int u) { return u == 0 ? 9 : (u == 1 ? 10 : 12); }
constexpr int rt_proc(int u) { return u == 0 ? 11 : (u == 1 ? 12 : 14); }
constexpr int rt_slot(int u) { return (u == 1 ? NS + 1 : NS) * SLOT; }
struct Sched { int npro; int pro[16]; int nops[19]; int op[19][16]; int wait[19]; int rt_done; };
constexpr Sched make_sched() {
  Sched p{}; int pos = 0; int tile_end[26] = {}; int h_end[10] = {}; int u_end[3] = {}; int next_tile = 3;
  auto pro_h = [&](int i) { for (int k = 0; k < 4; ++k) p.pro[p.npro++] = 100 + 4 * i + k; pos += 4; h_end[i] = pos; };
  pro_h(0); pro_h(1);
  tile_end[0] = tile_end[1] = tile_end[2] = 0; int last_rt_store = 1 << 20; p.rt_done = -1;
  for (int g = 0; g < 19; ++g) {
    int need = 0;
    for (int idx = 0; idx < next_tile; ++idx) if (need_bar(idx) <= g && tile_end[idx] > need) need = tile_end[idx];
    if (g < 10 && h_end[g] > need) need = h_end[g];
    for (int u = 0; u < 3; ++u) if (rt_proc(u) <= g && u_end[u] > need) need = u_end[u];
    p.wait[g] = pos - need;
    if (p.rt_done < 0 && need >= last_rt_store) p.rt_done = g;
    int n = 0;
    for (int u = 0; u < 3; ++u) if (g == rt_proc(u)) { p.op[g][n++] = 300 + 2 * u; p.op[g][n++] = 301 + 2 * u; }
    if (g == 0) { for (int k = 0; k < 4; ++k) p.op[g][n++] = 100 + 4 * 2 + k; pos += 4; h_end[2] = pos; }
    const int maxt = g < 7 ? 1 : 2;
    for (int c = 0; c < maxt && next_tile < 26; ++c) {
      if (next_tile >= NS && lastuse(next_tile - NS) >= g) break;
      p.op[g][n++] = 2 * next_tile; p.op[g][n++] = 2 * next_tile + 1; pos += 2; tile_end[next_tile] = pos; ++next_tile;
    }
    if (g + 3 < 10) { for (int k = 0; k < 4; ++k) p.op[g][n++] = 100 + 4 * (g + 3) + k; pos += 4; h_end[g + 3] = pos; }
    for (int u = 0; u < 3; ++u) if (g == rt_step(u)) { p.op[g][n++] = 200 + 2 * u; p.op[g][n++] = 201 + 2 * u; pos += 2; u_end[u] = pos; }
    for (int u = 0; u < 3; ++u) if (g == rt_proc(u)) { p.op[g][n++] = 310 + 2 * u; p.op[g][n++] = 311 + 2 * u; pos += 2; if (u == 2) last_rt_store = pos; }
    p.nops[g] = n;
  }
  for (int idx = 3; idx < 26; ++idx) if (tile_end[idx] == 0) p.wait[0] = -1;
  if (p.rt_done < 0) p.wait[0] = -1;
  return p;
}
constexpr Sched SCH = make_sched();
static_assert(SCH.wait[0] >= 0, "schedule incomplete");
constexpr bool sched_ok() {
  int step_of[26] = {}; for (int i = 0; i < 26; ++i) step_of[i] = -1;
  for (int g = 0; g < 19; ++g) for (int j = 0; j < SCH.nops[g]; ++j) if (SCH.op[g][j] < 100) step_of[SCH.op[g][j] >> 1] = g;
  for (int i = 3; i < 26; ++i) if (step_of[i] >= need_bar(i)) return false;
  return true;
}
static_assert(sched_ok(), "a tile is issued too late");

DI void dma16(unsigned ldsaddr, unsigned voff, __amdgpu_buffer_rsrc_t srd, unsigned soff) {
  asm volatile("s_mov_b32 m0, %0\n\ts_nop 0\n\tbuffer_load_dwordx4 %1, %2, %3 offen lds" ::"s"(ldsaddr), "v"(voff), "s"(srd), "s"(soff) : "memory");
}
template <int OFF> DI void bload16c(bf16x8& dst, unsigned voff, __amdgpu_buffer_rsrc_t srd, unsigned soff) {
  asm volatile("buffer_load_dwordx4 %0, %1, %2, %3 offen offset:%4 sc1" : "=v"(dst) : "v"(voff), "s"(srd), "s"(soff), "i"(OFF) : "memory");
}
DI void dma16c(unsigned ldsaddr, unsigned voff, __amdgpu_buffer_rsrc_t srd, unsigned soff) {
  asm volatile("s_mov_b32 m0, %0\n\ts_nop 0\n\tbuffer_load_dwordx4 %1, %2, %3 offen sc1 lds" ::"s"(ldsaddr), "v"(voff), "s"(srd), "s"(soff) : "memory");
}
typedef LAS const char* lds_cptr;
typedef short v4i16_t __attribute__((ext_vector_type(4)));
DI s16x4 vtr(lds_cptr p) { return __builtin_bit_cast(s16x4, __builtin_amdgcn_ds_read_tr16_b64_v4i16((LAS v4i16_t*)p)); }
DI bf16x8 trfrag(lds_cptr p0, lds_cptr p1) { const s16x4 lo = vtr(p0), hi = vtr(p1); return __builtin_shufflevector(lo, hi, 0, 1, 2, 3, 4, 5, 6, 7); }
DI bf16x8 rowfrag(lds_cptr p) { return *(LAS const bf16x8*)p; }
DI float max3(float a, float b, float c) { return __builtin_fmaxf(__builtin_fmaxf(a, b), c); }
template <int N> DI void wait_bar4(bf16x8 (&b)[4]) {
  asm volatile("s_waitcnt vmcnt(%4)\n\ts_barrier" : "+v"(b[0]), "+v"(b[1]), "+v"(b[2]), "+v"(b[3]) : "i"(N) : "memory");
}
template <int N> DI void wait_bar() { asm volatile("s_waitcnt vmcnt(%0)\n\ts_barrier" ::"i"(N) : "memory"); }
template <int V> using IC = std::integral_constant<int, V>;

template <int N> DI void wait_bar4b(bf16x8 (&b)[2][2]) {
  asm volatile("s_waitcnt vmcnt(%4)\n\ts_barrier" : "+v"(b[0][0]), "+v"(b[0][1]), "+v"(b[1][0]), "+v"(b[1][1]) : "i"(N) : "memory");
}
template <int NQB, int NT, int KS> struct Ring {
  static constexpr int KSP = 8 / NQB, SK = 2 * KSP, NSTG = (KS + SK - 1) / SK, NP = SK * NT, PA = (NP + 7) / 8, SB = PA * 8 * 1024, FR = 2 * NT;
};
template <int NQB, int NT, int KS, int KC_A, int NSLOT> struct RingA {
  using R = Ring<NQB, NT, KS>;
  unsigned voff[R::PA]; unsigned ldsw;
  DI void init(unsigned lds0, int wid, int lane) {
    ldsw = lds0 + wid * 1024;
#pragma unroll
    for (int i = 0; i < R::PA; ++i) { const int p = i * 8 + wid, ks = p / NT, nt = p - ks * NT; voff[i] = p < R::NP ? (unsigned)(nt * KC_A * 256 + ks * 1024 + lane * 16) : 0x7ff00000u; }
  }
  template <int S, int I> DI void issue(__amdgpu_buffer_rsrc_t ra) const { dma16c(ldsw + ((S % NSLOT) * R::SB + I * 8192), voff[I], ra, S * R::SK * 1024); }
  template <int S0, int S1> DI void pre(__amdgpu_buffer_rsrc_t ra) const { static_for<S1 - S0>(LAMBDA(sc) { static_for<R::PA>(LAMBDA(ic) { this->template issue<S0 + sc.value, ic.value>(ra); }); }); }
};
template <int NQB, int NT, int KS, int KC_A, int KC_B, int NPRE, int NSLOT, int NHOOK, class HOOK>
DI void ring_gemm(f32x4 (&acc)[NT][2], const RingA<NQB, NT, KS, KC_A, NSLOT>& RA, __amdgpu_buffer_rsrc_t ra, __amdgpu_buffer_rsrc_t rb, const char* lds, int wid, int lane, HOOK&& hook) {
  using R = Ring<NQB, NT, KS>;
  constexpr int SK = R::SK, NSTG = R::NSTG, PA = R::PA, FR = R::FR, PD = 5;
  static_assert(NSTG >= 3 && FR > PD + 1, "");
  const int kq = wid / NQB;
  bf16x8 B[3][2][2];
  const unsigned voffB = lane * 16 + 2 * kq * 1024;
  auto issueA1 = [&](auto sc, auto ic) __attribute__((always_inline)) { RA.template issue<sc.value, ic.value>(ra); };
  auto issueB1 = [&](auto sc, auto ic) __attribute__((always_inline)) {
    constexpr int s = sc.value, i = ic.value, ks = i >> 1, q = i & 1;
    bload16c<0>(B[s % 3][ks][q], voffB, rb, q * KC_B * 256 + (s * SK + ks) * 1024);
  };
  if constexpr (NPRE == 0) static_for<PA>(LAMBDA(ic) { issueA1(IC<0>{}, ic); });
  static_for<4>(LAMBDA(ic) { issueB1(IC<0>{}, ic); });
  if constexpr (NPRE > 1) { static_for<4>(LAMBDA(ic) { issueB1(IC<1>{}, ic); }); wait_bar4b<4>(B[0]); } else wait_bar4b<0>(B[0]);
  const LAS bf16x8* pl = (const LAS bf16x8*)lds + lane + 2 * kq * NT * 64;
  bf16x8 A[PD + 1];
  auto rd = [&](auto mc) __attribute__((always_inline)) { constexpr int M = mc.value, s = M / FR, m = M % FR; A[M % (PD + 1)] = pl[((s % NSLOT) * PA * 8 + m) * 64]; };
  static_for<PD>(LAMBDA(jc) { rd(jc); });
  SBAR();
  static_for<NSTG>(LAMBDA(sc) {
    constexpr int s = sc.value;
    constexpr int n0 = (NPRE <= 1 && s == 0) ? PA + 4 : 0;
    constexpr int sa = s + 2;
    constexpr int nA = (sa < NSTG && sa >= NPRE) ? PA : 0, nB = (s + 2 < NSTG) ? 4 : 0, NI = n0 + nA + nB, NGI = FR - PD - 1;
    constexpr bool full = (s + 1) * SK <= KS;
    static_for<FR>(LAMBDA(mc) {
      constexpr int m = mc.value, M = s * FR + m, ks = m / NT, nt = m % NT;
      if constexpr (m == FR - PD && s + 1 < NSTG) wait_bar4b<nA + nB>(B[(s + 1) % 3]);
      if constexpr (M + PD < NSTG * FR) rd(IC<M + PD>{});
      if constexpr (m < NGI) {
        constexpr int i0 = (m * NI + NGI - 1) / NGI, i1 = ((m + 1) * NI + NGI - 1) / NGI;
        static_for<i1 - i0>(LAMBDA(uc) {
          constexpr int i = i0 + uc.value;
          if constexpr (i < n0) { if constexpr (i < PA) issueA1(IC<1>{}, IC<i>{}); else issueB1(IC<1>{}, IC<i - PA>{}); }
          else if constexpr (i - n0 < nA) issueA1(IC<sa>{}, IC<i - n0>{});
          else issueB1(IC<s + 2>{}, IC<i - n0 - nA>{});
        });
      }
      if constexpr (NHOOK > 0 && M >= (NSTG - 1) * FR - PD) {
        constexpr int j = M - ((NSTG - 1) * FR - PD), HG = FR + PD - 2, i0 = (j * NHOOK + HG - 1) / HG, i1 = ((j + 1) * NHOOK + HG - 1) / HG;
        if constexpr (j < HG) static_for<i1 - i0>(LAMBDA(uc) { hook(IC<i0 + uc.value>{}); });
      }
      if (full || s * SK + 2 * kq + ks < KS) { acc[nt][0] = mfma16(A[M % (PD + 1)], B[s % 3][ks][0], acc[nt][0]); acc[nt][1] = mfma16(A[M % (PD + 1)], B[s % 3][ks][1], acc[nt][1]); }
      SBAR();
    });
  });
}
template <int NT> DI void pair_reduce(f32x4 (&acc)[NT][2], char* lds, int wid, int lane) {
  constexpr int H0 = (NT + 1) / 2;
  f32x4* red = (f32x4*)lds + (size_t)wid * H0 * 2 * 64 + lane;
  const f32x4* in = (const f32x4*)lds + (size_t)(wid ^ 4) * H0 * 2 * 64 + lane;
  lds_barrier();
  if (wid < 4) {
    asm volatile("; lo-send" ::: "memory");
#pragma unroll
    for (int nt = H0; nt < NT; ++nt) { red[((nt - H0) * 2) * 64] = acc[nt][0]; red[((nt - H0) * 2 + 1) * 64] = acc[nt][1]; }
    asm volatile("; lo-sent" ::: "memory");
  } else {
    asm volatile("; hi-send" ::: "memory");
#pragma unroll
    for (int nt = 0; nt < H0; ++nt) { red[(nt * 2) * 64] = acc[nt][0]; red[(nt * 2 + 1) * 64] = acc[nt][1]; }
    asm volatile("; hi-sent" ::: "memory");
  }
  lds_barrier();
  if (wid < 4) {
    asm volatile("; lo-recv" ::: "memory");
#pragma unroll
    for (int nt = 0; nt < H0; ++nt) { acc[nt][0] += in[(nt * 2) * 64]; acc[nt][1] += in[(nt * 2 + 1) * 64]; PIN(acc[nt][0]); PIN(acc[nt][1]); }
    asm volatile("; lo-done" ::: "memory");
  } else {
    asm volatile("; hi-recv" ::: "memory");
#pragma unroll
    for (int nt = H0; nt < NT; ++nt) { acc[nt][0] += in[((nt - H0) * 2) * 64]; acc[nt][1] += in[((nt - H0) * 2 + 1) * 64]; PIN(acc[nt][0]); PIN(acc[nt][1]); }
    asm volatile("; hi-done" ::: "memory");
  }
}

__global__ __launch_bounds__(512, 2) void k_block(const u16* a_x, const u16* a_kc, const u16* a_vc, const u16* a_wq, const u16* a_wo, const u16* a_w1, const u16* a_b1, const u16* a_w2,
                                                   const u16* a_b2, const u16* a_c, const u16* a_wmod, u16* a_out, char* a_ws) {
  extern __shared__ __attribute__((aligned(16))) char lds[];
  const int tid = threadIdx.x, lane = tid & 63, r32 = lane & 31, hi = lane >> 5;
  const int wid = __builtin_amdgcn_readfirstlane(tid >> 6);
  const int b = blockIdx.x, xcd = b & 7, idx = b >> 3;
  const int head = (xcd & 3) * 8 + (idx & 7), qt = (xcd >> 2) * 4 + (idx >> 3);
  const unsigned lds0 = (unsigned)(uintptr_t)lds;
  char* const ws = karg<char>(KA_WS);
  unsigned* const sync = (unsigned*)(ws + WS_SYNC);
  const __amdgpu_buffer_rsrc_t srdW = __builtin_amdgcn_make_buffer_rsrc((void*)(karg<const u16>(KA_WQ) + head * DH), 0, (DM - 1) * 8192 + 256, 0x00020000);
  const int dr = (lane >> 2) & 7, dx = lane & 3, dc = lane >> 5, dz = (2 * wid + (dr >> 2)) & 3;
  const unsigned voffW = (unsigned)((8 * wid + dr) * 8192 + 64 * dc + 16 * (dx ^ dz));
  const unsigned ldsw = lds0 + 2048 * wid;
  unsigned tag;
  {
    float* sm = (float*)(lds + NS * SLOT);
    const u16* x = karg<const u16>(KA_X);
    const int r = tid & 7, row = b * 8 + r; const bool two = wid < 2;
    u32x4 w[5]; unsigned cv = 0;
    const int cc = tid & 3, r0 = tid >> 2;
    if (b < 120) {
      const u16* wmod = karg<const u16>(KA_WMOD);
#pragma unroll
      for (int i = 0; i < 5; ++i) w[i] = *(const u32x4*)(wmod + (size_t)(r0 + 128 * i) * 3840 + b * 32 + cc * 8);
      cv = *(const unsigned*)(karg<const u16>(KA_C) + 2 * (tid < 320 ? tid : 0));
    }
    u32x4 xv[2];
    xv[0] = *(const u32x4*)(x + (size_t)row * DM + (tid >> 3) * 8); xv[1] = *(const u32x4*)(x + (size_t)row * DM + ((two ? 64 : 0) + (tid >> 3)) * 8);
    unsigned tg = __hip_atomic_load((gu32*)sync, RLX_AGENT) + 1u;
    if (b < 120 && tid < 320) {
      const float v0 = bf_lo(cv), v1 = bf_hi(cv);
      const unsigned pk = cvtpk(v0 * __builtin_amdgcn_rcpf(1.f + __expf(-v0)), v1 * __builtin_amdgcn_rcpf(1.f + __expf(-v1)));
      *(f32x2*)(sm + 512 + 2 * tid) = f32x2{bf_lo(pk), bf_hi(pk)};
    }
    PIN(tg);
    tag = __builtin_amdgcn_readfirstlane(tg);
    lds_barrier();
    if (tid == 0) __hip_atomic_store((gu32*)sync + SY_STARTED + b, tag, RLX_AGENT);
    if (b < 120) {
      float a[8] = {};
#pragma unroll
      for (int i = 0; i < 5; ++i) {
        const float sv = sm[512 + r0 + 128 * i];
#pragma unroll
        for (int j = 0; j < 4; ++j) { a[2 * j] += sv * bf_lo(w[i][j]); a[2 * j + 1] += sv * bf_hi(w[i][j]); }
      }
      float v4[4], v2[2];
#pragma unroll
      for (int j = 0; j < 4; ++j) { const auto r = __builtin_amdgcn_permlane32_swap(__float_as_uint(a[j]), __float_as_uint(a[j + 4]), false, false); v4[j] = __uint_as_float(r[0]) + __uint_as_float(r[1]); }
#pragma unroll
      for (int j = 0; j < 2; ++j) { const auto r = __builtin_amdgcn_permlane16_swap(__float_as_uint(v4[j]), __float_as_uint(v4[j + 2]), false, false); v2[j] = xor_add<8>(xor_add<4>(__uint_as_float(r[0]) + __uint_as_float(r[1]))); }
      if ((lane & 12) == 0) *(f32x2*)(sm + wid * 32 + cc * 8 + 4 * (lane >> 5) + 2 * ((lane >> 4) & 1)) = f32x2{v2[0], v2[1]};
      lds_barrier();
      {
        const int u = tid & 15;
        float lo = 0.f, hi_ = 0.f;
#pragma unroll
        for (int w8 = 0; w8 < 8; ++w8) { const f32x2 t2 = *(const f32x2*)(sm + w8 * 32 + 2 * u); lo += t2[0]; hi_ += t2[1]; }
        unsigned pk = cvtpk(lo, hi_);
        if ((b >= 20 && b < 40) || (b >= 80 && b < 100)) pk = cvtpk(1.f + bf_lo(pk), 1.f + bf_hi(pk));
        if (b < 40) { if (tid < 16 * NREP) __hip_atomic_store((gu64*)(sync + SY_GRAN + (tid >> 4) * 2048) + b * 16 + u, ((unsigned long long)tag << 32) | pk, RLX_AGENT); }
        else if (tid < 16) __hip_atomic_store((gu32*)(ws + WS_MOD) + b * 16 + u, pk, RLX_AGENT);
      }
    }
#pragma unroll
    for (int i = 0; i < 3; ++i) { dma16(ldsw + slot_of(i), voffW, srdW, i * 64 * 8192); dma16(ldsw + slot_of(i) + 1024, voffW, srdW, i * 64 * 8192 + 128); }
    float* red = sm + 256;
    float f[2][8]; float s = 0;
#pragma unroll
    for (int e = 0; e < 4; ++e) { f[0][2 * e] = bf_lo(xv[0][e]); f[0][2 * e + 1] = bf_hi(xv[0][e]); f[1][2 * e] = bf_lo(xv[1][e]); f[1][2 * e + 1] = bf_hi(xv[1][e]); s += f[0][2 * e] + f[0][2 * e + 1]; }
    if (two) {
#pragma unroll
      for (int e = 0; e < 8; ++e) s += f[1][e];
    }
    s = xor_add<32>(xor_add<16>(xor_add<8>(s)));
    if (lane < 8) red[wid * 8 + r] = s;
    lds_barrier();
    float tot = 0;
#pragma unroll
    for (int i = 0; i < 8; ++i) tot += red[i * 8 + r];
    const float mean = tot * (1.f / DM); float q = 0;
#pragma unroll
    for (int e = 0; e < 8; ++e) { const float d = f[0][e] - mean; q += d * d; }
    if (two) {
#pragma unroll
      for (int e = 0; e < 8; ++e) { const float d = f[1][e] - mean; q += d * d; }
    }
    q = xor_add<32>(xor_add<16>(xor_add<8>(q)));
    if (lane < 8) red[64 + wid * 8 + r] = q;
    lds_barrier();
    float qt_ = 0;
#pragma unroll
    for (int i = 0; i < 8; ++i) qt_ += red[64 + i * 8 + r];
    const float mb = rbf(mean), rsd = 1.f / rbf(sqrtf(rbf(rbf(qt_ * (1.f / DM)) + 1e-5f)));
#pragma unroll
    for (int u = 0; u < 2; ++u)
#pragma unroll
      for (int e = 0; e < 8; e += 2) {
        const unsigned d = cvtpk(f[u][e] - mb, f[u][e + 1] - mb), n = cvtpk(bf_lo(d) * rsd, bf_hi(d) * rsd);
        f[u][e] = bf_lo(n); f[u][e + 1] = bf_hi(n);
      }
    if (wid == 0) {
      const __amdgpu_buffer_rsrc_t rg = __builtin_amdgcn_make_buffer_rsrc((void*)(sync + SY_GRAN + (b % NREP) * 2048), 0, 5120, 0x00020000);
      u32x4 gv[5];
      for (unsigned spins = 0; spins < SPIN_CAP; ++spins) {
        bool ok = true;
#pragma unroll
        for (int k = 0; k < 5; ++k) { gv[k] = ld16c(rg, (lane + 64 * k) * 16); ok &= gv[k][1] == tag && gv[k][3] == tag; }
        if (__all(ok)) break;
        __builtin_amdgcn_s_sleep(4);
      }
#pragma unroll
      for (int k = 0; k < 5; ++k) *(f32x4*)(sm + 1280 + 4 * (lane + 64 * k)) = f32x4{bf_lo(gv[k][0]), bf_hi(gv[k][0]), bf_lo(gv[k][2]), bf_hi(gv[k][2])};
    }
    lds_barrier();
    {
      const __amdgpu_buffer_rsrc_t rh = __builtin_amdgcn_make_buffer_rsrc((void*)(ws + WS_H), 0, SQ * DM * 2, 0x00020000);
      const unsigned base = (unsigned)(((b >> 1) * 80 * 16 + (b & 1) * 8 + r) * 16);
      auto apply = [&](auto uc) __attribute__((always_inline)) {
        constexpr int u = uc.value;
        const int c = (tid >> 3) + 64 * u, k0 = c * 8;
        const f32x4 s0 = *(const f32x4*)(sm + 1280 + k0), s1 = *(const f32x4*)(sm + 1284 + k0), c0 = *(const f32x4*)(sm + 1920 + k0), c1 = *(const f32x4*)(sm + 1924 + k0);
        const float sa[8] = {s0[0], s0[1], s0[2], s0[3], s1[0], s1[1], s1[2], s1[3]}, oc[8] = {c0[0], c0[1], c0[2], c0[3], c1[0], c1[1], c1[2], c1[3]};
        u32x4 o;
#pragma unroll
        for (int e = 0; e < 4; ++e) {
          const unsigned m = cvtpk(f[u][2 * e] * oc[2 * e], f[u][2 * e + 1] * oc[2 * e + 1]);
          o[e] = cvtpk(bf_lo(m) + sa[2 * e], bf_hi(m) + sa[2 * e + 1]);
        }
        st16(rh, base + c * 256, o);
      };
      apply(IC<0>{});
      if (two) apply(IC<1>{});
    }
    publish(sync + SY_HF + b * FS, tag);
    await_flags<32>(sync + SY_HF + qt * 32 * FS, tag);
  }
  const __amdgpu_buffer_rsrc_t srdK = __builtin_amdgcn_make_buffer_rsrc((void*)(karg<const u16>(KA_KC) + ((size_t)head * SK + (SK - WIN)) * DH), 0, WIN * 256, 0x00020000);
  const __amdgpu_buffer_rsrc_t srdV = __builtin_amdgcn_make_buffer_rsrc((void*)(karg<const u16>(KA_VC) + ((size_t)head * SK + (SK - WIN)) * DH), 0, WIN * 256, 0x00020000);
  const __amdgpu_buffer_rsrc_t srdH = __builtin_amdgcn_make_buffer_rsrc((void*)(ws + WS_H + (size_t)(qt * 16 + 2 * wid) * 80 * 256), 0, 2 * 80 * 256, 0x00020000);
  const unsigned voffKV = (unsigned)((8 * wid + dr) * 256 + 64 * dc + 16 * (dx ^ dz));
  const unsigned voffH = (unsigned)(((lane >> 4) & 1) * 20480 + (lane >> 5) * 256 + (lane & 15) * 16);
  bf16x8 hf[10][4], rtf[2];
  __amdgpu_buffer_rsrc_t rsrc_u[3]; unsigned pitch_u[3], dst_u[3];
  const __amdgpu_buffer_rsrc_t srdT = __builtin_amdgcn_make_buffer_rsrc((void*)(ws + WS_WOT), 0, (int)(WS_W2T - WS_WOT) + DM * FF * 2, 0x00020000);
  auto vmem_op = [&](auto oc) __attribute__((always_inline)) {
    constexpr int op = oc.value;
    if constexpr (op >= 310) {
      constexpr int u = (op - 310) >> 1, ks = op & 1;
      __builtin_amdgcn_raw_buffer_store_b128(__builtin_bit_cast(u32x4, rtf[ks]), srdT, lane * 16 + ks * 1024, dst_u[u], SC1);
    } else if constexpr (op >= 300) {
      constexpr int u = (op - 300) >> 1, ks = op & 1;
      const int g4 = lane >> 4, chq = 2 * wid + ((lane & 3) >> 1), q4_ = (lane & 15) >> 2;
      const lds_cptr p0 = (lds_cptr)lds + rt_slot(u) + 2048 * (4 * ks + g4) + 512 * (chq >> 2) + 64 * q4_ + 16 * ((chq & 3) ^ ((2 * g4) & 3)) + 8 * (lane & 1);
      const lds_cptr p1 = (lds_cptr)lds + rt_slot(u) + 2048 * (4 * ks + g4) + 512 * (chq >> 2) + 64 * (4 + q4_) + 16 * ((chq & 3) ^ ((2 * g4 + 1) & 3)) + 8 * (lane & 1);
      rtf[ks] = trfrag(p0, p1);
    } else if constexpr (op >= 200) {
      constexpr int u = (op - 200) >> 1, half = op & 1;
      dma16(ldsw + rt_slot(u) + 1024 * half, (unsigned)(8 * wid + dr) * pitch_u[u] + (unsigned)(64 * dc + 16 * (dx ^ dz)), rsrc_u[u], 128 * half);
    } else if constexpr (op >= 100) { constexpr int i = (op - 100) >> 2, k = (op - 100) & 3; bload16c<512 * k>(hf[i][k], voffH, srdH, i * 2048); }
    else {
      constexpr int i = op >> 1, half = op & 1; constexpr unsigned so = slot_of(i) + 1024 * half;
      if constexpr (i < 10) dma16(ldsw + so, voffW, srdW, i * 64 * 8192 + 128 * half);
      else {
        constexpr bool isK = (i == 10) || (i != 25 && (i & 1)); constexpr int t = i == 10 ? 0 : (i == 25 ? 7 : (isK ? (i - 9) / 2 : (i - 12) / 2));
        dma16(ldsw + so, voffKV, isK ? srdK : srdV, t * 16384 + 128 * half);
      }
    }
  };
  static_for<SCH.npro>(LAMBDA(jc) { vmem_op(IC<SCH.pro[jc.value]>{}); });
  SBAR();
#pragma unroll
  for (int u = 0; u < 3; ++u) {
    const int id = blockIdx.x + 256 * u;
    const u16* src; int pitch, rows, kt, nt, kc, nsub; unsigned dbase;
    if (id < 320) { src = karg<const u16>(KA_WO); pitch = 1280; rows = 4096; kt = id / 5; nt = id - kt * 5; kc = 512; nsub = 40; dbase = 0; }
    else if (id < 455) { const int v = id - 320; src = karg<const u16>(KA_W2); pitch = 1280; rows = 1728; kt = v / 5; nt = v - kt * 5; kc = 216; nsub = 40; dbase = (unsigned)(WS_W2T - WS_WOT); }
    else { const int v = id < 595 ? id - 455 : 0; src = karg<const u16>(KA_W1); pitch = 3456; rows = id < 595 ? 640 : 0; kt = v / 14; nt = v - kt * 14; kc = 80; nsub = id < 595 ? 108 : 0; dbase = (unsigned)(WS_W1T - WS_WOT); }
    const int first = kt * 64 * pitch + nt * 256;
    rsrc_u[u] = __builtin_amdgcn_make_buffer_rsrc((void*)((const char*)src + first), 0, rows ? rows * pitch - first : 0, 0x00020000);
    pitch_u[u] = pitch;
    dst_u[u] = nt * 8 + wid < nsub ? dbase + ((nt * 8 + wid) * kc + kt * 8) * 256 : 0x7ff00000u;
  }
  auto vmem_gap = [&](auto gc, auto jc, auto nc) __attribute__((always_inline)) {
    constexpr int g = gc.value, j = jc.value, n = nc.value, no = SCH.nops[g];
    constexpr int i0 = (j * no + n - 1) / n, i1 = ((j + 1) * no + n - 1) / n;
    static_for<i1 - i0>(LAMBDA(uc) { vmem_op(IC<SCH.op[g][i0 + uc.value]>{}); });
  };

  const int blk = (lane >> 4) & 1, q4 = (lane & 15) >> 2, p4 = lane & 3;
  constexpr int NR = (NS * SLOT + 65535) / 65536;
  const lds_cptr L = (lds_cptr)lds;
  lds_cptr wb[NR][2], vb[NR][2], kb_[NR][2];
#pragma unroll
  for (int r = 0; r < NR; ++r)
#pragma unroll
    for (int t = 0; t < 2; ++t) {
      wb[r][t] = L + 65536 * r + 2048 * hi + 64 * (4 * t + q4) + 16 * ((2 * blk + (p4 & 1)) ^ (2 * hi + t)) + 8 * (p4 >> 1);
      PIN(wb[r][t]);
      vb[r][t] = L + 65536 * r + 64 * (4 * hi + q4) + 16 * ((2 * blk + (p4 & 1)) ^ (2 * t + hi)) + 8 * (p4 >> 1) + 2048 * t;
      kb_[r][t] = L + 65536 * r + 2048 * (r32 >> 3) + 64 * (r32 & 7) + 16 * ((2 * t + hi) ^ ((r32 >> 2) & 3));
    }
  constexpr int PD = 5, NG = 416;
  bf16x8 fr[PD + 1];
  auto ldfrag_g = [&](auto Gc) __attribute__((always_inline)) {
    constexpr int G = Gc.value;
    if constexpr (G < NG) {
      if constexpr (G < 160) {
        constexpr int i = G >> 4, ks = (G >> 2) & 3, db = G & 3, o = slot_of(i) + 4096 * ks + 512 * db;
        fr[G % (PD + 1)] = trfrag(wb[o >> 16][0] + (o & 65535), wb[o >> 16][1] + (o & 65535));
      } else {
        constexpr int u = G - 176, t = G < 176 ? 0 : (G >= 400 ? 8 : 1 + u / 32), g = G < 176 ? G - 160 : (G >= 400 ? G - 400 + 16 : u % 32);
        if constexpr (g < 16) {
          constexpr int kb = g >> 3, s = g & 7, o = slot_of(tile_K(t)) + 8192 * kb + 512 * (s >> 1);
          fr[G % (PD + 1)] = rowfrag(kb_[o >> 16][s & 1] + (o & 65535));
        } else {
          constexpr int gg = g - 16, ks = t == 8 ? (gg & 3) : (gg >> 2), db = t == 8 ? (gg >> 2) : (gg & 3), o = slot_of(tile_V(t - 1)) + 4096 * ks + 512 * db;
          fr[G % (PD + 1)] = trfrag(vb[o >> 16][0] + (o & 65535), vb[o >> 16][1] + (o & 65535));
        }
      }
    }
  };
  auto barrier = [&](auto gc) __attribute__((always_inline)) {
    constexpr int g = gc.value;
    if constexpr (g < 10) wait_bar4<SCH.wait[g]>(hf[g]); else wait_bar<SCH.wait[g]>();
    if constexpr (g == SCH.rt_done) { if (tid == 0) __hip_atomic_store((gu32*)sync + SY_RF + b, tag, RLX_AGENT); }
    SBAR();
  };

  f32x16 acc[4] = {};
  static_for<10>(LAMBDA(ic) {
    constexpr int i = ic.value;
    barrier(ic);
    if constexpr (i == 0) { static_for<PD>(LAMBDA(jc) { ldfrag_g(jc); }); SBAR(); }
    static_for<16>(LAMBDA(mc) {
      constexpr int m = mc.value, G = 16 * i + m;
      ldfrag_g(IC<G + PD>{});
      vmem_gap(ic, mc, IC<16>{});
      acc[m & 3] = mfma32(fr[G % (PD + 1)], hf[i][m >> 2], acc[m & 3]);
      SBAR();
    });
  });
  bf16x8 qf[8];
#pragma unroll
  for (int s = 0; s < 8; ++s) {
    u32x4 w;
#pragma unroll
    for (int e = 0; e < 4; ++e) w[e] = cvtpk(acc[s >> 1][8 * (s & 1) + 2 * e] * QSCALE, acc[s >> 1][8 * (s & 1) + 2 * e + 1] * QSCALE);
    qf[s] = __builtin_bit_cast(bf16x8, w);
  }
#pragma unroll
  for (int r = 0; r < NR; ++r)
#pragma unroll
    for (int t = 0; t < 2; ++t) { PIN(vb[r][t]); PIN(kb_[r][t]); }

  const int qrow = qt * 256 + wid * 32 + r32;
  const __amdgpu_buffer_rsrc_t srdO = __builtin_amdgcn_make_buffer_rsrc((void*)(ws + WS_O), 0, SQ * 4096 * 2, 0x00020000);
  const unsigned voffO = (((qrow >> 4) * 512 + head * 16 + hi) * 16 + (qrow & 15)) * 16;
  f32x16 o[4] = {}; f32x16 sA[2], sB[2], negm; u32x4 pw[4];
  float mref, lsum = 0.f; bool bad = false;
  barrier(IC<10>{});
  {
    const f32x16 zero = {};
    static_for<16>(LAMBDA(gc) {
      constexpr int g = gc.value, G = 160 + g;
      ldfrag_g(IC<G + PD>{});
      vmem_gap(IC<10>{}, gc, IC<16>{});
      sA[g >> 3] = mfma32(fr[G % (PD + 1)], qf[g & 7], (g & 7) == 0 ? zero : sA[g >> 3]);
      SBAR();
    });
    float a = max3(sA[0][0], sA[0][1], sA[1][0]), b = max3(sA[0][2], sA[0][3], sA[1][1]); a = max3(a, sA[1][2], sA[1][3]);
#pragma unroll
    for (int r = 4; r < 16; r += 4) { a = max3(a, sA[0][r], sA[0][r + 1]); b = max3(b, sA[0][r + 2], sA[0][r + 3]); a = max3(a, sA[1][r], sA[1][r + 1]); b = max3(b, sA[1][r + 2], sA[1][r + 3]); }
    mref = swap_max(__builtin_fmaxf(a, b)) + PBIAS;
#pragma unroll
    for (int r = 0; r < 16; ++r) negm[r] = -mref;
    PIN(negm);
    sA[0][0] = __builtin_amdgcn_exp2f(sA[0][0] - mref); sA[0][1] = __builtin_amdgcn_exp2f(sA[0][1] - mref);
    SBAR();
  }
  auto step = [&](auto tc, f32x16(&C)[2], f32x16(&P)[2]) __attribute__((always_inline)) {
    constexpr int t = tc.value;
    constexpr int G0 = 176 + 32 * (t - 1), GB = t < 8 ? G0 + 16 : 400;
    barrier(IC<10 + t>{});
    float sacc = 0.f;
    static_for<16>(LAMBDA(gc) {
      constexpr int g = gc.value;
      if constexpr (t < 8) {
        ldfrag_g(IC<G0 + g + PD>{});
        vmem_gap(IC<10 + t>{}, gc, IC<32>{});
        C[g >> 3] = mfma32(fr[(G0 + g) % (PD + 1)], qf[g & 7], (g & 7) == 0 ? negm : C[g >> 3]);
      }
      if constexpr (t == 1 && g < 15) {
        constexpr int e1 = 2 * (g + 1);
        P[e1 >> 4][e1 & 15] = __builtin_amdgcn_exp2f(P[e1 >> 4][e1 & 15] - mref); P[e1 >> 4][(e1 & 15) + 1] = __builtin_amdgcn_exp2f(P[e1 >> 4][(e1 & 15) + 1] - mref);
        PIN(P[e1 >> 4]);
      }
      constexpr int ks = g >> 2, w = g & 3, e = 8 * (ks & 1) + 2 * w;
      pw[ks][w] = cvtpk(P[ks >> 1][e], P[ks >> 1][e + 1]);
      sacc += P[ks >> 1][e]; sacc += P[ks >> 1][e + 1];
      PIN(pw[ks]); PIN(sacc);
      SBAR();
    });
    bad |= !(sacc <= PLIM);
    lsum += sacc;
    if constexpr (t < 8) {
      static_for<16>(LAMBDA(gc) {
        constexpr int g = gc.value;
        ldfrag_g(IC<GB + g + PD>{});
        vmem_gap(IC<10 + t>{}, IC<16 + g>{}, IC<32>{});
        o[g & 3] = mfma32(fr[(GB + g) % (PD + 1)], __builtin_bit_cast(bf16x8, pw[g >> 2]), o[g & 3]);
        C[g >> 3][(2 * g) & 15] = __builtin_amdgcn_exp2f(C[g >> 3][(2 * g) & 15]); C[g >> 3][(2 * g + 1) & 15] = __builtin_amdgcn_exp2f(C[g >> 3][(2 * g + 1) & 15]);
        PIN(C[g >> 3]);
        SBAR();
      });
    }
  };
  auto out_db = [&](auto dc, float inv) __attribute__((always_inline)) {
    constexpr int db = dc.value;
#pragma unroll
    for (int s = 0; s < 2; ++s) {
      u32x4 w;
#pragma unroll
      for (int e = 0; e < 4; ++e) w[e] = cvtpk(o[db][8 * s + 2 * e] * inv, o[db][8 * s + 2 * e + 1] * inv);
      __builtin_amdgcn_raw_buffer_store_b128(w, srdO, voffO + (4 * db + 2 * s) * 256, 0, SC1);
    }
  };
  step(IC<1>{}, sB, sA);
  step(IC<2>{}, sA, sB);
  step(IC<3>{}, sB, sA);
  step(IC<4>{}, sA, sB);
  step(IC<5>{}, sB, sA);
  step(IC<6>{}, sA, sB);
  step(IC<7>{}, sB, sA);
  step(IC<8>{}, sA, sB);
  if (__builtin_expect(!__any(bad), 1)) {
    const float inv = 1.f / swap_add(lsum);
    static_for<16>(LAMBDA(gc) {
      constexpr int g = gc.value;
      ldfrag_g(IC<400 + g + PD>{});
      o[g >> 2] = mfma32(fr[(400 + g) % (PD + 1)], __builtin_bit_cast(bf16x8, pw[g & 3]), o[g >> 2]);
      if constexpr (g >= 5 && (g & 3) == 1) out_db(IC<(g >> 2) - 1>{}, inv);
      SBAR();
    });
    out_db(IC<3>{}, inv);
  } else {
    const unsigned reg = (unsigned)((wid == 0 ? 0 : wid + 1) * SLOT);
    static_assert(slot_of(tile_V(7)) == SLOT, "");
    const lds_cptr kbs[2] = {kb_[0][0] + reg, kb_[0][1] + reg}, vbs[2] = {vb[0][0] + reg + 8192, vb[0][1] + reg + 8192};
    float m = -__builtin_inff(), l = 0.f;
#pragma unroll
    for (int d = 0; d < 4; ++d) o[d] = f32x16{};
#pragma unroll 1
    for (int kb = 0; kb < WIN / 32; ++kb) {
#pragma unroll
      for (int R = 0; R < 4; ++R) {
        const unsigned vo = (unsigned)((8 * R + dr) * 256 + 64 * dc + 16 * (dx ^ ((2 * R + (dr >> 2)) & 3)));
        const unsigned so = __builtin_amdgcn_readfirstlane(kb * 8192), ld = __builtin_amdgcn_readfirstlane(lds0 + reg + 2048 * R);
        asm volatile("s_nop 4" ::: "memory");
        dma16(ld, vo, srdK, so); dma16(ld + 1024, vo + 128, srdK, so); dma16(ld + 8192, vo, srdV, so); dma16(ld + 9216, vo + 128, srdV, so);
      }
      asm volatile("s_waitcnt vmcnt(0)" ::: "memory");
      f32x16 sc = {};
#pragma unroll
      for (int s = 0; s < 8; ++s) { sc = mfma32(rowfrag(kbs[s & 1] + 512 * (s >> 1)), qf[s], sc); SBAR(); }
      float mx = sc[0];
#pragma unroll
      for (int r = 1; r < 16; ++r) mx = __builtin_fmaxf(mx, sc[r]);
      const float mn = __builtin_fmaxf(m, swap_max(mx)), al = __builtin_amdgcn_exp2f(m - mn);
      m = mn; l *= al;
#pragma unroll
      for (int r = 0; r < 16; ++r) { sc[r] = __builtin_amdgcn_exp2f(sc[r] - mn); l += sc[r]; }
#pragma unroll
      for (int d = 0; d < 4; ++d)
#pragma unroll
        for (int r = 0; r < 16; ++r) o[d][r] *= al;
#pragma unroll
      for (int ks = 0; ks < 2; ++ks) {
        const u32x4 pk = {cvtpk(sc[8 * ks], sc[8 * ks + 1]), cvtpk(sc[8 * ks + 2], sc[8 * ks + 3]), cvtpk(sc[8 * ks + 4], sc[8 * ks + 5]), cvtpk(sc[8 * ks + 6], sc[8 * ks + 7])};
#pragma unroll
        for (int d = 0; d < 4; ++d) { o[d] = mfma32(trfrag(vbs[0] + 4096 * ks + 512 * d, vbs[1] + 4096 * ks + 512 * d), __builtin_bit_cast(bf16x8, pk), o[d]); SBAR(); }
      }
      asm volatile("s_waitcnt lgkmcnt(0)" ::: "memory");
    }
    const float inv = 1.f / swap_add(l);
    static_for<4>(LAMBDA(dc_) { out_db(dc_, inv); });
  }
  const int rt = (xcd >> 2) * 8 + (idx >> 2), gi = (xcd & 3) * 4 + (idx & 3);
  {
    unsigned rf[4] = {tag, tag, tag, tag};
    if (wid == 0) {
#pragma unroll
      for (int k = 0; k < 4; ++k) rf[k] = __hip_atomic_load((gu32*)sync + SY_RF + lane + 64 * k, RLX_AGENT);
    }
    asm volatile("s_waitcnt vmcnt(0)" ::: "memory");
    if (wid == 0) {
      for (unsigned spins = 0; spins < SPIN_CAP; ++spins) {
        if (__all(rf[0] == tag && rf[1] == tag && rf[2] == tag && rf[3] == tag)) break;
#pragma unroll
        for (int k = 0; k < 4; ++k) rf[k] = __hip_atomic_load((gu32*)sync + SY_RF + lane + 64 * k, RLX_AGENT);
      }
    }
    asm volatile("" ::: "memory");
    __syncthreads();
    if (tid == 0) __hip_atomic_store((gu32*)sync + SY_OF + (qt * 32 + head) * FS, tag, RLX_AGENT);
  }
  unsigned* const c1 = sync + SY_CF + rt * 16 * FS, * const c2 = c1 + 256 * FS, * const c3 = c1 + 512 * FS;
  const u16* const mod = (const u16*)(ws + WS_MOD);
  const __amdgpu_buffer_rsrc_t rmod = __builtin_amdgcn_make_buffer_rsrc((void*)mod, 0, 3840 * 2, 0x00020000);
  u16 *const obuf = (u16*)(ws + WS_O), *const wot = (u16*)(ws + WS_WOT), *const w1t = (u16*)(ws + WS_W1T), *const w2t = (u16*)(ws + WS_W2T);
  u16 *const x1 = (u16*)(ws + WS_X1), *const hid = (u16*)(ws + WS_HID), *const h2 = (u16*)(ws + WS_H2), *const part = (u16*)(ws + WS_P);
  const int nft = gi < 15 ? 7 : 3;
  const __amdgpu_buffer_rsrc_t raD = __builtin_amdgcn_make_buffer_rsrc((void*)(w1t + (size_t)(7 * gi) * 80 * 128), 0, nft * 80 * 256, 0x00020000);
  RingA<4, 7, 20, 80, 5> RD; RD.init(lds0, wid, lane);
  {
    const int kq = gi >> 2, j = gi & 3, qb = wid & 3;
    const __amdgpu_buffer_rsrc_t ra = __builtin_amdgcn_make_buffer_rsrc((void*)(wot + ((size_t)(10 * j) * 512 + kq * 128) * 128), 0, 10 * 512 * 256, 0x00020000);
    const __amdgpu_buffer_rsrc_t rb = __builtin_amdgcn_make_buffer_rsrc((void*)(obuf + ((size_t)(8 * rt + 2 * qb) * 512 + kq * 128) * 128), 0, 2 * 512 * 256, 0x00020000);
    const __amdgpu_buffer_rsrc_t rp = __builtin_amdgcn_make_buffer_rsrc((void*)part, 0, 4 * SQ * DM * 2, 0x00020000);
    f32x4 acc[10][2] = {};
    RingA<4, 10, 32, 512, 4> RA; RA.init(lds0, wid, lane);
    RA.pre<0, 1>(ra);
    await_flags<8>(sync + SY_OF + ((rt >> 1) * 32 + kq * 8) * FS, tag);
    ring_gemm<4, 10, 32, 512, 512, 1, 4, 8>(acc, RA, ra, rb, lds, wid, lane, [&](auto ic) __attribute__((always_inline)) { RD.template issue<ic.value / 4, ic.value % 4>(raD); });
    pair_reduce<10>(acc, lds + 81920, wid, lane);
    const int nt0 = wid < 4 ? 0 : 5;
    const unsigned vo = ((((8 * rt + 2 * qb + ((lane >> 4) & 1)) * 80 + j * 20 + nt0 * 2 + (lane >> 5)) * 4 + kq) * 16 + (lane & 15)) * 16;
    auto outp = [&](auto ntc) __attribute__((always_inline)) {
      constexpr int nt = ntc.value, o = nt % 5;
      const u32x2 w0 = {cvtpk(acc[nt][0][0], acc[nt][0][1]), cvtpk(acc[nt][0][2], acc[nt][0][3])}, w1 = {cvtpk(acc[nt][1][0], acc[nt][1][1]), cvtpk(acc[nt][1][2], acc[nt][1][3])};
      st16(rp, vo + o * 2 * 1024, pair16(w0, w1));
    };
    if (wid < 4) { asm volatile("; lo-out" ::: "memory"); static_for<5>(LAMBDA(c) { outp(c); }); asm volatile("; lo-end" ::: "memory"); }
    else { asm volatile("; hi-out" ::: "memory"); static_for<5>(LAMBDA(c) { outp(IC<5 + c.value>{}); }); asm volatile("; hi-end" ::: "memory"); }
  }
  publish(c1 + gi * FS, tag);
  {
    float* red = (float*)(lds + 98304);
    const int r = tid & 7, row = rt * 128 + gi * 8 + r;
    const bool two = wid < 2;
    const __amdgpu_buffer_rsrc_t rp = __builtin_amdgcn_make_buffer_rsrc((void*)part, 0, 4 * SQ * DM * 2, 0x00020000);
    const __amdgpu_buffer_rsrc_t rx1 = __builtin_amdgcn_make_buffer_rsrc((void*)x1, 0, SQ * DM * 2, 0x00020000);
    const __amdgpu_buffer_rsrc_t rh2 = __builtin_amdgcn_make_buffer_rsrc((void*)h2, 0, SQ * DM * 2, 0x00020000);
    const u16* x = karg<const u16>(KA_X);
    u32x4 p[2][4], xv[2], gv[2], sbv[2], cbv[2]; unsigned blk[2], pblk[2];
#pragma unroll
    for (int u = 0; u < 2; ++u) {
      const int c = (u == 0 || two) ? (tid >> 3) + 64 * u : (tid >> 3);
      blk[u] = (((row >> 4) * 80 + c) * 16 + (row & 15)) * 16; pblk[u] = (((row >> 4) * 80 + c) * 64 + (row & 15)) * 16;
      xv[u] = *(const u32x4*)(x + (size_t)row * DM + c * 8);
      gv[u] = ld16c(rmod, (2 * 640 + c * 8) * 2); sbv[u] = ld16c(rmod, (3 * 640 + c * 8) * 2); cbv[u] = ld16c(rmod, (4 * 640 + c * 8) * 2);
    }
    RD.pre<2, 3>(raD);
    if (b == 119 && wid == 7) {
      bool ok = false;
      for (unsigned spins = 0; spins < SPIN_CAP && !ok; ++spins) {
        bool all = true;
#pragma unroll
        for (int k = 0; k < 4; ++k) all &= __hip_atomic_load((gu32*)sync + SY_STARTED + lane + 64 * k, RLX_AGENT) == tag;
        ok = __all(all);
      }
      if (lane == 0) __hip_atomic_store((gu32*)sync, tag, RLX_AGENT);
    }
    await_flags<16>(c1, tag);
#pragma unroll
    for (int k = 0; k < 4; ++k) p[0][k] = ld16c(rp, pblk[0] + k * 256);
    if (two) {
#pragma unroll
      for (int k = 0; k < 4; ++k) p[1][k] = ld16c(rp, pblk[1] + k * 256);
    }
    float v[2][8]; float s = 0; u32x4 x1v[2];
    auto resid = [&](auto uc) __attribute__((always_inline)) {
      constexpr int u = uc.value;
#pragma unroll
      for (int e = 0; e < 4; ++e) {
        const float a = (bf_lo(p[u][0][e]) + bf_lo(p[u][1][e])) + (bf_lo(p[u][2][e]) + bf_lo(p[u][3][e])), b = (bf_hi(p[u][0][e]) + bf_hi(p[u][1][e])) + (bf_hi(p[u][2][e]) + bf_hi(p[u][3][e]));
        x1v[u][e] = cvtpk(__builtin_fmaf(bf_lo(gv[u][e]), a, bf_lo(xv[u][e])), __builtin_fmaf(bf_hi(gv[u][e]), b, bf_hi(xv[u][e])));
        v[u][2 * e] = bf_lo(x1v[u][e]); v[u][2 * e + 1] = bf_hi(x1v[u][e]); s += v[u][2 * e] + v[u][2 * e + 1];
      }
    };
    resid(IC<0>{});
    if (two) resid(IC<1>{});
    s = xor_add<32>(xor_add<16>(xor_add<8>(s)));
    if (lane < 8) red[wid * 8 + r] = s;
    lds_barrier();
    float tot = 0;
#pragma unroll
    for (int i = 0; i < 8; ++i) tot += red[i * 8 + r];
    const float mean = tot * (1.f / DM); float q = 0;
#pragma unroll
    for (int e = 0; e < 8; ++e) { v[0][e] -= mean; q += v[0][e] * v[0][e]; }
    if (two) {
#pragma unroll
      for (int e = 0; e < 8; ++e) { v[1][e] -= mean; q += v[1][e] * v[1][e]; }
    }
    q = xor_add<32>(xor_add<16>(xor_add<8>(q)));
    if (lane < 8) red[64 + wid * 8 + r] = q;
    lds_barrier();
    float qt = 0;
#pragma unroll
    for (int i = 0; i < 8; ++i) qt += red[64 + i * 8 + r];
    const float rstd = __builtin_amdgcn_rsqf(qt * (1.f / DM) + 1e-5f);
    auto norm = [&](auto uc) __attribute__((always_inline)) {
      constexpr int u = uc.value;
      u32x4 hv;
#pragma unroll
      for (int e = 0; e < 4; ++e)
        hv[e] = cvtpk(__builtin_fmaf(v[u][2 * e] * rstd, bf_lo(cbv[u][e]), bf_lo(sbv[u][e])), __builtin_fmaf(v[u][2 * e + 1] * rstd, bf_hi(cbv[u][e]), bf_hi(sbv[u][e])));
      st16(rh2, blk[u], hv); st16(rx1, blk[u], x1v[u]);
    };
    norm(IC<0>{});
    if (two) norm(IC<1>{});
  }
  publish(c2 + gi * FS, tag);
  const int jE = gi & 7, rtE = rt * 2 + (gi >> 3);
  const __amdgpu_buffer_rsrc_t raE = __builtin_amdgcn_make_buffer_rsrc((void*)(w2t + (size_t)(5 * jE) * 216 * 128), 0, 5 * 216 * 256, 0x00020000);
  RingA<2, 5, 54, 216, 4> RE; RE.init(lds0, wid, lane);
  {
    const int qb = wid & 3, nt0 = wid < 4 ? 0 : 4;
    const __amdgpu_buffer_rsrc_t rb = __builtin_amdgcn_make_buffer_rsrc((void*)(h2 + (size_t)(8 * rt + 2 * qb) * 80 * 128), 0, 2 * 80 * 256, 0x00020000);
    const __amdgpu_buffer_rsrc_t rh = __builtin_amdgcn_make_buffer_rsrc((void*)hid, 0, SQ * FF * 2, 0x00020000);
    const u16* b1 = karg<const u16>(KA_B1);
    u32x2 bv[4];
#pragma unroll
    for (int o = 0; o < 4; ++o) bv[o] = *(const u32x2*)(b1 + (nt0 + o < nft ? (7 * gi + nt0 + o) * 16 + 4 * (lane >> 4) : 0));
    f32x4 acc[7][2] = {};
    await_flags<16>(c2, tag);
    ring_gemm<4, 7, 20, 80, 80, 3, 5, 10>(acc, RD, raD, rb, lds, wid, lane, [&](auto ic) __attribute__((always_inline)) { RE.template issue<ic.value / 5, ic.value % 5>(raE); });
    pair_reduce<7>(acc, lds + 98304, wid, lane);
    const unsigned vo = (((8 * rt + 2 * qb + ((lane >> 4) & 1)) * 216 + (7 * gi + nt0) * 2 + (lane >> 5)) * 16 + (lane & 15)) * 16;
    auto outp = [&](auto ntc, auto oc) __attribute__((always_inline)) {
      constexpr int nt = ntc.value, o = oc.value;
      if (nt < nft) {
        const float b0 = bf_lo(bv[o][0]), b1_ = bf_hi(bv[o][0]), b2_ = bf_lo(bv[o][1]), b3 = bf_hi(bv[o][1]);
        u32x2 w[2];
#pragma unroll
        for (int q = 0; q < 2; ++q) {
          const float v0 = rbf(__builtin_fmaxf(rbf(acc[nt][q][0]) + b0, 0.f)), v1 = rbf(__builtin_fmaxf(rbf(acc[nt][q][1]) + b1_, 0.f));
          const float v2 = rbf(__builtin_fmaxf(rbf(acc[nt][q][2]) + b2_, 0.f)), v3 = rbf(__builtin_fmaxf(rbf(acc[nt][q][3]) + b3, 0.f));
          w[q] = u32x2{cvtpk(v0 * v0, v1 * v1), cvtpk(v2 * v2, v3 * v3)};
        }
        st16(rh, vo + o * 2 * 256, pair16(w[0], w[1]));
      }
    };
    if (wid < 4) { asm volatile("; lo-out" ::: "memory"); static_for<4>(LAMBDA(c) { outp(c, c); }); asm volatile("; lo-end" ::: "memory"); }
    else { asm volatile("; hi-out" ::: "memory"); static_for<3>(LAMBDA(c) { outp(IC<4 + c.value>{}, c); }); asm volatile("; hi-end" ::: "memory"); }
  }
  publish(c3 + gi * FS, tag);
  {
    const int qb = wid & 1;
    const __amdgpu_buffer_rsrc_t rb = __builtin_amdgcn_make_buffer_rsrc((void*)(hid + (size_t)(4 * rtE + 2 * qb) * 216 * 128), 0, 2 * 216 * 256, 0x00020000);
    const __amdgpu_buffer_rsrc_t rx1 = __builtin_amdgcn_make_buffer_rsrc((void*)x1, 0, SQ * DM * 2, 0x00020000);
    const u16* b2 = karg<const u16>(KA_B2);
    u16* out = karg<u16>(KA_OUT);
    u32x4 xv[2], gv[2], bv[2];
#pragma unroll
    for (int it = 0; it < 2; ++it) {
      const int item = tid + it * 512, q = item & 63, c = item < 640 ? item >> 6 : 0, row = rtE * 64 + q;
      xv[it] = ld16c(rx1, (((row >> 4) * 80 + jE * 10 + c) * 16 + (row & 15)) * 16);
      gv[it] = ld16c(rmod, (5 * 640 + jE * 80 + c * 8) * 2); bv[it] = *(const u32x4*)(b2 + jE * 80 + c * 8);
    }
    f32x4 acc[5][2] = {};
    RE.pre<2, 3>(raE);
    await_flags<16>(c3, tag);
    ring_gemm<2, 5, 54, 216, 216, 3, 4, 0>(acc, RE, raE, rb, lds, wid, lane, [&](auto) {});
    f32x4* red = (f32x4*)lds;
    constexpr int HI = (3 * 40960 - 4 * 10240) / 16;
    {
      f32x4* mine = red + (wid * 10) * 64 + (wid >= 4 ? HI : 0) + lane;
#pragma unroll
      for (int n = 0; n < 5; ++n) { mine[(n * 2) * 64] = acc[n][0]; mine[(n * 2 + 1) * 64] = acc[n][1]; }
    }
    lds_barrier();
#pragma unroll
    for (int it = 0; it < 2; ++it) {
      const int item = tid + it * 512;
      if (item < 640) {
        const int q = item & 63, c = item >> 6, n = c >> 1, l0 = (q & 15) + 32 * (c & 1), base = (((q >> 5) * 5 + n) * 2 + ((q >> 4) & 1)) * 64 + l0;
        f32x4 lo = {0, 0, 0, 0}, hi = {0, 0, 0, 0};
#pragma unroll
        for (int w = 0; w < 4; ++w) { const int o_ = base + w * 2 * 5 * 2 * 64 + (w >= 2 ? HI : 0); lo += red[o_]; hi += red[o_ + 16]; }
        const float v[8] = {lo[0], lo[1], lo[2], lo[3], hi[0], hi[1], hi[2], hi[3]};
        u32x4 o;
#pragma unroll
        for (int e = 0; e < 4; ++e) {
          const float a = bf_lo(xv[it][e]) + rbf(bf_lo(gv[it][e]) * rbf(rbf(v[2 * e]) + bf_lo(bv[it][e]))), b = bf_hi(xv[it][e]) + rbf(bf_hi(gv[it][e]) * rbf(rbf(v[2 * e + 1]) + bf_hi(bv[it][e])));
          o[e] = cvtpk(a, b);
        }
        *(u32x4*)(out + (size_t)(rtE * 64 + q) * DM + jE * 80 + c * 8) = o;
      }
    }
  }
}

extern "C" void kernel_launch(void* const* d_in, const int* in_sizes, int n_in, void* d_out, int out_size, void* d_ws, size_t ws_size, hipStream_t stream) {
  char* ws = (char*)d_ws;
  (void)hipFuncSetAttribute((const void*)k_block, hipFuncAttributeMaxDynamicSharedMemorySize, 163840);
  k_block<<<256, 512, 163840, stream>>>((const u16*)d_in[0], (const u16*)d_in[1], (const u16*)d_in[2], (const u16*)d_in[3], (const u16*)d_in[4], (const u16*)d_in[6], (const u16*)d_in[7],
                                        (const u16*)d_in[8], (const u16*)d_in[9], (const u16*)d_in[10], (const u16*)d_in[11], (u16*)d_out, ws);
}
```

```cpp
#include <hip/hip_runtime.h>
#include <stdint.h>
#include <utility>

typedef unsigned short u16;
typedef short bf16x8 __attribute__((ext_vector_type(8)));
typedef short s16x4 __attribute__((ext_vector_type(4)));
typedef float f32x16 __attribute__((ext_vector_type(16)));
typedef float f32x4 __attribute__((ext_vector_type(4)));
typedef float f32x2 __attribute__((ext_vector_type(2)));
typedef __bf16 bf16x2 __attribute__((ext_vector_type(2)));
typedef unsigned u32x4 __attribute__((ext_vector_type(4)));
typedef unsigned u32x2 __attribute__((ext_vector_type(2)));
#define DI __device__ __forceinline__
#define GAS __attribute__((address_space(1)))
#define LAS __attribute__((address_space(3)))
#define SBAR() __builtin_amdgcn_sched_barrier(0)
#define PIN(x) asm volatile("" : "+v"(x))

constexpr int SQ = 2048, DM = 640, NH = 32, DH = 128, SK = 2048, WIN = 512, FF = 1728;
constexpr size_t WS_MOD = 0;
constexpr size_t WS_SYNC = 32768;
constexpr size_t WS_H = 524288;
constexpr size_t WS_O = WS_H + (size_t)SQ * DM * 2;
constexpr size_t WS_WOT = WS_O + (size_t)SQ * 4096 * 2;
constexpr size_t WS_W1T = WS_WOT + (size_t)4096 * DM * 2;
constexpr size_t WS_W2T = WS_W1T + (size_t)DM * FF * 2;
constexpr size_t WS_X1 = WS_W2T + (size_t)DM * FF * 2 + 65536;
constexpr size_t WS_HID = WS_X1 + (size_t)SQ * DM * 2;
constexpr size_t WS_H2 = WS_HID + (size_t)SQ * FF * 2 + 65536;
constexpr size_t WS_P = WS_H2 + (size_t)SQ * DM * 2;
constexpr size_t WS_END = WS_P + (size_t)4 * SQ * DM * 2;

template <int... I, class F> DI void static_for_impl(std::integer_sequence<int, I...>, F&& f) { (f(std::integral_constant<int, I>{}), ...); }
template <int N, class F> DI void static_for(F&& f) { static_for_impl(std::make_integer_sequence<int, N>{}, f); }
#define LAMBDA(ic) [&](auto ic) __attribute__((always_inline))

DI float bf_lo(unsigned w) { return __uint_as_float(w << 16); }
DI float bf_hi(unsigned w) { return __uint_as_float(w & 0xffff0000u); }
DI unsigned cvtpk(float lo, float hi) { f32x2 v = {lo, hi}; bf16x2 b = __builtin_convertvector(v, bf16x2); return __builtin_bit_cast(unsigned, b); }
DI float rbf(float x) { return bf_lo(cvtpk(x, 0.f)); }
DI f32x16 mfma32(bf16x8 a, bf16x8 b, f32x16 c) { return __builtin_amdgcn_mfma_f32_32x32x16_bf16(a, b, c, 0, 0, 0); }
DI f32x4 mfma16(bf16x8 a, bf16x8 b, f32x4 c) { return __builtin_amdgcn_mfma_f32_16x16x32_bf16(a, b, c, 0, 0, 0); }
DI float swap_max(float m) { auto r = __builtin_amdgcn_permlane32_swap(__float_as_uint(m), __float_as_uint(m), false, false); return __builtin_fmaxf(__uint_as_float(r[0]), __uint_as_float(r[1])); }
DI float swap_add(float m) { auto r = __builtin_amdgcn_permlane32_swap(__float_as_uint(m), __float_as_uint(m), false, false); return __uint_as_float(r[0]) + __uint_as_float(r[1]); }

DI void lds_barrier() { asm volatile("s_waitcnt lgkmcnt(0)\n\ts_barrier" ::: "memory"); }
template <int CTRL> DI float dpp_mov(float v) { return __builtin_bit_cast(float, __builtin_amdgcn_update_dpp(0, __builtin_bit_cast(int, v), CTRL, 0xf, 0xf, true)); }
template <int M> DI float xor_add(float v) {
  if constexpr (M == 1) return v + dpp_mov<0xB1>(v);
  else if constexpr (M == 2) return v + dpp_mov<0x4E>(v);
  else if constexpr (M == 4) return v + dpp_mov<0x141>(dpp_mov<0x1B>(v));
  else if constexpr (M == 8) return v + dpp_mov<0x128>(v);
  else if constexpr (M == 16) { const auto r = __builtin_amdgcn_permlane16_swap(__float_as_uint(v), __float_as_uint(v), false, false); return __uint_as_float(r[0]) + __uint_as_float(r[1]); }
  else { const auto r = __builtin_amdgcn_permlane32_swap(__float_as_uint(v), __float_as_uint(v), false, false); return __uint_as_float(r[0]) + __uint_as_float(r[1]); }
}
typedef GAS unsigned gu32;
typedef GAS unsigned long long gu64;
#define RLX_AGENT __ATOMIC_RELAXED, __HIP_MEMORY_SCOPE_AGENT
constexpr unsigned SPIN_CAP = 1u << 18;
constexpr int SC1 = 16;
constexpr int FS = 32;
constexpr int SY_STARTED = 64, SY_RF = 512, SY_GRAN = 1024  , SY_HF = 36864, SY_OF = SY_HF + 256 * FS, SY_CF = SY_OF + 256 * FS  ;
constexpr int NREP = 8;
DI void st16(__amdgpu_buffer_rsrc_t r, unsigned voff, u32x4 v) { __builtin_amdgcn_raw_buffer_store_b128(v, r, voff, 0, SC1); }
DI u32x4 ld16c(__amdgpu_buffer_rsrc_t r, unsigned voff) { return __builtin_amdgcn_raw_buffer_load_b128(r, voff, 0, SC1); }
DI void publish(unsigned* flag, unsigned tag) {
  asm volatile("s_waitcnt vmcnt(0)" ::: "memory");
  __syncthreads();
  if (threadIdx.x == 0) __hip_atomic_store((gu32*)flag, tag, RLX_AGENT);
}
template <int N> DI void await_flags(const unsigned* flags, unsigned tag) {
  if (threadIdx.x < 64) {
    gu32* p = (gu32*)flags + (threadIdx.x < N ? threadIdx.x : 0) * FS;
    unsigned a = __hip_atomic_load(p, RLX_AGENT); __builtin_amdgcn_s_sleep(2);
    unsigned b = __hip_atomic_load(p, RLX_AGENT);
    for (unsigned spins = 0; spins < SPIN_CAP; ++spins) {
      if (__all(a == tag)) break;
      a = __hip_atomic_load(p, RLX_AGENT); __builtin_amdgcn_s_sleep(2);
      if (__all(b == tag)) break;
      b = __hip_atomic_load(p, RLX_AGENT); __builtin_amdgcn_s_sleep(2);
    }
  }
  asm volatile("" ::: "memory");
  __syncthreads();
}
template <class T> DI T* karg(int i) {
  const __attribute__((address_space(4))) char* p = (const __attribute__((address_space(4))) char*)__builtin_amdgcn_kernarg_segment_ptr();
  asm volatile("" : "+s"(p));
  return *(T* const __attribute__((address_space(4)))*)(p + 8 * i);
}
enum { KA_X = 0, KA_KC, KA_VC, KA_WQ, KA_WO, KA_W1, KA_B1, KA_W2, KA_B2, KA_C, KA_WMOD, KA_OUT, KA_WS };

DI u32x4 pair16(u32x2 x, u32x2 y) {
  const auto a = __builtin_amdgcn_permlane16_swap(x[0], y[0], false, false), b = __builtin_amdgcn_permlane16_swap(x[1], y[1], false, false);
  return u32x4{a[0], b[0], a[1], b[1]};
}
constexpr int NS = 8, SLOT = 16384;
constexpr float QSCALE = 0.088388347648318440f * 1.4426950408889634f;
constexpr float PBIAS = 96.f;
#define PLIM_V 1073741824.f
constexpr float OMIN = 7.7037198e-34f;
constexpr float PLIM = PLIM_V;
constexpr int tile_K(int t) { return t == 0 ? 10 : 9 + 2 * t; }
constexpr int tile_V(int t) { return t == 7 ? 25 : 12 + 2 * t; }
constexpr int slot_of(int idx) { return (idx % NS) * SLOT; }
constexpr int lastuse(int idx) { return idx < 10 ? idx : (idx == 10 ? 10 : (idx == 25 ? 18 : ((idx & 1) ? 10 + (idx - 9) / 2 : 11 + (idx - 12) / 2))); }
constexpr int need_bar(int idx) {
  if (idx < 10) return idx == 0 ? 0 : idx - 1;
  if (idx == 10) return 9;
  if (idx == 25) return 17;
  return (idx & 1) ? 10 + (idx - 9) / 2 - 1 : 11 + (idx - 12) / 2;
}
constexpr int rt_step(int u) { return u == 0 ? 9 : (u == 1 ? 10 : 12); }
constexpr int rt_proc(int u) { return u == 0 ? 11 : (u == 1 ? 12 : 14); }
constexpr int rt_slot(int u) { return (u == 1 ? NS + 1 : NS) * SLOT; }
struct Sched { int npro; int pro[16]; int nops[19]; int op[19][16]; int wait[19]; int rt_done; };
constexpr Sched make_sched() {
  Sched p{}; int pos = 0; int tile_end[26] = {}; int h_end[10] = {}; int u_end[3] = {}; int next_tile = 3;
  auto pro_h = [&](int i) { for (int k = 0; k < 4; ++k) p.pro[p.npro++] = 100 + 4 * i + k; pos += 4; h_end[i] = pos; };
  pro_h(0); pro_h(1);
  tile_end[0] = tile_end[1] = tile_end[2] = 0; int last_rt_store = 1 << 20; p.rt_done = -1;
  for (int g = 0; g < 19; ++g) {
    int need = 0;
    for (int idx = 0; idx < next_tile; ++idx) if (need_bar(idx) <= g && tile_end[idx] > need) need = tile_end[idx];
    if (g < 10 && h_end[g] > need) need = h_end[g];
    for (int u = 0; u < 3; ++u) if (rt_proc(u) <= g && u_end[u] > need) need = u_end[u];
    p.wait[g] = pos - need;
    if (p.rt_done < 0 && need >= last_rt_store) p.rt_done = g;
    int n = 0;
    for (int u = 0; u < 3; ++u) if (g == rt_proc(u)) { p.op[g][n++] = 300 + 2 * u; p.op[g][n++] = 301 + 2 * u; }
    if (g == 0) { for (int k = 0; k < 4; ++k) p.op[g][n++] = 100 + 4 * 2 + k; pos += 4; h_end[2] = pos; }
    const int maxt = g < 7 ? 1 : 2;
    for (int c = 0; c < maxt && next_tile < 26; ++c) {
      if (next_tile >= NS && lastuse(next_tile - NS) >= g) break;
      p.op[g][n++] = 2 * next_tile; p.op[g][n++] = 2 * next_tile + 1; pos += 2; tile_end[next_tile] = pos; ++next_tile;
    }
    if (g + 3 < 10) { for (int k = 0; k < 4; ++k) p.op[g][n++] = 100 + 4 * (g + 3) + k; pos += 4; h_end[g + 3] = pos; }
    for (int u = 0; u < 3; ++u) if (g == rt_step(u)) { p.op[g][n++] = 200 + 2 * u; p.op[g][n++] = 201 + 2 * u; pos += 2; u_end[u] = pos; }
    for (int u = 0; u < 3; ++u) if (g == rt_proc(u)) { p.op[g][n++] = 310 + 2 * u; p.op[g][n++] = 311 + 2 * u; pos += 2; if (u == 2) last_rt_store = pos; }
    p.nops[g] = n;
  }
  for (int idx = 3; idx < 26; ++idx) if (tile_end[idx] == 0) p.wait[0] = -1;
  if (p.rt_done < 0) p.wait[0] = -1;
  return p;
}
constexpr Sched SCH = make_sched();
static_assert(SCH.wait[0] >= 0, "schedule incomplete");
constexpr bool sched_ok() {
  int step_of[26] = {}; for (int i = 0; i < 26; ++i) step_of[i] = -1;
  for (int g = 0; g < 19; ++g) for (int j = 0; j < SCH.nops[g]; ++j) if (SCH.op[g][j] < 100) step_of[SCH.op[g][j] >> 1] = g;
  for (int i = 3; i < 26; ++i) if (step_of[i] >= need_bar(i)) return false;
  return true;
}
static_assert(sched_ok(), "a tile is issued too late");

DI void dma16(unsigned ldsaddr, unsigned voff, __amdgpu_buffer_rsrc_t srd, unsigned soff) {
  asm volatile("s_mov_b32 m0, %0\n\ts_nop 0\n\tbuffer_load_dwordx4 %1, %2, %3 offen lds" ::"s"(ldsaddr), "v"(voff), "s"(srd), "s"(soff) : "memory");
}
template <int OFF> DI void bload16c(bf16x8& dst, unsigned voff, __amdgpu_buffer_rsrc_t srd, unsigned soff) {
  asm volatile("buffer_load_dwordx4 %0, %1, %2, %3 offen offset:%4 sc1" : "=v"(dst) : "v"(voff), "s"(srd), "s"(soff), "i"(OFF) : "memory");
}
DI void dma16c(unsigned ldsaddr, unsigned voff, __amdgpu_buffer_rsrc_t srd, unsigned soff) {
  asm volatile("s_mov_b32 m0, %0\n\ts_nop 0\n\tbuffer_load_dwordx4 %1, %2, %3 offen sc1 lds" ::"s"(ldsaddr), "v"(voff), "s"(srd), "s"(soff) : "memory");
}
typedef LAS const char* lds_cptr;
typedef short v4i16_t __attribute__((ext_vector_type(4)));
DI s16x4 vtr(lds_cptr p) { return __builtin_bit_cast(s16x4, __builtin_amdgcn_ds_read_tr16_b64_v4i16((LAS v4i16_t*)p)); }
DI bf16x8 trfrag(lds_cptr p0, lds_cptr p1) { const s16x4 lo = vtr(p0), hi = vtr(p1); return __builtin_shufflevector(lo, hi, 0, 1, 2, 3, 4, 5, 6, 7); }
DI bf16x8 rowfrag(lds_cptr p) { return *(LAS const bf16x8*)p; }
DI float max3(float a, float b, float c) { return __builtin_fmaxf(__builtin_fmaxf(a, b), c); }
template <int N> DI void wait_bar4(bf16x8 (&b)[4]) {
  asm volatile("s_waitcnt vmcnt(%4)\n\ts_barrier" : "+v"(b[0]), "+v"(b[1]), "+v"(b[2]), "+v"(b[3]) : "i"(N) : "memory");
}
template <int N> DI void wait_bar() { asm volatile("s_waitcnt vmcnt(%0)\n\ts_barrier" ::"i"(N) : "memory"); }
template <int V> using IC = std::integral_constant<int, V>;

template <int N> DI void wait_bar4b(bf16x8 (&b)[2][2]) {
  asm volatile("s_waitcnt vmcnt(%4)\n\ts_barrier" : "+v"(b[0][0]), "+v"(b[0][1]), "+v"(b[1][0]), "+v"(b[1][1]) : "i"(N) : "memory");
}
template <int NQB, int NT, int KS> struct Ring {
  static constexpr int KSP = 8 / NQB, SK = 2 * KSP, NSTG = (KS + SK - 1) / SK, NP = SK * NT, PA = (NP + 7) / 8, SB = PA * 8 * 1024, FR = 2 * NT;
};
template <int NQB, int NT, int KS, int KC_A, int NSLOT> struct RingA {
  using R = Ring<NQB, NT, KS>;
  unsigned voff[R::PA]; unsigned ldsw;
  DI void init(unsigned lds0, int wid, int lane) {
    ldsw = lds0 + wid * 1024;
#pragma unroll
    for (int i = 0; i < R::PA; ++i) { const int p = i * 8 + wid, ks = p / NT, nt = p - ks * NT; voff[i] = p < R::NP ? (unsigned)(nt * KC_A * 256 + ks * 1024 + lane * 16) : 0x7ff00000u; }
  }
  template <int S, int I> DI void issue(__amdgpu_buffer_rsrc_t ra) const { dma16c(ldsw + ((S % NSLOT) * R::SB + I * 8192), voff[I], ra, S * R::SK * 1024); }
  template <int S0, int S1> DI void pre(__amdgpu_buffer_rsrc_t ra) const { static_for<S1 - S0>(LAMBDA(sc) { static_for<R::PA>(LAMBDA(ic) { this->template issue<S0 + sc.value, ic.value>(ra); }); }); }
};
template <int NQB, int NT, int KS, int KC_A, int KC_B, int NPRE, int NSLOT, int NHOOK, class HOOK>
DI void ring_gemm(f32x4 (&acc)[NT][2], const RingA<NQB, NT, KS, KC_A, NSLOT>& RA, __amdgpu_buffer_rsrc_t ra, __amdgpu_buffer_rsrc_t rb, const char* lds, int wid, int lane, HOOK&& hook) {
  using R = Ring<NQB, NT, KS>;
  constexpr int SK = R::SK, NSTG = R::NSTG, PA = R::PA, FR = R::FR, PD = 5;
  static_assert(NSTG >= 3 && FR > PD + 1, "");
  const int kq = wid / NQB;
  bf16x8 B[3][2][2];
  const unsigned voffB = lane * 16 + 2 * kq * 1024;
  auto issueA1 = [&](auto sc, auto ic) __attribute__((always_inline)) { RA.template issue<sc.value, ic.value>(ra); };
  auto issueB1 = [&](auto sc, auto ic) __attribute__((always_inline)) {
    constexpr int s = sc.value, i = ic.value, ks = i >> 1, q = i & 1;
    bload16c<0>(B[s % 3][ks][q], voffB, rb, q * KC_B * 256 + (s * SK + ks) * 1024);
  };
  if constexpr (NPRE == 0) static_for<PA>(LAMBDA(ic) { issueA1(IC<0>{}, ic); });
  static_for<4>(LAMBDA(ic) { issueB1(IC<0>{}, ic); });
  if constexpr (NPRE > 1) { static_for<4>(LAMBDA(ic) { issueB1(IC<1>{}, ic); }); wait_bar4b<4>(B[0]); } else wait_bar4b<0>(B[0]);
  const LAS bf16x8* pl = (const LAS bf16x8*)lds + lane + 2 * kq * NT * 64;
  bf16x8 A[PD + 1];
  auto rd = [&](auto mc) __attribute__((always_inline)) { constexpr int M = mc.value, s = M / FR, m = M % FR; A[M % (PD + 1)] = pl[((s % NSLOT) * PA * 8 + m) * 64]; };
  static_for<PD>(LAMBDA(jc) { rd(jc); });
  SBAR();
  static_for<NSTG>(LAMBDA(sc) {
    constexpr int s = sc.value;
    constexpr int n0 = (NPRE <= 1 && s == 0) ? PA + 4 : 0;
    constexpr int sa = s + 2;
    constexpr int nA = (sa < NSTG && sa >= NPRE) ? PA : 0, nB = (s + 2 < NSTG) ? 4 : 0, NI = n0 + nA + nB, NGI = FR - PD - 1;
    constexpr bool full = (s + 1) * SK <= KS;
    static_for<FR>(LAMBDA(mc) {
      constexpr int m = mc.value, M = s * FR + m, ks = m / NT, nt = m % NT;
      if constexpr (m == FR - PD && s + 1 < NSTG) wait_bar4b<nA + nB>(B[(s + 1) % 3]);
      if constexpr (M + PD < NSTG * FR) rd(IC<M + PD>{});
      if constexpr (m < NGI) {
        constexpr int i0 = (m * NI + NGI - 1) / NGI, i1 = ((m + 1) * NI + NGI - 1) / NGI;
        static_for<i1 - i0>(LAMBDA(uc) {
          constexpr int i = i0 + uc.value;
          if constexpr (i < n0) { if constexpr (i < PA) issueA1(IC<1>{}, IC<i>{}); else issueB1(IC<1>{}, IC<i - PA>{}); }
          else if constexpr (i - n0 < nA) issueA1(IC<sa>{}, IC<i - n0>{});
          else issueB1(IC<s + 2>{}, IC<i - n0 - nA>{});
        });
      }
      if constexpr (NHOOK > 0 && M >= (NSTG - 1) * FR - PD) {
        constexpr int j = M - ((NSTG - 1) * FR - PD), HG = FR + PD - 2, i0 = (j * NHOOK + HG - 1) / HG, i1 = ((j + 1) * NHOOK + HG - 1) / HG;
        if constexpr (j < HG) static_for<i1 - i0>(LAMBDA(uc) { hook(IC<i0 + uc.value>{}); });
      }
      if (full || s * SK + 2 * kq + ks < KS) { acc[nt][0] = mfma16(A[M % (PD + 1)], B[s % 3][ks][0], acc[nt][0]); acc[nt][1] = mfma16(A[M % (PD + 1)], B[s % 3][ks][1], acc[nt][1]); }
      SBAR();
    });
  });
}
template <int NT> DI void pair_reduce(f32x4 (&acc)[NT][2], char* lds, int wid, int lane) {
  constexpr int H0 = (NT + 1) / 2;
  f32x4* red = (f32x4*)lds + (size_t)wid * H0 * 2 * 64 + lane;
  const f32x4* in = (const f32x4*)lds + (size_t)(wid ^ 4) * H0 * 2 * 64 + lane;
  lds_barrier();
  if (wid < 4) {
    asm volatile("; lo-send" ::: "memory");
#pragma unroll
    for (int nt = H0; nt < NT; ++nt) { red[((nt - H0) * 2) * 64] = acc[nt][0]; red[((nt - H0) * 2 + 1) * 64] = acc[nt][1]; }
    asm volatile("; lo-sent" ::: "memory");
  } else {
    asm volatile("; hi-send" ::: "memory");
#pragma unroll
    for (int nt = 0; nt < H0; ++nt) { red[(nt * 2) * 64] = acc[nt][0]; red[(nt * 2 + 1) * 64] = acc[nt][1]; }
    asm volatile("; hi-sent" ::: "memory");
  }
  lds_barrier();
  if (wid < 4) {
    asm volatile("; lo-recv" ::: "memory");
#pragma unroll
    for (int nt = 0; nt < H0; ++nt) { acc[nt][0] += in[(nt * 2) * 64]; acc[nt][1] += in[(nt * 2 + 1) * 64]; PIN(acc[nt][0]); PIN(acc[nt][1]); }
    asm volatile("; lo-done" ::: "memory");
  } else {
    asm volatile("; hi-recv" ::: "memory");
#pragma unroll
    for (int nt = H0; nt < NT; ++nt) { acc[nt][0] += in[((nt - H0) * 2) * 64]; acc[nt][1] += in[((nt - H0) * 2 + 1) * 64]; PIN(acc[nt][0]); PIN(acc[nt][1]); }
    asm volatile("; hi-done" ::: "memory");
  }
}

__global__ __launch_bounds__(512, 2) void k_block(const u16* a_x, const u16* a_kc, const u16* a_vc, const u16* a_wq, const u16* a_wo, const u16* a_w1, const u16* a_b1, const u16* a_w2,
                                                   const u16* a_b2, const u16* a_c, const u16* a_wmod, u16* a_out, char* a_ws) {
  extern __shared__ __attribute__((aligned(16))) char lds[];
  const int tid = threadIdx.x, lane = tid & 63, r32 = lane & 31, hi = lane >> 5;
  const int wid = __builtin_amdgcn_readfirstlane(tid >> 6);
  const int b = blockIdx.x, xcd = b & 7, idx = b >> 3;
  const int head = (xcd & 3) * 8 + (idx & 7), qt = (xcd >> 2) * 4 + (idx >> 3);
  const unsigned lds0 = (unsigned)(uintptr_t)lds;
  char* const ws = karg<char>(KA_WS);
  unsigned* const sync = (unsigned*)(ws + WS_SYNC);
  const __amdgpu_buffer_rsrc_t srdW = __builtin_amdgcn_make_buffer_rsrc((void*)(karg<const u16>(KA_WQ) + head * DH), 0, (DM - 1) * 8192 + 256, 0x00020000);
  const int dr = (lane >> 2) & 7, dx = lane & 3, dc = lane >> 5, dz = (2 * wid + (dr >> 2)) & 3;
  const unsigned voffW = (unsigned)((8 * wid + dr) * 8192 + 64 * dc + 16 * (dx ^ dz));
  const unsigned ldsw = lds0 + 2048 * wid;
  unsigned tag;
  {
    float* sm = (float*)(lds + NS * SLOT);
    const u16* x = karg<const u16>(KA_X);
    const int r = tid & 7, row = b * 8 + r; const bool two = wid < 2;
    u32x4 w[5]; unsigned cv = 0;
    const int cc = tid & 3, r0 = tid >> 2;
    if (b < 120) {
      const u16* wmod = karg<const u16>(KA_WMOD);
#pragma unroll
      for (int i = 0; i < 5; ++i) w[i] = *(const u32x4*)(wmod + (size_t)(r0 + 128 * i) * 3840 + b * 32 + cc * 8);
      cv = *(const unsigned*)(karg<const u16>(KA_C) + 2 * (tid < 320 ? tid : 0));
    }
    u32x4 xv[2];
    xv[0] = *(const u32x4*)(x + (size_t)row * DM + (tid >> 3) * 8); xv[1] = *(const u32x4*)(x + (size_t)row * DM + ((two ? 64 : 0) + (tid >> 3)) * 8);
    unsigned tg = __hip_atomic_load((gu32*)sync, RLX_AGENT) + 1u;
    if (b < 120 && tid < 320) {
      const float v0 = bf_lo(cv), v1 = bf_hi(cv);
      const unsigned pk = cvtpk(v0 * __builtin_amdgcn_rcpf(1.f + __expf(-v0)), v1 * __builtin_amdgcn_rcpf(1.f + __expf(-v1)));
      *(f32x2*)(sm + 512 + 2 * tid) = f32x2{bf_lo(pk), bf_hi(pk)};
    }
    PIN(tg);
    tag = __builtin_amdgcn_readfirstlane(tg);
    lds_barrier();
    if (tid == 0) __hip_atomic_store((gu32*)sync + SY_STARTED + b, tag, RLX_AGENT);
    if (b < 120) {
      float a[8] = {};
#pragma unroll
      for (int i = 0; i < 5; ++i) {
        const float sv = sm[512 + r0 + 128 * i];
#pragma unroll
        for (int j = 0; j < 4; ++j) { a[2 * j] += sv * bf_lo(w[i][j]); a[2 * j + 1] += sv * bf_hi(w[i][j]); }
      }
      float v4[4], v2[2];
#pragma unroll
      for (int j = 0; j < 4; ++j) { const auto r = __builtin_amdgcn_permlane32_swap(__float_as_uint(a[j]), __float_as_uint(a[j + 4]), false, false); v4[j] = __uint_as_float(r[0]) + __uint_as_float(r[1]); }
#pragma unroll
      for (int j = 0; j < 2; ++j) { const auto r = __builtin_amdgcn_permlane16_swap(__float_as_uint(v4[j]), __float_as_uint(v4[j + 2]), false, false); v2[j] = xor_add<8>(xor_add<4>(__uint_as_float(r[0]) + __uint_as_float(r[1]))); }
      if ((lane & 12) == 0) *(f32x2*)(sm + wid * 32 + cc * 8 + 4 * (lane >> 5) + 2 * ((lane >> 4) & 1)) = f32x2{v2[0], v2[1]};
      lds_barrier();
      {
        const int u = tid & 15;
        float lo = 0.f, hi_ = 0.f;
#pragma unroll
        for (int w8 = 0; w8 < 8; ++w8) { const f32x2 t2 = *(const f32x2*)(sm + w8 * 32 + 2 * u); lo += t2[0]; hi_ += t2[1]; }
        unsigned pk = cvtpk(lo, hi_);
        if ((b >= 20 && b < 40) || (b >= 80 && b < 100)) pk = cvtpk(1.f + bf_lo(pk), 1.f + bf_hi(pk));
        if (b < 40) { if (tid < 16 * NREP) __hip_atomic_store((gu64*)(sync + SY_GRAN + (tid >> 4) * 2048) + b * 16 + u, ((unsigned long long)tag << 32) | pk, RLX_AGENT); }
        else if (tid < 16) __hip_atomic_store((gu32*)(ws + WS_MOD) + b * 16 + u, pk, RLX_AGENT);
      }
    }
#pragma unroll
    for (int i = 0; i < 3; ++i) { dma16(ldsw + slot_of(i), voffW, srdW, i * 64 * 8192); dma16(ldsw + slot_of(i) + 1024, voffW, srdW, i * 64 * 8192 + 128); }
    float* red = sm + 256;
    float f[2][8]; float s = 0;
#pragma unroll
    for (int e = 0; e < 4; ++e) { f[0][2 * e] = bf_lo(xv[0][e]); f[0][2 * e + 1] = bf_hi(xv[0][e]); f[1][2 * e] = bf_lo(xv[1][e]); f[1][2 * e + 1] = bf_hi(xv[1][e]); s += f[0][2 * e] + f[0][2 * e + 1]; }
    if (two) {
#pragma unroll
      for (int e = 0; e < 8; ++e) s += f[1][e];
    }
    s = xor_add<32>(xor_add<16>(xor_add<8>(s)));
    if (lane < 8) red[wid * 8 + r] = s;
    lds_barrier();
    float tot = 0;
#pragma unroll
    for (int i = 0; i < 8; ++i) tot += red[i * 8 + r];
    const float mean = tot * (1.f / DM); float q = 0;
#pragma unroll
    for (int e = 0; e < 8; ++e) { const float d = f[0][e] - mean; q += d * d; }
    if (two) {
#pragma unroll
      for (int e = 0; e < 8; ++e) { const float d = f[1][e] - mean; q += d * d; }
    }
    q = xor_add<32>(xor_add<16>(xor_add<8>(q)));
    if (lane < 8) red[64 + wid * 8 + r] = q;
    lds_barrier();
    float qt_ = 0;
#pragma unroll
    for (int i = 0; i < 8; ++i) qt_ += red[64 + i * 8 + r];
    const float mb = rbf(mean), rsd = 1.f / rbf(sqrtf(rbf(rbf(qt_ * (1.f / DM)) + 1e-5f)));
#pragma unroll
    for (int u = 0; u < 2; ++u)
#pragma unroll
      for (int e = 0; e < 8; e += 2) {
        const unsigned d = cvtpk(f[u][e] - mb, f[u][e + 1] - mb), n = cvtpk(bf_lo(d) * rsd, bf_hi(d) * rsd);
        f[u][e] = bf_lo(n); f[u][e + 1] = bf_hi(n);
      }
    if (wid == 0) {
      const __amdgpu_buffer_rsrc_t rg = __builtin_amdgcn_make_buffer_rsrc((void*)(sync + SY_GRAN + (b % NREP) * 2048), 0, 5120, 0x00020000);
      u32x4 gv[5];
      for (unsigned spins = 0; spins < SPIN_CAP; ++spins) {
        bool ok = true;
#pragma unroll
        for (int k = 0; k < 5; ++k) { gv[k] = ld16c(rg, (lane + 64 * k) * 16); ok &= gv[k][1] == tag && gv[k][3] == tag; }
        if (__all(ok)) break;
        __builtin_amdgcn_s_sleep(4);
      }
#pragma unroll
      for (int k = 0; k < 5; ++k) *(f32x4*)(sm + 1280 + 4 * (lane + 64 * k)) = f32x4{bf_lo(gv[k][0]), bf_hi(gv[k][0]), bf_lo(gv[k][2]), bf_hi(gv[k][2])};
    }
    lds_barrier();
    {
      const __amdgpu_buffer_rsrc_t rh = __builtin_amdgcn_make_buffer_rsrc((void*)(ws + WS_H), 0, SQ * DM * 2, 0x00020000);
      const unsigned base = (unsigned)(((b >> 1) * 80 * 16 + (b & 1) * 8 + r) * 16);
      auto apply = [&](auto uc) __attribute__((always_inline)) {
        constexpr int u = uc.value;
        const int c = (tid >> 3) + 64 * u, k0 = c * 8;
        const f32x4 s0 = *(const f32x4*)(sm + 1280 + k0), s1 = *(const f32x4*)(sm + 1284 + k0), c0 = *(const f32x4*)(sm + 1920 + k0), c1 = *(const f32x4*)(sm + 1924 + k0);
        const float sa[8] = {s0[0], s0[1], s0[2], s0[3], s1[0], s1[1], s1[2], s1[3]}, oc[8] = {c0[0], c0[1], c0[2], c0[3], c1[0], c1[1], c1[2], c1[3]};
        u32x4 o;
#pragma unroll
        for (int e = 0; e < 4; ++e) {
          const unsigned m = cvtpk(f[u][2 * e] * oc[2 * e], f[u][2 * e + 1] * oc[2 * e + 1]);
          o[e] = cvtpk(bf_lo(m) + sa[2 * e], bf_hi(m) + sa[2 * e + 1]);
        }
        st16(rh, base + c * 256, o);
      };
      apply(IC<0>{});
      if (two) apply(IC<1>{});
    }
    publish(sync + SY_HF + b * FS, tag);
    await_flags<32>(sync + SY_HF + qt * 32 * FS, tag);
  }
  const __amdgpu_buffer_rsrc_t srdK = __builtin_amdgcn_make_buffer_rsrc((void*)(karg<const u16>(KA_KC) + ((size_t)head * SK + (SK - WIN)) * DH), 0, WIN * 256, 0x00020000);
  const __amdgpu_buffer_rsrc_t srdV = __builtin_amdgcn_make_buffer_rsrc((void*)(karg<const u16>(KA_VC) + ((size_t)head * SK + (SK - WIN)) * DH), 0, WIN * 256, 0x00020000);
  const __amdgpu_buffer_rsrc_t srdH = __builtin_amdgcn_make_buffer_rsrc((void*)(ws + WS_H + (size_t)(qt * 16 + 2 * wid) * 80 * 256), 0, 2 * 80 * 256, 0x00020000);
  const unsigned voffKV = (unsigned)((8 * wid + dr) * 256 + 64 * dc + 16 * (dx ^ dz));
  const unsigned voffH = (unsigned)(((lane >> 4) & 1) * 20480 + (lane >> 5) * 256 + (lane & 15) * 16);
  bf16x8 hf[10][4], rtf[2];
  __amdgpu_buffer_rsrc_t rsrc_u[3]; unsigned pitch_u[3], dst_u[3];
  const __amdgpu_buffer_rsrc_t srdT = __builtin_amdgcn_make_buffer_rsrc((void*)(ws + WS_WOT), 0, (int)(WS_W2T - WS_WOT) + DM * FF * 2, 0x00020000);
  auto vmem_op = [&](auto oc) __attribute__((always_inline)) {
    constexpr int op = oc.value;
    if constexpr (op >= 310) {
      constexpr int u = (op - 310) >> 1, ks = op & 1;
      __builtin_amdgcn_raw_buffer_store_b128(__builtin_bit_cast(u32x4, rtf[ks]), srdT, lane * 16 + ks * 1024, dst_u[u], SC1);
    } else if constexpr (op >= 300) {
      constexpr int u = (op - 300) >> 1, ks = op & 1;
      const int g4 = lane >> 4, chq = 2 * wid + ((lane & 3) >> 1), q4_ = (lane & 15) >> 2;
      const lds_cptr p0 = (lds_cptr)lds + rt_slot(u) + 2048 * (4 * ks + g4) + 512 * (chq >> 2) + 64 * q4_ + 16 * ((chq & 3) ^ ((2 * g4) & 3)) + 8 * (lane & 1);
      const lds_cptr p1 = (lds_cptr)lds + rt_slot(u) + 2048 * (4 * ks + g4) + 512 * (chq >> 2) + 64 * (4 + q4_) + 16 * ((chq & 3) ^ ((2 * g4 + 1) & 3)) + 8 * (lane & 1);
      rtf[ks] = trfrag(p0, p1);
    } else if constexpr (op >= 200) {
      constexpr int u = (op - 200) >> 1, half = op & 1;
      dma16(ldsw + rt_slot(u) + 1024 * half, (unsigned)(8 * wid + dr) * pitch_u[u] + (unsigned)(64 * dc + 16 * (dx ^ dz)), rsrc_u[u], 128 * half);
    } else if constexpr (op >= 100) { constexpr int i = (op - 100) >> 2, k = (op - 100) & 3; bload16c<512 * k>(hf[i][k], voffH, srdH, i * 2048); }
    else {
      constexpr int i = op >> 1, half = op & 1; constexpr unsigned so = slot_of(i) + 1024 * half;
      if constexpr (i < 10) dma16(ldsw + so, voffW, srdW, i * 64 * 8192 + 128 * half);
      else {
        constexpr bool isK = (i == 10) || (i != 25 && (i & 1)); constexpr int t = i == 10 ? 0 : (i == 25 ? 7 : (isK ? (i - 9) / 2 : (i - 12) / 2));
        dma16(ldsw + so, voffKV, isK ? srdK : srdV, t * 16384 + 128 * half);
      }
    }
  };
  static_for<SCH.npro>(LAMBDA(jc) { vmem_op(IC<SCH.pro[jc.value]>{}); });
  SBAR();
#pragma unroll
  for (int u = 0; u < 3; ++u) {
    const int id = blockIdx.x + 256 * u;
    const u16* src; int pitch, rows, kt, nt, kc, nsub; unsigned dbase;
    if (id < 320) { src = karg<const u16>(KA_WO); pitch = 1280; rows = 4096; kt = id / 5; nt = id - kt * 5; kc = 512; nsub = 40; dbase = 0; }
    else if (id < 455) { const int v = id - 320; src = karg<const u16>(KA_W2); pitch = 1280; rows = 1728; kt = v / 5; nt = v - kt * 5; kc = 216; nsub = 40; dbase = (unsigned)(WS_W2T - WS_WOT); }
    else { const int v = id < 595 ? id - 455 : 0; src = karg<const u16>(KA_W1); pitch = 3456; rows = id < 595 ? 640 : 0; kt = v / 14; nt = v - kt * 14; kc = 80; nsub = id < 595 ? 108 : 0; dbase = (unsigned)(WS_W1T - WS_WOT); }
    const int first = kt * 64 * pitch + nt * 256;
    rsrc_u[u] = __builtin_amdgcn_make_buffer_rsrc((void*)((const char*)src + first), 0, rows ? rows * pitch - first : 0, 0x00020000);
    pitch_u[u] = pitch;
    dst_u[u] = nt * 8 + wid < nsub ? dbase + ((nt * 8 + wid) * kc + kt * 8) * 256 : 0x7ff00000u;
  }
  auto vmem_gap = [&](auto gc, auto jc, auto nc) __attribute__((always_inline)) {
    constexpr int g = gc.value, j = jc.value, n = nc.value, no = SCH.nops[g];
    constexpr int i0 = (j * no + n - 1) / n, i1 = ((j + 1) * no + n - 1) / n;
    static_for<i1 - i0>(LAMBDA(uc) { vmem_op(IC<SCH.op[g][i0 + uc.value]>{}); });
  };

  const int blk = (lane >> 4) & 1, q4 = (lane & 15) >> 2, p4 = lane & 3;
  constexpr int NR = (NS * SLOT + 65535) / 65536;
  const lds_cptr L = (lds_cptr)lds;
  lds_cptr wb[NR][2], vb[NR][2], kb_[NR][2];
#pragma unroll
  for (int r = 0; r < NR; ++r)
#pragma unroll
    for (int t = 0; t < 2; ++t) {
      wb[r][t] = L + 65536 * r + 2048 * hi + 64 * (4 * t + q4) + 16 * ((2 * blk + (p4 & 1)) ^ (2 * hi + t)) + 8 * (p4 >> 1);
      PIN(wb[r][t]);
      vb[r][t] = L + 65536 * r + 64 * (4 * hi + q4) + 16 * ((2 * blk + (p4 & 1)) ^ (2 * t + hi)) + 8 * (p4 >> 1) + 2048 * t;
      kb_[r][t] = L + 65536 * r + 2048 * (r32 >> 3) + 64 * (r32 & 7) + 16 * ((2 * t + hi) ^ ((r32 >> 2) & 3));
    }
  constexpr int PD = 4, NG = 416;
  bf16x8 fr[PD + 1];
  auto ldfrag_g = [&](auto Gc) __attribute__((always_inline)) {
    constexpr int G = Gc.value;
    if constexpr (G < NG) {
      if constexpr (G < 160) {
        constexpr int i = G >> 4, ks = (G >> 2) & 3, db = G & 3, o = slot_of(i) + 4096 * ks + 512 * db;
        fr[G % (PD + 1)] = trfrag(wb[o >> 16][0] + (o & 65535), wb[o >> 16][1] + (o & 65535));
      } else {
        constexpr int u = G - 176, t = G < 176 ? 0 : (G >= 400 ? 8 : 1 + u / 32), g = G < 176 ? G - 160 : (G >= 400 ? G - 400 + 16 : u % 32);
        if constexpr (g < 16) {
          constexpr int kb = g >> 3, s = g & 7, o = slot_of(tile_K(t)) + 8192 * kb + 512 * (s >> 1);
          fr[G % (PD + 1)] = rowfrag(kb_[o >> 16][s & 1] + (o & 65535));
        } else {
          constexpr int gg = g - 16, ks = t == 8 ? (gg & 3) : (gg >> 2), db = t == 8 ? (gg >> 2) : (gg & 3), o = slot_of(tile_V(t - 1)) + 4096 * ks + 512 * db;
          fr[G % (PD + 1)] = trfrag(vb[o >> 16][0] + (o & 65535), vb[o >> 16][1] + (o & 65535));
        }
      }
    }
  };
  auto barrier = [&](auto gc) __attribute__((always_inline)) {
    constexpr int g = gc.value;
    if constexpr (g < 10) wait_bar4<SCH.wait[g]>(hf[g]); else wait_bar<SCH.wait[g]>();
    if constexpr (g == SCH.rt_done) { if (tid == 0) __hip_atomic_store((gu32*)sync + SY_RF + b, tag, RLX_AGENT); }
    SBAR();
  };

  f32x16 acc[4] = {};
  static_for<10>(LAMBDA(ic) {
    constexpr int i = ic.value;
    barrier(ic);
    if constexpr (i == 0) { static_for<PD>(LAMBDA(jc) { ldfrag_g(jc); }); SBAR(); }
    static_for<16>(LAMBDA(mc) {
      constexpr int m = mc.value, G = 16 * i + m;
      ldfrag_g(IC<G + PD>{});
      vmem_gap(ic, mc, IC<16>{});
      acc[m & 3] = mfma32(fr[G % (PD + 1)], hf[i][m >> 2], acc[m & 3]);
      SBAR();
    });
  });
  bf16x8 qf[8];
#pragma unroll
  for (int s = 0; s < 8; ++s) {
    u32x4 w;
#pragma unroll
    for (int e = 0; e < 4; ++e) w[e] = cvtpk(acc[s >> 1][8 * (s & 1) + 2 * e] * QSCALE, acc[s >> 1][8 * (s & 1) + 2 * e + 1] * QSCALE);
    qf[s] = __builtin_bit_cast(bf16x8, w);
  }
#pragma unroll
  for (int r = 0; r < NR; ++r)
#pragma unroll
    for (int t = 0; t < 2; ++t) { PIN(vb[r][t]); PIN(kb_[r][t]); }

  const int qrow = qt * 256 + wid * 32 + r32;
  const __amdgpu_buffer_rsrc_t srdO = __builtin_amdgcn_make_buffer_rsrc((void*)(ws + WS_O), 0, SQ * 4096 * 2, 0x00020000);
  const unsigned voffO = (((qrow >> 4) * 512 + head * 16 + hi) * 16 + (qrow & 15)) * 16;
  f32x16 o[4] = {}; f32x16 sA[2], sB[2], negm; u32x4 pw[4];
  float mref, lsum = 0.f; bool bad = false;
  barrier(IC<10>{});
  {
    const f32x16 zero = {};
    static_for<16>(LAMBDA(gc) {
      constexpr int g = gc.value, G = 160 + g;
      ldfrag_g(IC<G + PD>{});
      vmem_gap(IC<10>{}, gc, IC<16>{});
      sA[g >> 3] = mfma32(fr[G % (PD + 1)], qf[g & 7], (g & 7) == 0 ? zero : sA[g >> 3]);
      SBAR();
    });
    float a = max3(sA[0][0], sA[0][1], sA[1][0]), b = max3(sA[0][2], sA[0][3], sA[1][1]); a = max3(a, sA[1][2], sA[1][3]);
#pragma unroll
    for (int r = 4; r < 16; r += 4) { a = max3(a, sA[0][r], sA[0][r + 1]); b = max3(b, sA[0][r + 2], sA[0][r + 3]); a = max3(a, sA[1][r], sA[1][r + 1]); b = max3(b, sA[1][r + 2], sA[1][r + 3]); }
    mref = swap_max(__builtin_fmaxf(a, b)) + PBIAS;
#pragma unroll
    for (int r = 0; r < 16; ++r) negm[r] = -mref;
    PIN(negm);
    sA[0][0] = __builtin_amdgcn_exp2f(sA[0][0] - mref); sA[0][1] = __builtin_amdgcn_exp2f(sA[0][1] - mref);
    SBAR();
  }
  auto step = [&](auto tc, f32x16(&C)[2], f32x16(&P)[2]) __attribute__((always_inline)) {
    constexpr int t = tc.value;
    constexpr int G0 = 176 + 32 * (t - 1), GB = t < 8 ? G0 + 16 : 400;
    barrier(IC<10 + t>{});
    float sacc = 0.f;
    static_for<16>(LAMBDA(gc) {
      constexpr int g = gc.value;
      if constexpr (t < 8) {
        ldfrag_g(IC<G0 + g + PD>{});
        vmem_gap(IC<10 + t>{}, gc, IC<32>{});
        C[g >> 3] = mfma32(fr[(G0 + g) % (PD + 1)], qf[g & 7], (g & 7) == 0 ? negm : C[g >> 3]);
      }
      if constexpr (t == 1 && g < 15) {
        constexpr int e1 = 2 * (g + 1);
        P[e1 >> 4][e1 & 15] = __builtin_amdgcn_exp2f(P[e1 >> 4][e1 & 15] - mref); P[e1 >> 4][(e1 & 15) + 1] = __builtin_amdgcn_exp2f(P[e1 >> 4][(e1 & 15) + 1] - mref);
        PIN(P[e1 >> 4]);
      }
      constexpr int ks = g >> 2, w = g & 3, e = 8 * (ks & 1) + 2 * w;
      pw[ks][w] = cvtpk(P[ks >> 1][e], P[ks >> 1][e + 1]);
      sacc += P[ks >> 1][e]; sacc += P[ks >> 1][e + 1];
      PIN(pw[ks]); PIN(sacc);
      SBAR();
    });
    bad |= !(sacc <= PLIM);
    if constexpr (t == 8) {
      float om = 0.f;
#pragma unroll
      for (int d = 0; d < 4; ++d)
#pragma unroll
        for (int r = 0; r < 16; r += 2) om = max3(om, __builtin_fabsf(o[d][r]), __builtin_fabsf(o[d][r + 1]));
      bad |= om < OMIN;
    }
    lsum += sacc;
    if constexpr (t < 8) {
      static_for<16>(LAMBDA(gc) {
        constexpr int g = gc.value;
        ldfrag_g(IC<GB + g + PD>{});
        vmem_gap(IC<10 + t>{}, IC<16 + g>{}, IC<32>{});
        o[g & 3] = mfma32(fr[(GB + g) % (PD + 1)], __builtin_bit_cast(bf16x8, pw[g >> 2]), o[g & 3]);
        C[g >> 3][(2 * g) & 15] = __builtin_amdgcn_exp2f(C[g >> 3][(2 * g) & 15]); C[g >> 3][(2 * g + 1) & 15] = __builtin_amdgcn_exp2f(C[g >> 3][(2 * g + 1) & 15]);
        PIN(C[g >> 3]);
        SBAR();
      });
    }
  };
  auto out_db = [&](auto dc, float inv) __attribute__((always_inline)) {
    constexpr int db = dc.value;
#pragma unroll
    for (int s = 0; s < 2; ++s) {
      u32x4 w;
#pragma unroll
      for (int e = 0; e < 4; ++e) w[e] = cvtpk(o[db][8 * s + 2 * e] * inv, o[db][8 * s + 2 * e + 1] * inv);
      __builtin_amdgcn_raw_buffer_store_b128(w, srdO, voffO + (4 * db + 2 * s) * 256, 0, SC1);
    }
  };
  step(IC<1>{}, sB, sA);
  step(IC<2>{}, sA, sB);
  step(IC<3>{}, sB, sA);
  step(IC<4>{}, sA, sB);
  step(IC<5>{}, sB, sA);
  step(IC<6>{}, sA, sB);
  step(IC<7>{}, sB, sA);
  step(IC<8>{}, sA, sB);
  if (__builtin_expect(!__any(bad), 1)) {
    const float inv = 1.f / swap_add(lsum);
    static_for<16>(LAMBDA(gc) {
      constexpr int g = gc.value;
      ldfrag_g(IC<400 + g + PD>{});
      o[g >> 2] = mfma32(fr[(400 + g) % (PD + 1)], __builtin_bit_cast(bf16x8, pw[g & 3]), o[g >> 2]);
      if constexpr (g >= 5 && (g & 3) == 1) out_db(IC<(g >> 2) - 1>{}, inv);
      SBAR();
    });
    out_db(IC<3>{}, inv);
  } else {
    const unsigned reg = (unsigned)((wid == 0 ? 0 : wid + 1) * SLOT);
    static_assert(slot_of(tile_V(7)) == SLOT, "");
    const lds_cptr kbs[2] = {kb_[0][0] + reg, kb_[0][1] + reg}, vbs[2] = {vb[0][0] + reg + 8192, vb[0][1] + reg + 8192};
    float m = -__builtin_inff(), l = 0.f;
#pragma unroll
    for (int d = 0; d < 4; ++d) o[d] = f32x16{};
#pragma unroll 1
    for (int kb = 0; kb < WIN / 32; ++kb) {
#pragma unroll
      for (int R = 0; R < 4; ++R) {
        const unsigned vo = (unsigned)((8 * R + dr) * 256 + 64 * dc + 16 * (dx ^ ((2 * R + (dr >> 2)) & 3)));
        const unsigned so = __builtin_amdgcn_readfirstlane(kb * 8192), ld = __builtin_amdgcn_readfirstlane(lds0 + reg + 2048 * R);
        asm volatile("s_nop 4" ::: "memory");
        dma16(ld, vo, srdK, so); dma16(ld + 1024, vo + 128, srdK, so); dma16(ld + 8192, vo, srdV, so); dma16(ld + 9216, vo + 128, srdV, so);
      }
      asm volatile("s_waitcnt vmcnt(0)" ::: "memory");
      f32x16 sc = {};
#pragma unroll
      for (int s = 0; s < 8; ++s) { sc = mfma32(rowfrag(kbs[s & 1] + 512 * (s >> 1)), qf[s], sc); SBAR(); }
      float mx = sc[0];
#pragma unroll
      for (int r = 1; r < 16; ++r) mx = __builtin_fmaxf(mx, sc[r]);
      const float mn = __builtin_fmaxf(m, swap_max(mx)), al = __builtin_amdgcn_exp2f(m - mn);
      m = mn; l *= al;
#pragma unroll
      for (int r = 0; r < 16; ++r) { sc[r] = __builtin_amdgcn_exp2f(sc[r] - mn); l += sc[r]; }
#pragma unroll
      for (int d = 0; d < 4; ++d)
#pragma unroll
        for (int r = 0; r < 16; ++r) o[d][r] *= al;
#pragma unroll
      for (int ks = 0; ks < 2; ++ks) {
        const u32x4 pk = {cvtpk(sc[8 * ks], sc[8 * ks + 1]), cvtpk(sc[8 * ks + 2], sc[8 * ks + 3]), cvtpk(sc[8 * ks + 4], sc[8 * ks + 5]), cvtpk(sc[8 * ks + 6], sc[8 * ks + 7])};
#pragma unroll
        for (int d = 0; d < 4; ++d) { o[d] = mfma32(trfrag(vbs[0] + 4096 * ks + 512 * d, vbs[1] + 4096 * ks + 512 * d), __builtin_bit_cast(bf16x8, pk), o[d]); SBAR(); }
      }
      asm volatile("s_waitcnt lgkmcnt(0)" ::: "memory");
    }
    const float inv = 1.f / swap_add(l);
    static_for<4>(LAMBDA(dc_) { out_db(dc_, inv); });
  }
  const int rt = (xcd >> 2) * 8 + (idx >> 2), gi = (xcd & 3) * 4 + (idx & 3);
  {
    unsigned rf[4] = {tag, tag, tag, tag};
    if (wid == 0) {
#pragma unroll
      for (int k = 0; k < 4; ++k) rf[k] = __hip_atomic_load((gu32*)sync + SY_RF + lane + 64 * k, RLX_AGENT);
    }
    asm volatile("s_waitcnt vmcnt(0)" ::: "memory");
    if (wid == 0) {
      for (unsigned spins = 0; spins < SPIN_CAP; ++spins) {
        if (__all(rf[0] == tag && rf[1] == tag && rf[2] == tag && rf[3] == tag)) break;
#pragma unroll
        for (int k = 0; k < 4; ++k) rf[k] = __hip_atomic_load((gu32*)sync + SY_RF + lane + 64 * k, RLX_AGENT);
      }
    }
    asm volatile("" ::: "memory");
    __syncthreads();
    if (tid == 0) __hip_atomic_store((gu32*)sync + SY_OF + (qt * 32 + head) * FS, tag, RLX_AGENT);
  }
  unsigned* const c1 = sync + SY_CF + rt * 16 * FS, * const c2 = c1 + 256 * FS, * const c3 = c1 + 512 * FS;
  const u16* const mod = (const u16*)(ws + WS_MOD);
  const __amdgpu_buffer_rsrc_t rmod = __builtin_amdgcn_make_buffer_rsrc((void*)mod, 0, 3840 * 2, 0x00020000);
  u16 *const obuf = (u16*)(ws + WS_O), *const wot = (u16*)(ws + WS_WOT), *const w1t = (u16*)(ws + WS_W1T), *const w2t = (u16*)(ws + WS_W2T);
  u16 *const x1 = (u16*)(ws + WS_X1), *const hid = (u16*)(ws + WS_HID), *const h2 = (u16*)(ws + WS_H2), *const part = (u16*)(ws + WS_P);
  const int nft = gi < 15 ? 7 : 3;
  const __amdgpu_buffer_rsrc_t raD = __builtin_amdgcn_make_buffer_rsrc((void*)(w1t + (size_t)(7 * gi) * 80 * 128), 0, nft * 80 * 256, 0x00020000);
  RingA<4, 7, 20, 80, 5> RD; RD.init(lds0, wid, lane);
  {
    const int kq = gi >> 2, j = gi & 3, qb = wid & 3;
    const __amdgpu_buffer_rsrc_t ra = __builtin_amdgcn_make_buffer_rsrc((void*)(wot + ((size_t)(10 * j) * 512 + kq * 128) * 128), 0, 10 * 512 * 256, 0x00020000);
    const __amdgpu_buffer_rsrc_t rb = __builtin_amdgcn_make_buffer_rsrc((void*)(obuf + ((size_t)(8 * rt + 2 * qb) * 512 + kq * 128) * 128), 0, 2 * 512 * 256, 0x00020000);
    const __amdgpu_buffer_rsrc_t rp = __builtin_amdgcn_make_buffer_rsrc((void*)part, 0, 4 * SQ * DM * 2, 0x00020000);
    f32x4 acc[10][2] = {};
    RingA<4, 10, 32, 512, 4> RA; RA.init(lds0, wid, lane);
    RA.pre<0, 1>(ra);
    await_flags<8>(sync + SY_OF + ((rt >> 1) * 32 + kq * 8) * FS, tag);
    ring_gemm<4, 10, 32, 512, 512, 1, 4, 8>(acc, RA, ra, rb, lds, wid, lane, [&](auto ic) __attribute__((always_inline)) { RD.template issue<ic.value / 4, ic.value % 4>(raD); });
    pair_reduce<10>(acc, lds + 81920, wid, lane);
    const int nt0 = wid < 4 ? 0 : 5;
    const unsigned vo = ((((8 * rt + 2 * qb + ((lane >> 4) & 1)) * 80 + j * 20 + nt0 * 2 + (lane >> 5)) * 4 + kq) * 16 + (lane & 15)) * 16;
    auto outp = [&](auto ntc) __attribute__((always_inline)) {
      constexpr int nt = ntc.value, o = nt % 5;
      const u32x2 w0 = {cvtpk(acc[nt][0][0], acc[nt][0][1]), cvtpk(acc[nt][0][2], acc[nt][0][3])}, w1 = {cvtpk(acc[nt][1][0], acc[nt][1][1]), cvtpk(acc[nt][1][2], acc[nt][1][3])};
      st16(rp, vo + o * 2 * 1024, pair16(w0, w1));
    };
    if (wid < 4) { asm volatile("; lo-out" ::: "memory"); static_for<5>(LAMBDA(c) { outp(c); }); asm volatile("; lo-end" ::: "memory"); }
    else { asm volatile("; hi-out" ::: "memory"); static_for<5>(LAMBDA(c) { outp(IC<5 + c.value>{}); }); asm volatile("; hi-end" ::: "memory"); }
  }
  publish(c1 + gi * FS, tag);
  {
    float* red = (float*)(lds + 98304);
    const int r = tid & 7, row = rt * 128 + gi * 8 + r;
    const bool two = wid < 2;
    const __amdgpu_buffer_rsrc_t rp = __builtin_amdgcn_make_buffer_rsrc((void*)part, 0, 4 * SQ * DM * 2, 0x00020000);
    const __amdgpu_buffer_rsrc_t rx1 = __builtin_amdgcn_make_buffer_rsrc((void*)x1, 0, SQ * DM * 2, 0x00020000);
    const __amdgpu_buffer_rsrc_t rh2 = __builtin_amdgcn_make_buffer_rsrc((void*)h2, 0, SQ * DM * 2, 0x00020000);
    const u16* x = karg<const u16>(KA_X);
    u32x4 p[2][4], xv[2], gv[2], sbv[2], cbv[2]; unsigned blk[2], pblk[2];
#pragma unroll
    for (int u = 0; u < 2; ++u) {
      const int c = (u == 0 || two) ? (tid >> 3) + 64 * u : (tid >> 3);
      blk[u] = (((row >> 4) * 80 + c) * 16 + (row & 15)) * 16; pblk[u] = (((row >> 4) * 80 + c) * 64 + (row & 15)) * 16;
      xv[u] = *(const u32x4*)(x + (size_t)row * DM + c * 8);
      gv[u] = ld16c(rmod, (2 * 640 + c * 8) * 2); sbv[u] = ld16c(rmod, (3 * 640 + c * 8) * 2); cbv[u] = ld16c(rmod, (4 * 640 + c * 8) * 2);
    }
    RD.pre<2, 3>(raD);
    if (b == 119 && wid == 7) {
      bool ok = false;
      for (unsigned spins = 0; spins < SPIN_CAP && !ok; ++spins) {
        bool all = true;
#pragma unroll
        for (int k = 0; k < 4; ++k) all &= __hip_atomic_load((gu32*)sync + SY_STARTED + lane + 64 * k, RLX_AGENT) == tag;
        ok = __all(all);
      }
      if (lane == 0) __hip_atomic_store((gu32*)sync, tag, RLX_AGENT);
    }
    await_flags<16>(c1, tag);
#pragma unroll
    for (int k = 0; k < 4; ++k) p[0][k] = ld16c(rp, pblk[0] + k * 256);
    if (two) {
#pragma unroll
      for (int k = 0; k < 4; ++k) p[1][k] = ld16c(rp, pblk[1] + k * 256);
    }
    float v[2][8]; float s = 0; u32x4 x1v[2];
    auto resid = [&](auto uc) __attribute__((always_inline)) {
      constexpr int u = uc.value;
#pragma unroll
      for (int e = 0; e < 4; ++e) {
        const float a = (bf_lo(p[u][0][e]) + bf_lo(p[u][1][e])) + (bf_lo(p[u][2][e]) + bf_lo(p[u][3][e])), b = (bf_hi(p[u][0][e]) + bf_hi(p[u][1][e])) + (bf_hi(p[u][2][e]) + bf_hi(p[u][3][e]));
        x1v[u][e] = cvtpk(__builtin_fmaf(bf_lo(gv[u][e]), a, bf_lo(xv[u][e])), __builtin_fmaf(bf_hi(gv[u][e]), b, bf_hi(xv[u][e])));
        v[u][2 * e] = bf_lo(x1v[u][e]); v[u][2 * e + 1] = bf_hi(x1v[u][e]); s += v[u][2 * e] + v[u][2 * e + 1];
      }
    };
    resid(IC<0>{});
    if (two) resid(IC<1>{});
    s = xor_add<32>(xor_add<16>(xor_add<8>(s)));
    if (lane < 8) red[wid * 8 + r] = s;
    lds_barrier();
    float tot = 0;
#pragma unroll
    for (int i = 0; i < 8; ++i) tot += red[i * 8 + r];
    const float mean = tot * (1.f / DM); float q = 0;
#pragma unroll
    for (int e = 0; e < 8; ++e) { v[0][e] -= mean; q += v[0][e] * v[0][e]; }
    if (two) {
#pragma unroll
      for (int e = 0; e < 8; ++e) { v[1][e] -= mean; q += v[1][e] * v[1][e]; }
    }
    q = xor_add<32>(xor_add<16>(xor_add<8>(q)));
    if (lane < 8) red[64 + wid * 8 + r] = q;
    lds_barrier();
    float qt = 0;
#pragma unroll
    for (int i = 0; i < 8; ++i) qt += red[64 + i * 8 + r];
    const float rstd = __builtin_amdgcn_rsqf(qt * (1.f / DM) + 1e-5f);
    auto norm = [&](auto uc) __attribute__((always_inline)) {
      constexpr int u = uc.value;
      u32x4 hv;
#pragma unroll
      for (int e = 0; e < 4; ++e)
        hv[e] = cvtpk(__builtin_fmaf(v[u][2 * e] * rstd, bf_lo(cbv[u][e]), bf_lo(sbv[u][e])), __builtin_fmaf(v[u][2 * e + 1] * rstd, bf_hi(cbv[u][e]), bf_hi(sbv[u][e])));
      st16(rh2, blk[u], hv); st16(rx1, blk[u], x1v[u]);
    };
    norm(IC<0>{});
    if (two) norm(IC<1>{});
  }
  publish(c2 + gi * FS, tag);
  const int jE = gi & 7, rtE = rt * 2 + (gi >> 3);
  const __amdgpu_buffer_rsrc_t raE = __builtin_amdgcn_make_buffer_rsrc((void*)(w2t + (size_t)(5 * jE) * 216 * 128), 0, 5 * 216 * 256, 0x00020000);
  RingA<2, 5, 54, 216, 4> RE; RE.init(lds0, wid, lane);
  {
    const int qb = wid & 3, nt0 = wid < 4 ? 0 : 4;
    const __amdgpu_buffer_rsrc_t rb = __builtin_amdgcn_make_buffer_rsrc((void*)(h2 + (size_t)(8 * rt + 2 * qb) * 80 * 128), 0, 2 * 80 * 256, 0x00020000);
    const __amdgpu_buffer_rsrc_t rh = __builtin_amdgcn_make_buffer_rsrc((void*)hid, 0, SQ * FF * 2, 0x00020000);
    const u16* b1 = karg<const u16>(KA_B1);
    u32x2 bv[4];
#pragma unroll
    for (int o = 0; o < 4; ++o) bv[o] = *(const u32x2*)(b1 + (nt0 + o < nft ? (7 * gi + nt0 + o) * 16 + 4 * (lane >> 4) : 0));
    f32x4 acc[7][2] = {};
    await_flags<16>(c2, tag);
    ring_gemm<4, 7, 20, 80, 80, 3, 5, 10>(acc, RD, raD, rb, lds, wid, lane, [&](auto ic) __attribute__((always_inline)) { RE.template issue<ic.value / 5, ic.value % 5>(raE); });
    pair_reduce<7>(acc, lds + 98304, wid, lane);
    const unsigned vo = (((8 * rt + 2 * qb + ((lane >> 4) & 1)) * 216 + (7 * gi + nt0) * 2 + (lane >> 5)) * 16 + (lane & 15)) * 16;
    auto outp = [&](auto ntc, auto oc) __attribute__((always_inline)) {
      constexpr int nt = ntc.value, o = oc.value;
      if (nt < nft) {
        const float b0 = bf_lo(bv[o][0]), b1_ = bf_hi(bv[o][0]), b2_ = bf_lo(bv[o][1]), b3 = bf_hi(bv[o][1]);
        u32x2 w[2];
#pragma unroll
        for (int q = 0; q < 2; ++q) {
          const float v0 = rbf(__builtin_fmaxf(rbf(acc[nt][q][0]) + b0, 0.f)), v1 = rbf(__builtin_fmaxf(rbf(acc[nt][q][1]) + b1_, 0.f));
          const float v2 = rbf(__builtin_fmaxf(rbf(acc[nt][q][2]) + b2_, 0.f)), v3 = rbf(__builtin_fmaxf(rbf(acc[nt][q][3]) + b3, 0.f));
          w[q] = u32x2{cvtpk(v0 * v0, v1 * v1), cvtpk(v2 * v2, v3 * v3)};
        }
        st16(rh, vo + o * 2 * 256, pair16(w[0], w[1]));
      }
    };
    if (wid < 4) { asm volatile("; lo-out" ::: "memory"); static_for<4>(LAMBDA(c) { outp(c, c); }); asm volatile("; lo-end" ::: "memory"); }
    else { asm volatile("; hi-out" ::: "memory"); static_for<3>(LAMBDA(c) { outp(IC<4 + c.value>{}, c); }); asm volatile("; hi-end" ::: "memory"); }
  }
  publish(c3 + gi * FS, tag);
  {
    const int qb = wid & 1;
    const __amdgpu_buffer_rsrc_t rb = __builtin_amdgcn_make_buffer_rsrc((void*)(hid + (size_t)(4 * rtE + 2 * qb) * 216 * 128), 0, 2 * 216 * 256, 0x00020000);
    const __amdgpu_buffer_rsrc_t rx1 = __builtin_amdgcn_make_buffer_rsrc((void*)x1, 0, SQ * DM * 2, 0x00020000);
    const u16* b2 = karg<const u16>(KA_B2);
    u16* out = karg<u16>(KA_OUT);
    u32x4 xv[2], gv[2], bv[2];
#pragma unroll
    for (int it = 0; it < 2; ++it) {
      const int item = tid + it * 512, q = item & 63, c = item < 640 ? item >> 6 : 0, row = rtE * 64 + q;
      xv[it] = ld16c(rx1, (((row >> 4) * 80 + jE * 10 + c) * 16 + (row & 15)) * 16);
      gv[it] = ld16c(rmod, (5 * 640 + jE * 80 + c * 8) * 2); bv[it] = *(const u32x4*)(b2 + jE * 80 + c * 8);
    }
    f32x4 acc[5][2] = {};
    RE.pre<2, 3>(raE);
    await_flags<16>(c3, tag);
    ring_gemm<2, 5, 54, 216, 216, 3, 4, 0>(acc, RE, raE, rb, lds, wid, lane, [&](auto) {});
    f32x4* red = (f32x4*)lds;
    constexpr int HI = (3 * 40960 - 4 * 10240) / 16;
    {
      f32x4* mine = red + (wid * 10) * 64 + (wid >= 4 ? HI : 0) + lane;
#pragma unroll
      for (int n = 0; n < 5; ++n) { mine[(n * 2) * 64] = acc[n][0]; mine[(n * 2 + 1) * 64] = acc[n][1]; }
    }
    lds_barrier();
#pragma unroll
    for (int it = 0; it < 2; ++it) {
      const int item = tid + it * 512;
      if (item < 640) {
        const int q = item & 63, c = item >> 6, n = c >> 1, l0 = (q & 15) + 32 * (c & 1), base = (((q >> 5) * 5 + n) * 2 + ((q >> 4) & 1)) * 64 + l0;
        f32x4 lo = {0, 0, 0, 0}, hi = {0, 0, 0, 0};
#pragma unroll
        for (int w = 0; w < 4; ++w) { const int o_ = base + w * 2 * 5 * 2 * 64 + (w >= 2 ? HI : 0); lo += red[o_]; hi += red[o_ + 16]; }
        const float v[8] = {lo[0], lo[1], lo[2], lo[3], hi[0], hi[1], hi[2], hi[3]};
        u32x4 o;
#pragma unroll
        for (int e = 0; e < 4; ++e) {
          const float a = bf_lo(xv[it][e]) + rbf(bf_lo(gv[it][e]) * rbf(rbf(v[2 * e]) + bf_lo(bv[it][e]))), b = bf_hi(xv[it][e]) + rbf(bf_hi(gv[it][e]) * rbf(rbf(v[2 * e + 1]) + bf_hi(bv[it][e])));
          o[e] = cvtpk(a, b);
        }
        *(u32x4*)(out + (size_t)(rtE * 64 + q) * DM + jE * 80 + c * 8) = o;
      }
    }
  }
}

extern "C" void kernel_launch(void* const* d_in, const int* in_sizes, int n_in, void* d_out, int out_size, void* d_ws, size_t ws_size, hipStream_t stream) {
  char* ws = (char*)d_ws;
  (void)hipFuncSetAttribute((const void*)k_block, hipFuncAttributeMaxDynamicSharedMemorySize, 163840);
  k_block<<<256, 512, 163840, stream>>>((const u16*)d_in[0], (const u16*)d_in[1], (const u16*)d_in[2], (const u16*)d_in[3], (const u16*)d_in[4], (const u16*)d_in[6], (const u16*)d_in[7],
                                        (const u16*)d_in[8], (const u16*)d_in[9], (const u16*)d_in[10], (const u16*)d_in[11], (u16*)d_out, ws);
}
```
